```python
import torch

def _softmax(x, axis=-1):
    return torch.softmax(x, dim=axis)
def _gelu(x):
    return torch.nn.functional.gelu(x, approximate="tanh")
def _silu(x):
    return torch.nn.functional.silu(x)
def _sigmoid(x):
    return torch.sigmoid(x)
def _layernorm(x, axis=-1, eps=1e-5):
    m = x.mean(dim=axis, keepdim=True)
    v = x.var(dim=axis, keepdim=True, unbiased=False)
    return (x - m) / torch.sqrt(v + eps)
def _scatter_add(tgt, idx, src, axis=0):
    return tgt.clone().index_add_(axis, idx, src)
def _segment_sum(src, seg, n):
    out = torch.zeros((n,) + src.shape[1:], dtype=src.dtype)
    return out.index_add_(0, seg, src)
def _pad(x, pw):
    flat = tuple(v for pair in reversed(pw) for v in pair)
    return torch.nn.functional.pad(x, flat)

kernel_name = 'concat_heads__linear_kernel_hedgehog__ssm_conv_h3__prompt'

def setup_inputs():

    _w = (lambda a: (lambda t: t.to(torch.bfloat16) if t.is_floating_point() else t)(torch.as_tensor(a)))
    def _A_setup():
        B, S, D, H = 1, 2048, 640, 32
        Dh = D // H
        return {
            "x": _w((lambda *s: torch.randn(*s, dtype=torch.bfloat16) * 0.4)(B, S, D)),
            "wqkv": _w((lambda *s: torch.randn(*s, dtype=torch.bfloat16) * 0.4)(D, 3 * D)),
            "w_phi": _w((lambda *s: torch.randn(*s, dtype=torch.bfloat16) * 0.4)(H, Dh, Dh) * (Dh ** -0.5)),
            "wo": _w((lambda *s: torch.randn(*s, dtype=torch.bfloat16) * 0.05)(D, D)),
        }
    _A_in = _A_setup()

    def _B_setup():
        B, S, D = 1, 2048, 640
        ks = 4
        return {
            "q": _w((lambda *s: torch.randn(*s, dtype=torch.bfloat16) * 0.05)(B, S, D)),
            "k": _w((lambda *s: torch.randn(*s, dtype=torch.bfloat16) * 0.8)(B, S, D)),
            "v": _w((lambda *s: torch.randn(*s, dtype=torch.bfloat16) * 0.8)(B, S, D)),
            "shift_k": _w((lambda *s: torch.randn(*s, dtype=torch.bfloat16) * 0.8)(D, ks) * 0.5),
            "ssm_k": _w((lambda *s: torch.randn(*s, dtype=torch.bfloat16) * 0.8)(D, S) * (1.0 / S ** 0.5)),
            "ssm_D": _w((lambda *s: torch.randn(*s, dtype=torch.bfloat16) * 0.8)(D)),
        }
    _B_in = _B_setup()

    return {
        "A_x": _A_in["x"],
        "A_wqkv": _A_in["wqkv"],
        "A_w_phi": _A_in["w_phi"],
        "A_wo": _A_in["wo"],
        "B_q": _B_in["q"],
        "B_k": _B_in["k"],
        "B_v": _B_in["v"],
        "B_shift_k": _B_in["shift_k"],
        "B_ssm_k": _B_in["ssm_k"],
        "B_ssm_D": _B_in["ssm_D"],
    }

def reference(A_x, A_wqkv, A_w_phi, A_wo, B_q, B_k, B_v, B_shift_k, B_ssm_k, B_ssm_D):
    def _A_ref(x, wqkv, w_phi, wo):
        B, S, D = x.shape
        H = 32; Dh = D // H
        qkv = (lambda x, *d: x.permute(*d))((x @ wqkv).reshape(B, S, 3, H, Dh), 2, 0, 3, 1, 4)
        q, k, v = qkv[0], qkv[1], qkv[2]
        pq = torch.einsum("bhsd,hdf->bhsf", q, w_phi)
        pk = torch.einsum("bhsd,hdf->bhsf", k, w_phi)
        phi_q = torch.concatenate([_softmax(pq, axis=-1), _softmax(-pq, axis=-1)], axis=-1)
        phi_k = torch.concatenate([_softmax(pk, axis=-1), _softmax(-pk, axis=-1)], axis=-1)
        kv = torch.einsum("bhsf,bhsd->bhfd", phi_k, v)
        z  = torch.einsum("bhsf->bhf", phi_k)
        num = torch.einsum("bhqf,bhfd->bhqd", phi_q, kv)
        den = torch.einsum("bhqf,bhf->bhq", phi_q, z)[..., None] + 1e-6
        o = num / den
        return (lambda x, *d: x.permute(*d))(o, 0, 2, 1, 3).reshape(B, S, D) @ wo

    def _B_ref(q, k, v, shift_k, ssm_k, ssm_D):
        B, S, D = q.shape
        ks = shift_k.shape[-1]
        k_pad = _pad(k, ((0, 0), (ks - 1, 0), (0, 0)))
        k_shift = torch.zeros_like(k)
        for t in range(ks):
            k_shift = k_shift + k_pad[:, t:t + S, :] * shift_k[:, ks - 1 - t][None, None, :]
        kv = k_shift * v
        L = 2 * S
        kv_pad = _pad(kv, ((0, 0), (0, S), (0, 0)))
        kf_pad = _pad(ssm_k, ((0, 0), (0, S)))
        kv_f = (lambda x, axes=(-2,-1): torch.fft.rfft2(x.float(), dim=axes))(kv_pad[:, None, :, :], axes=(1, 2))
        kf_f = (lambda x, axes=(-2,-1): torch.fft.rfft2(x.float(), dim=axes))(kf_pad[:, None, :], axes=(1, 2))
        kf_f = (lambda x, *d: x.permute(*d))(kf_f, 1, 2, 0)
        y_f = kv_f * kf_f[None, :, :, :]
        y = (lambda x, s, axes=(-2,-1): torch.fft.irfft2(x, s=s, dim=axes).to(torch.bfloat16))(y_f, (1, L), axes=(1, 2))[:, 0, :S, :]
        y = y + kv * ssm_D[None, None, :]
        return q * y

    a = _A_ref(A_x, A_wqkv, A_w_phi, A_wo)
    b = _B_ref(B_q, B_k, B_v, B_shift_k, B_ssm_k, B_ssm_D)
    return torch.concatenate([a, b], axis=-1)
```

```cpp
#include <hip/hip_runtime.h>
#include <hip/hip_bf16.h>
typedef unsigned short u16;
typedef short bf16x8 __attribute__((ext_vector_type(8)));
typedef float f32x4 __attribute__((ext_vector_type(4)));
__device__ __forceinline__ float b2f(u16 x) { return __uint_as_float((unsigned)x << 16); }
__device__ __forceinline__ u16 f2b(float f) { unsigned u = __float_as_uint(f); u += 0x7fff + ((u >> 16) & 1); return (u16)(u >> 16); }
constexpr int S = 2048, D = 640, H = 32, DH = 20;
__global__ void k_gemm(const u16* A, const u16* B, u16* C, int N, int K, int ldc) {
    int lane = threadIdx.x & 63, w = threadIdx.x >> 6;
    int tn = blockIdx.x * 4 + w, tm = blockIdx.y;
    int r = lane & 15, g = lane >> 4;
    f32x4 acc = {0, 0, 0, 0};
    for (int k = 0; k < K; k += 32) {
        bf16x8 a = *(const bf16x8*)(A + (tm * 16 + r) * K + k + 8 * g);
        bf16x8 b;
        for (int j = 0; j < 8; ++j) b[j] = B[(k + 8 * g + j) * N + tn * 16 + r];
        acc = __builtin_amdgcn_mfma_f32_16x16x32_bf16(a, b, acc, 0, 0, 0);
    }
    for (int i = 0; i < 4; ++i) C[(tm * 16 + 4 * g + i) * ldc + tn * 16 + r] = f2b(acc[i]);
}
__global__ __launch_bounds__(256) void k_phi(const u16* qkv, const u16* wphi, float* phi) {
    int idx = blockIdx.x * blockDim.x + threadIdx.x;
    int s = idx & 2047, h = (idx >> 11) & 31, which = idx >> 16;
    float x[DH], p[DH];
#pragma unroll
    for (int d = 0; d < DH; ++d) x[d] = b2f(qkv[s * 1920 + which * 640 + h * 20 + d]);
    float mx = -1e30f, mn = 1e30f;
#pragma unroll
    for (int f = 0; f < DH; ++f) { float a = 0;
#pragma unroll
        for (int d = 0; d < DH; ++d) a += x[d] * b2f(wphi[h * 400 + d * 20 + f]);
        p[f] = a; mx = fmaxf(mx, a); mn = fminf(mn, a); }
    float s1 = 0, s2 = 0;
#pragma unroll
    for (int f = 0; f < DH; ++f) { s1 += __expf(p[f] - mx); s2 += __expf(mn - p[f]); }
#pragma unroll
    for (int f = 0; f < DH; ++f) {
        phi[((which * 32 + h) * 40 + f) * 2048 + s] = __expf(p[f] - mx) / s1;
        phi[((which * 32 + h) * 40 + 20 + f) * 2048 + s] = __expf(mn - p[f]) / s2;
    }
}
__global__ void k_kv(const u16* qkv, const float* phi, float* kv) {
    int h = blockIdx.x / 40, f = blockIdx.x % 40, t = threadIdx.x;
    float a[21];
#pragma unroll
    for (int d = 0; d < 21; ++d) a[d] = 0;
    for (int s = t; s < S; s += 256) { float p = phi[((32 + h) * 40 + f) * 2048 + s];
#pragma unroll
        for (int d = 0; d < 20; ++d) a[d] += p * b2f(qkv[s * 1920 + 1280 + h * 20 + d]);
        a[20] += p; }
    __shared__ float red[21 * 4];
#pragma unroll
    for (int d = 0; d < 21; ++d) { float v = a[d]; for (int o = 32; o; o >>= 1) v += __shfl_xor(v, o); if ((t & 63) == 0) red[d * 4 + (t >> 6)] = v; }
    __syncthreads();
    if (t < 21) kv[(h * 40 + f) * 21 + t] = red[t * 4] + red[t * 4 + 1] + red[t * 4 + 2] + red[t * 4 + 3];
}
__global__ void k_o(const float* phi, const float* kv, u16* o) {
    int idx = blockIdx.x * blockDim.x + threadIdx.x;
    int s = idx & 2047, h = idx >> 11;
    float a[21];
#pragma unroll
    for (int d = 0; d < 21; ++d) a[d] = 0;
    for (int f = 0; f < 40; ++f) { float p = phi[(h * 40 + f) * 2048 + s];
#pragma unroll
        for (int d = 0; d < 21; ++d) a[d] += p * kv[(h * 40 + f) * 21 + d]; }
    float inv = 1.0f / (a[20] + 1e-6f);
#pragma unroll
    for (int d = 0; d < 20; ++d) o[s * 640 + h * 20 + d] = f2b(a[d] * inv);
}
__global__ void k_u(const u16* k, const u16* v, const u16* sk, float* uT) {
    int idx = blockIdx.x * blockDim.x + threadIdx.x;
    int c = idx % 640, t = idx / 640;
    float a = 0;
    for (int j = 0; j < 4; ++j) if (t - j >= 0) a += b2f(k[(t - j) * 640 + c]) * b2f(sk[c * 4 + j]);
    uT[c * 2048 + t] = b2f(f2b(b2f(f2b(a)) * b2f(v[t * 640 + c])));
}
__global__ void k_conv(const float* uT, const u16* h, const u16* Dv, const u16* q, u16* out) {
    __shared__ float su[2048], sh[2048];
    int c = blockIdx.x;
    for (int i = threadIdx.x; i < 2048; i += 1024) { su[i] = uT[c * 2048 + i]; sh[i] = b2f(h[c * 2048 + i]); }
    __syncthreads();
    for (int rep = 0; rep < 2; ++rep) {
        int t = rep ? 2047 - threadIdx.x : threadIdx.x;
        float a = 0;
        for (int j = 0; j <= t; ++j) a += sh[j] * su[t - j];
        float y = b2f(f2b(a)) + su[t] * b2f(Dv[c]);
        out[t * 1280 + 640 + c] = f2b(b2f(q[t * 640 + c]) * y);
    }
}
extern "C" void kernel_launch(void* const* d_in, const int* in_sizes, int n_in,
                              void* d_out, int out_size, void* d_ws, size_t ws_size,
                              hipStream_t stream) {
    const u16 *x = (const u16*)d_in[0], *wqkv = (const u16*)d_in[1], *wphi = (const u16*)d_in[2], *wo = (const u16*)d_in[3];
    const u16 *q = (const u16*)d_in[4], *k = (const u16*)d_in[5], *v = (const u16*)d_in[6], *sk = (const u16*)d_in[7], *h = (const u16*)d_in[8], *Dv = (const u16*)d_in[9];
    u16* out = (u16*)d_out;
    char* ws = (char*)d_ws;
    u16* qkv = (u16*)ws; float* phi = (float*)(ws + (16 << 20)); float* kv = (float*)(ws + (48 << 20)); u16* o = (u16*)(ws + (49 << 20)); float* uT = (float*)(ws + (56 << 20));
    k_gemm<<<dim3(1920 / 64, 128), 256, 0, stream>>>(x, wqkv, qkv, 1920, 640, 1920);
    k_phi<<<2 * 32 * 2048 / 256, 256, 0, stream>>>(qkv, wphi, phi);
    k_kv<<<32 * 40, 256, 0, stream>>>(qkv, phi, kv);
    k_o<<<32 * 2048 / 256, 256, 0, stream>>>(phi, kv, o);
    k_gemm<<<dim3(640 / 64, 128), 256, 0, stream>>>(o, wo, out, 640, 640, 1280);
    k_u<<<2048 * 640 / 256, 256, 0, stream>>>(k, v, sk, uT);
    k_conv<<<640, 1024, 0, stream>>>(uT, h, Dv, q, out);
}
```

```cpp
#include <hip/hip_runtime.h>

__global__ __launch_bounds__(512, 2) void fused_kernel(const void* x, const void* wqkv, const void* wphi, const void* wo, const void* q, const void* k,
                                                       const void* v, const void* sk, const void* h, const void* d, void* out, void* ws) {
    __shared__ __attribute__((aligned(16))) char smem[163840];
    asm volatile(
        "s_load_dwordx16 s[4:19], s[0:1], 0\n"
        "s_load_dwordx8 s[20:27], s[0:1], 64\n"
        "v_readfirstlane_b32 s28, v0\n"
        "s_and_b32 s29, s2, 7\n"
        "s_lshr_b32 s30, s2, 3\n"
        "s_mov_b32 s31, 0xffff0000\n"
        "s_lshr_b32 s28, s28, 6\n"
        "s_mov_b32 s68, 0x800000\n"
        "s_cmp_eq_u32 s28, 7\n"
        "s_cbranch_scc0 .Lnopf\n"
        "s_getpc_b64 s[60:61]\n"
        "s_and_b32 s3, s30, 7\n"
        "s_lshl_b32 s3, s3, 13\n"
        "v_and_b32 v1, 63, v0\n"
        "v_lshl_add_u32 v1, v1, 7, s3\n"
        "v_min_u32 v1, .Lend-.Lnopf, v1\n"
        "global_load_dword v255, v1, s[60:61]\n"
        ".Lnopf:\n"
        "s_cmp_lt_u32 s30, 16\n"
        "s_cbranch_scc1 .LisA\n"
        "s_cmp_lt_u32 s28, 8\n"
        "s_cbranch_scc0 .Lh0done\n"
        "v_and_b32 v252, 63, v0\n"
        "s_lshl_b32 s67, s28, 4\n"
        "s_add_u32 s67, s67, s30\n"
        "s_sub_u32 s67, s67, 16\n"
        "s_mov_b32 s3, s28\n"
        "s_cmp_eq_u32 s3, 0\n"
        "s_cbranch_scc1 .Lh0go\n"
        "s_waitcnt lgkmcnt(0)\n"
        ".Lh0z:\n"
        "s_sleep 5\n"
        "s_sub_u32 s3, s3, 1\n"
        "s_cmp_eq_u32 s3, 0\n"
        "s_cbranch_scc0 .Lh0z\n"
        ".Lh0go:\n"
        "s_mul_i32 s62, s67, 79\n"
        "s_lshr_b32 s62, s62, 10\n"
        "s_mul_i32 s3, s62, 13\n"
        "s_sub_u32 s63, s67, s3\n"
        "s_lshr_b32 s3, s29, 1\n"
        "s_lshl_b32 s3, s3, 3\n"
        "s_add_u32 s3, s3, s63\n"
        "s_mul_i32 s3, s3, 640\n"
        "s_add_u32 s66, s3, s62\n"
        "s_and_b32 s3, s29, 1\n"
        "s_mul_i32 s3, s3, 5\n"
        "s_add_u32 s3, s3, s63\n"
        "s_mul_i32 s62, s62, 1920\n"
        "s_add_u32 s3, s3, s62\n"
        "s_add_u32 s3, s3, 2\n"
        "s_cmp_lt_u32 s63, 8\n"
        "s_cselect_b32 s62, s66, s3\n"
        "s_movk_i32 s3, 3840\n"
        "s_cselect_b32 s3, 1280, s3\n"
        "s_mov_b32 s46, 2457600\n"
        "s_cselect_b32 s46, 2621440, s46\n"
        "v_mul_u32_u24 v253, s3, v252\n"
        "s_mov_b32 s47, 0x20000\n"
        "s_waitcnt lgkmcnt(0)\n"
        "s_cselect_b64 s[44:45], s[4:5], s[6:7]\n"
        "s_lshl_b32 s62, s62, 7\n"
        "s_and_b32 s45, s45, 0xffff\n"
        "buffer_load_dword v254, v253, s[44:47], s62 offen\n"
        ".Lh0done:\n"
        "s_branch .LnotA\n"
        ".LisA:\n"
        "s_cmp_lt_u32 s28, 4\n"
        "s_cbranch_scc0 .LroleG\n"
        "v_and_b32 v0, 63, v0\n"
        "s_mov_b32 s35, s28\n"
        "v_lshrrev_b32 v20, 3, v0\n"
        "v_lshrrev_b32 v21, 4, v0\n"
        "v_and_b32 v22, 7, v0\n"
        "v_xor_b32 v22, v21, v22\n"
        "v_lshlrev_b32 v22, 4, v22\n"
        "s_movk_i32 s3, 1280\n"
        "v_mad_u32_u24 v244, v20, s3, v22\n"
        "v_xor_b32_e64 v245, 64, v244\n"
        "v_add_u32 v245, 9216, v245\n"
        "v_add_u32 v246, 18432, v244\n"
        "v_add_u32 v247, 18432, v245\n"
        "s_lshr_b32 s3, s29, 1\n"
        "s_lshl_b32 s3, s3, 2\n"
        "s_lshr_b32 s33, s30, 2\n"
        "s_add_u32 s33, s33, s3\n"
        "s_and_b32 s3, s29, 1\n"
        "s_lshl_b32 s3, s3, 2\n"
        "s_and_b32 s34, s30, 3\n"
        "s_add_u32 s34, s34, s3\n"
        "s_lshr_b32 s3, s30, 2\n"
        "s_add_u32 s3, s3, s30\n"
        "s_and_b32 s3, s3, 3\n"
        "s_mul_i32 s3, s3, 0\n"
        "s_lshr_b32 s3, s3, 1\n"
        "s_mul_i32 s52, s3, 245760\n"
        "s_lshl_b32 s64, s3, 7\n"
        "s_lshl_b32 s65, s33, 2\n"
        "s_add_u32 s65, s65, s35\n"
        "s_mul_i32 s65, s65, 40960\n"
        "s_and_b32 s62, s35, 1\n"
        "s_mul_i32 s62, s62, 192\n"
        "s_lshr_b32 s63, s35, 1\n"
        "s_movk_i32 s3, 3680\n"
        "s_mov_b32 s66, -46080\n"
        "v_add_u32 v23, s62, v0\n"
        "v_lshlrev_b32_e64 v21, 4, v23\n"
        "v_mul_u32_u24 v20, 6554, v23\n"
        "v_lshrrev_b32_e64 v20, 16, v20\n"
        "v_mad_u32_u24 v248, v20, s3, v21\n"
        "v_and_b32_e64 v22, 12, v20\n"
        "v_mul_u32_u24 v22, 3840, v22\n"
        "v_lshrrev_b32_e64 v20, 4, v20\n"
        "v_mad_i32_i24 v20, v20, s66, v22\n"
        "v_add_u32 v248, v20, v248\n"
        "v_add_u32 v23, 64, v23\n"
        "v_mul_u32_u24 v20, 6554, v23\n"
        "v_lshrrev_b32_e64 v20, 16, v20\n"
        "v_mad_u32_u24 v249, v20, s3, v21\n"
        "v_and_b32_e64 v22, 12, v20\n"
        "v_mul_u32_u24 v22, 3840, v22\n"
        "v_lshrrev_b32_e64 v20, 4, v20\n"
        "v_mad_i32_i24 v20, v20, s66, v22\n"
        "v_add_u32_e64 v249, v20, v249\n"
        "v_add_u32 v23, 128, v0\n"
        "v_lshlrev_b32_e64 v21, 4, v23\n"
        "v_mul_u32_u24 v20, 6554, v23\n"
        "v_lshrrev_b32_e64 v20, 16, v20\n"
        "v_mad_u32_u24 v250, v20, s3, v21\n"
        "v_and_b32_e64 v22, 12, v20\n"
        "v_mul_u32_u24 v22, 3840, v22\n"
        "v_lshrrev_b32_e64 v20, 4, v20\n"
        "v_mad_i32_i24 v20, v20, s66, v22\n"
        "v_add_u32_e64 v250, v20, v250\n"
        "s_mul_i32 s66, s63, 122880\n"
        "s_mul_i32 s3, s34, 160\n"
        "s_add_u32 s66, s66, s3\n"
        "s_add_u32 s67, s66, 1280\n"
        "v_lshlrev_b32 v5, 4, v0\n"
        "s_mul_i32 s69, s34, 3200\n"
        "s_lshl_b32 s3, s35, 10\n"
        "s_add_u32 s69, s69, s3\n"
        "s_movk_i32 s46, 25600\n"
        "s_mov_b32 s47, 0x20000\n"
        "s_waitcnt lgkmcnt(0)\n"
        "s_mov_b32 s44, s8\n"
        "s_and_b32 s45, s9, 0xffff\n"
        "buffer_load_dwordx4 v[162:165], v5, s[44:47], s69 offen\n"
        "s_mov_b32 s12, s64\n"
        "s_lshl_b32 s13, s35, 14\n"
        "s_add_u32 s13, s13, 71680\n"
        "s_add_u32 s36, s4, s65\n"
        "s_addc_u32 s37, s5, 0\n"
        "s_and_b32 s37, s37, 0xffff\n"
        "s_sub_u32 s38, 2621440, s65\n"
        "s_mov_b32 s39, 0x20000\n"
        "s_add_u32 m0, s13, 0\n"
        "s_nop 0\n"
        "buffer_load_dwordx4 v244, s[36:39], s12 offen lds\n"
        "buffer_load_dwordx4 v245, s[36:39], s12 offen offset:1024 lds\n"
        "buffer_load_dwordx4 v246, s[36:39], s12 offen offset:2048 lds\n"
        "buffer_load_dwordx4 v247, s[36:39], s12 offen offset:3072 lds\n"
        "s_add_u32 s12, s12, 128\n"
        "s_cmp_eq_u32 s12, 1280\n"
        "s_cselect_b32 s12, 0, s12\n"
        "s_mul_i32 s15, s63, 5120\n"
        "s_lshl_b32 s62, s62, 4\n"
        "s_add_u32 s14, s15, s62\n"
        "s_add_u32 s15, s15, 2048\n"
        "s_add_u32 s40, s6, s67\n"
        "s_addc_u32 s41, s7, 0\n"
        "s_and_b32 s41, s41, 0xffff\n"
        "s_sub_u32 s42, 2457600, s67\n"
        "s_mov_b32 s43, 0x20000\n"
        "s_bitcmp0_b32 s35, 0\n"
        "s_cbranch_scc1 .Lm243\n"
        "s_add_u32 m0, s15, 0\n"
        "s_nop 0\n"
        "buffer_load_dwordx4 v250, s[40:43], s52 offen lds\n"
        ".Lm243:\n"
        "s_add_u32 m0, s14, 0\n"
        "s_nop 0\n"
        "buffer_load_dwordx4 v248, s[40:43], s52 offen lds\n"
        "buffer_load_dwordx4 v249, s[40:43], s52 offen offset:1024 lds\n"
        "s_add_u32 s53, s52, 245760\n"
        "s_cmp_eq_u32 s53, 2457600\n"
        "s_cselect_b32 s53, 0, s53\n"
        "s_barrier\n"
        "v_mov_b32 v24, 0\n"
        "s_mov_b32 s16, s26\n"
        "s_and_b32 s17, s27, 0xffff\n"
        "s_mov_b32 s18, 8388608\n"
        "s_mov_b32 s19, 0x20000\n"
        "buffer_load_dwordx2 v[6:7], v24, s[16:19], 0 offen sc1\n"
        "s_add_u32 s67, s66, 2560\n"
        "s_add_u32 s44, s6, s67\n"
        "s_addc_u32 s45, s7, 0\n"
        "s_and_b32 s45, s45, 0xffff\n"
        "s_sub_u32 s46, 2457600, s67\n"
        "s_mov_b32 s47, 0x20000\n"
        "s_add_u32 s48, s6, s66\n"
        "s_addc_u32 s49, s7, 0\n"
        "s_and_b32 s49, s49, 0xffff\n"
        "s_sub_u32 s50, 2457600, s66\n"
        "s_mov_b32 s51, 0x20000\n"
        "v_lshrrev_b32_e64 v102, 4, v0\n"
        "v_bfe_u32 v103, v0, 2, 2\n"
        "v_and_b32 v104, 3, v0\n"
        "v_and_b32 v105, 15, v0\n"
        "v_mul_u32_u24 v106, 800, v102\n"
        "v_mul_u32_u24_e64 v107, 40, v103\n"
        "v_add_u32 v106, 137216, v106\n"
        "v_add_u32 v142, v106, v107\n"
        "s_mov_b32 s4, s26\n"
        "s_and_b32 s5, s27, 0xffff\n"
        "s_mov_b32 s6, 8388608\n"
        "s_mov_b32 s7, 0x20000\n"
        "v_and_b32 v25, 3, v0\n"
        "v_lshlrev_b32 v1, 2, v25\n"
        "v_add_u32 v1, 163584, v1\n"
        "s_lshl_b32 s3, s35, 2\n"
        "s_add_u32 s3, s3, 163584\n"
        "v_mov_b32 v2, s3\n"
        "s_and_b32 s3, s35, 1\n"
        "s_mul_i32 s1, s3, 245760\n"
        "s_mul_i32 s3, s3, 10240\n"
        "s_add_u32 s0, s15, s3\n"
        "v_add_u32 v251, s1, v250\n"
        "s_lshl_b32 s64, s34, 2\n"
        "s_add_u32 s64, s64, s35\n"
        "s_lshl_b32 s9, s64, 11\n"
        "s_add_u32 s9, s9, 1572864\n"
        "s_lshl_b32 s3, s64, 7\n"
        "s_add_u32 s3, s3, 49152\n"
        "v_mov_b32 v20, s3\n"
        "s_lshl_b32 s64, s64, 4\n"
        "s_add_u32 s8, s64, s33\n"
        "s_lshl_b32 s8, s8, 11\n"
        "s_add_u32 s8, s8, 262144\n"
        "s_lshl_b32 s64, s64, 7\n"
        "s_add_u32 s64, s64, 65536\n"
        "s_lshl_b32 s67, s33, 7\n"
        "s_add_u32 s67, s67, s64\n"
        "s_lshl_b32 s64, s35, 14\n"
        "s_add_u32 s64, s64, 71680\n"
        "s_add_u32 s65, s64, 10752\n"
        "v_mul_u32_u24 v106, 176, v105\n"
        "v_mul_u32_u24 v107, 40, v102\n"
        "v_add3_u32 v8, v106, v107, s65\n"
        "v_mul_u32_u24 v106, 336, v105\n"
        "v_lshl_add_u32 v106, v107, 1, v106\n"
        "v_add_u32 v9, s64, v106\n"
        "v_lshrrev_b32 v108, 2, v0\n"
        "s_mul_i32 s3, s35, 80\n"
        "s_add_u32 s3, s3, 71680\n"
        "v_mul_u32_u24 v106, 672, v108\n"
        "v_lshlrev_b32_e64 v107, 3, v104\n"
        "v_add3_u32 v10, v106, v107, s3\n"
        "s_mul_i32 s3, s35, 40\n"
        "v_mul_u32_u24 v106, 352, v108\n"
        "v_add_u32 v106, 82432, v106\n"
        "v_add3_u32 v11, v106, v107, s3\n"
        "v_cmp_eq_u32 s[22:23], 0, v104\n"
        "v_add_u32 v106, 160, v106\n"
        "v_add_u32 v107, 32, v11\n"
        "v_cndmask_b32 v12, v106, v107, s[22:23]\n"
        "v_lshlrev_b32_e64 v106, 4, v105\n"
        "v_lshl_add_u32 v13, v102, 7, v106\n"
        "v_lshlrev_b32_e64 v14, 3, v0\n"
        "v_cmp_eq_u32 s[16:17], s35, v104\n"
        "v_cmp_eq_u32 s[18:19], s35, v102\n"
        "s_mul_i32 s63, s35, 800\n"
        "s_add_u32 s63, s63, 137216\n"
        "v_lshrrev_b32 v108, 2, v0\n"
        "v_mul_u32_u24 v108, 40, v108\n"
        "v_add_u32_e64 v108, s63, v108\n"
        "v_lshl_add_u32 v110, v104, 3, v108\n"
        "v_mul_u32_u24_e64 v106, 40, v103\n"
        "s_add_u32 s3, s63, 640\n"
        "v_add_u32_e64 v106, s3, v106\n"
        "v_lshl_add_u32 v111, v104, 3, v106\n"
        "v_mov_b32 v107, 141312\n"
        "v_add_u32 v108, 32, v108\n"
        "v_add_u32 v106, 32, v106\n"
        "s_and_b64 s[22:23], s[16:17], s[18:19]\n"
        "s_lshl_b32 s69, s35, 10\n"
        "s_add_u32 s69, s69, 137216\n"
        "v_add_u32_e64 v166, s69, v5\n"
        "s_mov_b32 s69, 0x3fb8aa3b\n"
        "s_waitcnt vmcnt(7)\n"
        "v_lshlrev_b32 v168, 16, v162\n"
        "v_and_b32 v169, s31, v162\n"
        "v_mul_f32 v168, s69, v168\n"
        "v_mul_f32_e64 v169, s69, v169\n"
        "v_cvt_pk_bf16_f32 v162, v168, v169\n"
        "v_lshlrev_b32 v168, 16, v163\n"
        "v_and_b32 v169, s31, v163\n"
        "v_mul_f32 v168, s69, v168\n"
        "v_mul_f32 v169, s69, v169\n"
        "v_cvt_pk_bf16_f32 v163, v168, v169\n"
        "v_lshlrev_b32 v168, 16, v164\n"
        "v_and_b32 v169, s31, v164\n"
        "v_mul_f32 v168, s69, v168\n"
        "v_mul_f32 v169, s69, v169\n"
        "v_cvt_pk_bf16_f32 v164, v168, v169\n"
        "v_lshlrev_b32 v168, 16, v165\n"
        "v_and_b32 v169, s31, v165\n"
        "v_mul_f32 v168, s69, v168\n"
        "v_mul_f32 v169, s69, v169\n"
        "v_cvt_pk_bf16_f32 v165, v168, v169\n"
        "ds_write_b128 v166, v[162:165]\n"
        "s_add_u32 m0, s13, 4096\n"
        "s_nop 0\n"
        "buffer_load_dwordx4 v244, s[36:39], s12 offen lds\n"
        "buffer_load_dwordx4 v245, s[36:39], s12 offen offset:1024 lds\n"
        "buffer_load_dwordx4 v246, s[36:39], s12 offen offset:2048 lds\n"
        "buffer_load_dwordx4 v247, s[36:39], s12 offen offset:3072 lds\n"
        "s_add_u32 s12, s12, 128\n"
        "s_cmp_eq_u32 s12, 1280\n"
        "s_cselect_b32 s12, 0, s12\n"
        "s_bitcmp1_b32 s35, 0\n"
        "s_cbranch_scc1 .Lm244\n"
        "s_add_u32 m0, s15, 10240\n"
        "s_nop 0\n"
        "buffer_load_dwordx4 v250, s[40:43], s53 offen lds\n"
        ".Lm244:\n"
        "s_add_u32 m0, s14, 10240\n"
        "s_nop 0\n"
        "buffer_load_dwordx4 v248, s[40:43], s53 offen lds\n"
        "buffer_load_dwordx4 v249, s[40:43], s53 offen offset:1024 lds\n"
        "s_add_u32 s54, s53, 245760\n"
        "s_cmp_eq_u32 s54, 2457600\n"
        "s_cselect_b32 s54, 0, s54\n"
        "s_waitcnt vmcnt(7)\n"
        "s_barrier\n"
        "s_add_u32 m0, s13, 8192\n"
        "s_nop 0\n"
        "buffer_load_dwordx4 v244, s[36:39], s12 offen lds\n"
        "v_cndmask_b32 v111, v107, v111, s[18:19]\n"
        "buffer_load_dwordx4 v245, s[36:39], s12 offen offset:1024 lds\n"
        "buffer_load_dwordx4 v246, s[36:39], s12 offen offset:2048 lds\n"
        "v_cndmask_b32 v108, v107, v108, s[16:17]\n"
        "buffer_load_dwordx4 v247, s[36:39], s12 offen offset:3072 lds\n"
        "s_add_u32 s12, s12, 128\n"
        "s_cmp_eq_u32 s12, 1280\n"
        "s_cselect_b32 s12, 0, s12\n"
        "s_bitcmp0_b32 s35, 0\n"
        "s_cbranch_scc1 .Lm245\n"
        "s_add_u32 m0, s15, 20480\n"
        "s_nop 0\n"
        "buffer_load_dwordx4 v250, s[40:43], s54 offen lds\n"
        ".Lm245:\n"
        "v_cndmask_b32 v106, v107, v106, s[22:23]\n"
        "s_add_u32 m0, s14, 20480\n"
        "s_nop 0\n"
        "buffer_load_dwordx4 v248, s[40:43], s54 offen lds\n"
        "buffer_load_dwordx4 v249, s[40:43], s54 offen offset:1024 lds\n"
        "s_add_u32 s55, s54, 245760\n"
        "s_cmp_eq_u32 s55, 2457600\n"
        "s_cselect_b32 s55, 0, s55\n"
        "s_waitcnt vmcnt(6) lgkmcnt(0)\n"
        "s_barrier\n"
        "s_add_u32 m0, s13, 12288\n"
        "s_nop 0\n"
        "buffer_load_dwordx4 v244, s[36:39], s12 offen lds\n"
        "s_bitcmp0_b32 s35, 0\n"
        "s_cbranch_scc1 .Le253\n"
        "v_swap_b32 v110, v111\n"
        "v_swap_b32 v108, v106\n"
        ".Le253:\n"
        "buffer_load_dwordx4 v245, s[36:39], s12 offen offset:1024 lds\n"
        "buffer_load_dwordx4 v246, s[36:39], s12 offen offset:2048 lds\n"
        "buffer_load_dwordx4 v247, s[36:39], s12 offen offset:3072 lds\n"
        "s_add_u32 s12, s12, 128\n"
        "s_cmp_eq_u32 s12, 1280\n"
        "s_cselect_b32 s12, 0, s12\n"
        "s_bitcmp1_b32 s35, 0\n"
        "s_cbranch_scc1 .Lm246\n"
        "s_add_u32 m0, s15, 30720\n"
        "s_nop 0\n"
        "buffer_load_dwordx4 v250, s[40:43], s55 offen lds\n"
        ".Lm246:\n"
        "s_add_u32 m0, s14, 30720\n"
        "s_nop 0\n"
        "buffer_load_dwordx4 v248, s[40:43], s55 offen lds\n"
        "buffer_load_dwordx4 v249, s[40:43], s55 offen offset:1024 lds\n"
        "s_add_u32 s56, s55, 245760\n"
        "s_cmp_eq_u32 s56, 2457600\n"
        "s_cselect_b32 s56, 0, s56\n"
        "s_waitcnt vmcnt(6)\n"
        "s_barrier\n"
        "s_add_u32 m0, s13, 0\n"
        "s_nop 0\n"
        "buffer_load_dwordx4 v244, s[36:39], s12 offen lds\n"
        "s_lshl_b32 s3, s35, 11\n"
        "s_add_u32 s3, s3, 144128\n"
        "buffer_load_dwordx4 v245, s[36:39], s12 offen offset:1024 lds\n"
        "v_add_u32_e64 v15, s3, v5\n"
        "buffer_load_dwordx4 v246, s[36:39], s12 offen offset:2048 lds\n"
        "s_cmp_lt_u32 s30, 10\n"
        "s_cselect_b32 s3, 0, 1\n"
        "s_cselect_b32 s64, 0, 10\n"
        "s_sub_u32 s17, s30, s64\n"
        "s_lshl_b32 s16, s29, 1\n"
        "s_add_u32 s16, s16, s3\n"
        "v_mov_b32 v192, 0x7f817f81\n"
        "buffer_load_dwordx4 v247, s[36:39], s12 offen offset:3072 lds\n"
        "s_add_u32 s12, s12, 128\n"
        "s_cmp_eq_u32 s12, 1280\n"
        "s_cselect_b32 s12, 0, s12\n"
        "v_mov_b32 v193, 0x7f817f81\n"
        "s_bitcmp0_b32 s35, 0\n"
        "s_cbranch_scc1 .Lm247\n"
        "s_add_u32 m0, s15, 40960\n"
        "s_nop 0\n"
        "buffer_load_dwordx4 v250, s[40:43], s56 offen lds\n"
        ".Lm247:\n"
        "ds_read_b64_tr_b16 v[32:33], v142 offset:0\n"
        "ds_read_b64_tr_b16 v[34:35], v142 offset:8\n"
        "s_add_u32 m0, s14, 40960\n"
        "s_nop 0\n"
        "buffer_load_dwordx4 v248, s[40:43], s56 offen lds\n"
        "ds_read_b64_tr_b16 v[36:37], v142 offset:16\n"
        "buffer_load_dwordx4 v249, s[40:43], s56 offen offset:1024 lds\n"
        "s_add_u32 s57, s56, 245760\n"
        "s_cmp_eq_u32 s57, 2457600\n"
        "s_cselect_b32 s57, 0, s57\n"
        "ds_read_b64_tr_b16 v[38:39], v142 offset:24\n"
        "s_waitcnt vmcnt(6)\n"
        "s_barrier\n"
        "s_add_u32 m0, s13, 4096\n"
        "s_nop 0\n"
        "buffer_load_dwordx4 v244, s[36:39], s12 offen lds\n"
        "v_mov_b32 v194, 0x7f817f81\n"
        "buffer_load_dwordx4 v245, s[36:39], s12 offen offset:1024 lds\n"
        "v_mov_b32 v195, 0x7f817f81\n"
        "buffer_load_dwordx4 v246, s[36:39], s12 offen offset:2048 lds\n"
        "ds_read_b64_tr_b16 v[40:41], v142 offset:32\n"
        "buffer_load_dwordx4 v247, s[36:39], s12 offen offset:3072 lds\n"
        "s_add_u32 s12, s12, 128\n"
        "s_cmp_eq_u32 s12, 1280\n"
        "s_cselect_b32 s12, 0, s12\n"
        "ds_read_b64_tr_b16 v[42:43], v142 offset:160\n"
        "s_bitcmp1_b32 s35, 0\n"
        "s_cbranch_scc1 .Lm248\n"
        "s_add_u32 m0, s15, 51200\n"
        "s_nop 0\n"
        "buffer_load_dwordx4 v250, s[40:43], s57 offen lds\n"
        ".Lm248:\n"
        "ds_read_b64_tr_b16 v[44:45], v142 offset:168\n"
        "s_add_u32 m0, s14, 51200\n"
        "s_nop 0\n"
        "buffer_load_dwordx4 v248, s[40:43], s57 offen lds\n"
        "ds_read_b64_tr_b16 v[46:47], v142 offset:176\n"
        "buffer_load_dwordx4 v249, s[40:43], s57 offen offset:1024 lds\n"
        "s_add_u32 s58, s57, 245760\n"
        "s_cmp_eq_u32 s58, 2457600\n"
        "s_cselect_b32 s58, 0, s58\n"
        "s_waitcnt vmcnt(6)\n"
        "s_barrier\n"
        "s_add_u32 m0, s13, 8192\n"
        "s_nop 0\n"
        "buffer_load_dwordx4 v244, s[36:39], s12 offen lds\n"
        "v_mov_b32 v196, 163632\n"
        "v_mov_b32 v197, v14\n"
        "s_lshl_b32 s3, s33, 2\n"
        "buffer_load_dwordx4 v245, s[36:39], s12 offen offset:1024 lds\n"
        "s_add_u32 s3, s3, s35\n"
        "s_lshl_b32 s3, s3, 3\n"
        "s_add_u32 s3, s3, s34\n"
        "buffer_load_dwordx4 v246, s[36:39], s12 offen offset:2048 lds\n"
        "s_mul_i32 s18, s3, 5120\n"
        "s_add_u32 s18, s18, 2097152\n"
        "buffer_load_dwordx4 v247, s[36:39], s12 offen offset:3072 lds\n"
        "s_add_u32 s12, s12, 128\n"
        "s_cmp_eq_u32 s12, 1280\n"
        "s_cselect_b32 s12, 0, s12\n"
        "s_add_u32 s19, s18, 4096\n"
        "s_lshl_b32 s1, s2, 7\n"
        "s_add_u32 s1, s1, 4096\n"
        "s_bitcmp0_b32 s35, 0\n"
        "s_cbranch_scc1 .Lm249\n"
        "s_add_u32 m0, s15, 61440\n"
        "s_nop 0\n"
        "buffer_load_dwordx4 v250, s[40:43], s58 offen lds\n"
        ".Lm249:\n"
        "s_cmp_eq_u32 s35, 0\n"
        "s_cselect_b32 s1, s1, 8388608\n"
        "s_mov_b32 s66, 0x3fb8aa3b\n"
        "s_add_u32 m0, s14, 61440\n"
        "s_nop 0\n"
        "buffer_load_dwordx4 v248, s[40:43], s58 offen lds\n"
        "v_and_b32_e64 v107, 31, v0\n"
        "ds_read_b64_tr_b16 v[48:49], v142 offset:184\n"
        "ds_read_b64_tr_b16 v[50:51], v142 offset:192\n"
        "buffer_load_dwordx4 v249, s[40:43], s58 offen offset:1024 lds\n"
        "s_add_u32 s59, s58, 245760\n"
        "s_cmp_eq_u32 s59, 2457600\n"
        "s_cselect_b32 s59, 0, s59\n"
        "ds_read_b64_tr_b16 v[52:53], v142 offset:320\n"
        "ds_read_b64_tr_b16 v[54:55], v142 offset:328\n"
        "s_waitcnt vmcnt(6)\n"
        "s_barrier\n"
        "s_add_u32 m0, s13, 12288\n"
        "s_nop 0\n"
        "buffer_load_dwordx4 v244, s[36:39], s12 offen lds\n"
        "v_mul_u32_u24 v107, 176, v107\n"
        "buffer_load_dwordx4 v245, s[36:39], s12 offen offset:1024 lds\n"
        "v_add_u32_e64 v26, s65, v107\n"
        "buffer_load_dwordx4 v246, s[36:39], s12 offen offset:2048 lds\n"
        "ds_read_b64_tr_b16 v[56:57], v142 offset:336\n"
        "buffer_load_dwordx4 v247, s[36:39], s12 offen offset:3072 lds\n"
        "s_add_u32 s12, s12, 128\n"
        "s_cmp_eq_u32 s12, 1280\n"
        "s_cselect_b32 s12, 0, s12\n"
        "ds_read_b64_tr_b16 v[58:59], v142 offset:344\n"
        "s_bitcmp1_b32 s35, 0\n"
        "s_cbranch_scc1 .Lm250\n"
        "s_add_u32 m0, s15, 0\n"
        "s_nop 0\n"
        "buffer_load_dwordx4 v250, s[40:43], s59 offen lds\n"
        ".Lm250:\n"
        "ds_read_b64_tr_b16 v[60:61], v142 offset:352\n"
        "s_add_u32 m0, s14, 0\n"
        "s_nop 0\n"
        "buffer_load_dwordx4 v248, s[40:43], s59 offen lds\n"
        "ds_read_b64_tr_b16 v[62:63], v142 offset:480\n"
        "buffer_load_dwordx4 v249, s[40:43], s59 offen offset:1024 lds\n"
        "s_add_u32 s60, s59, 245760\n"
        "s_cmp_eq_u32 s60, 2457600\n"
        "s_cselect_b32 s60, 0, s60\n"
        "s_waitcnt vmcnt(6)\n"
        "s_barrier\n"
        "s_add_u32 m0, s13, 0\n"
        "s_nop 0\n"
        "buffer_load_dwordx4 v244, s[36:39], s12 offen lds\n"
        "v_mov_b32 v28, 0x3f80\n"
        "buffer_load_dwordx4 v245, s[36:39], s12 offen offset:1024 lds\n"
        "v_mov_b32_e64 v29, 0\n"
        "buffer_load_dwordx4 v246, s[36:39], s12 offen offset:2048 lds\n"
        "ds_read_b64_tr_b16 v[64:65], v142 offset:488\n"
        "buffer_load_dwordx4 v247, s[36:39], s12 offen offset:3072 lds\n"
        "s_add_u32 s12, s12, 128\n"
        "s_cmp_eq_u32 s12, 1280\n"
        "s_cselect_b32 s12, 0, s12\n"
        "ds_read_b64_tr_b16 v[66:67], v142 offset:496\n"
        "s_bitcmp0_b32 s35, 0\n"
        "s_cbranch_scc1 .Lm251\n"
        "s_add_u32 m0, s15, 10240\n"
        "s_nop 0\n"
        "buffer_load_dwordx4 v250, s[40:43], s60 offen lds\n"
        ".Lm251:\n"
        "ds_read_b64_tr_b16 v[68:69], v142 offset:504\n"
        "s_add_u32 m0, s14, 10240\n"
        "s_nop 0\n"
        "buffer_load_dwordx4 v248, s[40:43], s60 offen lds\n"
        "ds_read_b64_tr_b16 v[70:71], v142 offset:512\n"
        "buffer_load_dwordx4 v249, s[40:43], s60 offen offset:1024 lds\n"
        "s_add_u32 s61, s60, 245760\n"
        "s_cmp_eq_u32 s61, 2457600\n"
        "s_cselect_b32 s61, 0, s61\n"
        "s_waitcnt vmcnt(6)\n"
        "s_barrier\n"
        "s_add_u32 m0, s13, 4096\n"
        "s_nop 0\n"
        "buffer_load_dwordx4 v244, s[36:39], s12 offen lds\n"
        "ds_read_b64_tr_b16 v[72:73], v142 offset:640\n"
        "buffer_load_dwordx4 v245, s[36:39], s12 offen offset:1024 lds\n"
        "ds_read_b64_tr_b16 v[74:75], v142 offset:648\n"
        "buffer_load_dwordx4 v246, s[36:39], s12 offen offset:2048 lds\n"
        "ds_read_b64_tr_b16 v[76:77], v142 offset:656\n"
        "buffer_load_dwordx4 v247, s[36:39], s12 offen offset:3072 lds\n"
        "s_add_u32 s12, s12, 128\n"
        "s_cmp_eq_u32 s12, 1280\n"
        "s_cselect_b32 s12, 0, s12\n"
        "s_bitcmp1_b32 s35, 0\n"
        "s_cbranch_scc1 .Lm252\n"
        "s_add_u32 m0, s15, 20480\n"
        "s_nop 0\n"
        "buffer_load_dwordx4 v250, s[40:43], s61 offen lds\n"
        ".Lm252:\n"
        "ds_read_b64_tr_b16 v[78:79], v142 offset:664\n"
        "s_add_u32 m0, s14, 20480\n"
        "s_nop 0\n"
        "buffer_load_dwordx4 v248, s[40:43], s61 offen lds\n"
        "ds_read_b64_tr_b16 v[80:81], v142 offset:672\n"
        "buffer_load_dwordx4 v249, s[40:43], s61 offen offset:1024 lds\n"
        "s_waitcnt vmcnt(6)\n"
        "s_barrier\n"
        "s_add_u32 m0, s0, 30720\n"
        "ds_read_b64_tr_b16 v[182:183], v110\n"
        "buffer_load_dwordx4 v251, s[44:47], s52 offen lds\n"
        "ds_read_b64_tr_b16 v[184:185], v111\n"
        "s_add_u32 m0, s14, 30720\n"
        "ds_read_b64_tr_b16 v[186:187], v108\n"
        "buffer_load_dwordx4 v248, s[44:47], s52 offen lds\n"
        "buffer_load_dwordx4 v249, s[44:47], s52 offen offset:1024 lds\n"
        "ds_read_b64_tr_b16 v[188:189], v106\n"
        "s_add_u32 m0, s14, 40960\n"
        "v_add_co_u32 v6, vcc, 1, v6\n"
        "buffer_load_dwordx4 v248, s[44:47], s53 offen lds\n"
        "s_nop 0\n"
        "v_addc_co_u32 v7, vcc, 0, v7, vcc\n"
        "buffer_load_dwordx4 v249, s[44:47], s53 offen offset:1024 lds\n"
        "s_waitcnt vmcnt(5)\n"
        "s_barrier\n"
        "s_mov_b64 exec, 0xffff\n"
        "buffer_store_dwordx2 v[6:7], v197, s[4:7], s1 offen sc1\n"
        "s_mov_b64 exec, -1\n"
        "buffer_store_dwordx4 v[192:195], v5, s[4:7], s8 offen offset:0 sc1\n"
        "buffer_store_dwordx4 v[192:195], v5, s[4:7], s8 offen offset:1024 sc1\n"
        "s_waitcnt vmcnt(5)\n"
        "s_barrier\n"
        "ds_write_b64 v26, v[28:29] offset:160\n"
        "s_sleep 6\n"
        "s_add_u32 m0, s0, 51200\n"
        "s_nop 0\n"
        "buffer_load_dwordx4 v251, s[44:47], s54 offen lds\n"
        "s_add_u32 m0, s14, 51200\n"
        "s_nop 0\n"
        "buffer_load_dwordx4 v248, s[44:47], s54 offen lds\n"
        "buffer_load_dwordx4 v249, s[44:47], s54 offen offset:1024 lds\n"
        "s_add_u32 m0, s14, 61440\n"
        "s_nop 0\n"
        "buffer_load_dwordx4 v248, s[44:47], s55 offen lds\n"
        "buffer_load_dwordx4 v249, s[44:47], s55 offen offset:1024 lds\n"
        "s_waitcnt vmcnt(8)\n"
        "s_barrier\n"
        ".Ls0:\n"
        "ds_read_b64 v[82:83], v8 offset:0\n"
        "ds_read_b64 v[84:85], v8 offset:8\n"
        "ds_read_b64 v[86:87], v8 offset:16\n"
        "ds_read_b64 v[88:89], v8 offset:24\n"
        "ds_read_b64 v[90:91], v8 offset:32\n"
        "ds_read_b64 v[92:93], v8 offset:2816\n"
        "ds_read_b64 v[94:95], v8 offset:2824\n"
        "ds_read_b64 v[96:97], v8 offset:2832\n"
        "ds_read_b64 v[98:99], v8 offset:2840\n"
        "ds_read_b64 v[100:101], v8 offset:2848\n"
        "s_add_u32 m0, s0, 0\n"
        "s_nop 0\n"
        "buffer_load_dwordx4 v251, s[44:47], s56 offen lds\n"
        "s_add_u32 m0, s14, 0\n"
        "s_nop 0\n"
        "buffer_load_dwordx4 v248, s[44:47], s56 offen lds\n"
        "buffer_load_dwordx4 v249, s[44:47], s56 offen offset:1024 lds\n"
        "s_add_u32 m0, s14, 10240\n"
        "s_nop 0\n"
        "buffer_load_dwordx4 v248, s[44:47], s57 offen lds\n"
        "buffer_load_dwordx4 v249, s[44:47], s57 offen offset:1024 lds\n"
        "s_waitcnt lgkmcnt(9)\n"
        "v_mfma_f32_4x4x4_16b_bf16 v[102:105], v[32:33], v[82:83], 0\n"
        "v_mfma_f32_4x4x4_16b_bf16 v[106:109], v[34:35], v[82:83], 0\n"
        "v_mfma_f32_4x4x4_16b_bf16 v[110:113], v[36:37], v[82:83], 0\n"
        "v_mfma_f32_4x4x4_16b_bf16 v[114:117], v[38:39], v[82:83], 0\n"
        "v_mfma_f32_4x4x4_16b_bf16 v[118:121], v[40:41], v[82:83], 0\n"
        "s_waitcnt lgkmcnt(8)\n"
        "v_mfma_f32_4x4x4_16b_bf16 v[102:105], v[42:43], v[84:85], v[102:105]\n"
        "v_mfma_f32_4x4x4_16b_bf16 v[106:109], v[44:45], v[84:85], v[106:109]\n"
        "v_mfma_f32_4x4x4_16b_bf16 v[110:113], v[46:47], v[84:85], v[110:113]\n"
        "v_mfma_f32_4x4x4_16b_bf16 v[114:117], v[48:49], v[84:85], v[114:117]\n"
        "v_mfma_f32_4x4x4_16b_bf16 v[118:121], v[50:51], v[84:85], v[118:121]\n"
        "s_waitcnt lgkmcnt(7)\n"
        "v_mfma_f32_4x4x4_16b_bf16 v[102:105], v[52:53], v[86:87], v[102:105]\n"
        "v_mfma_f32_4x4x4_16b_bf16 v[106:109], v[54:55], v[86:87], v[106:109]\n"
        "v_mfma_f32_4x4x4_16b_bf16 v[110:113], v[56:57], v[86:87], v[110:113]\n"
        "v_mfma_f32_4x4x4_16b_bf16 v[114:117], v[58:59], v[86:87], v[114:117]\n"
        "v_mfma_f32_4x4x4_16b_bf16 v[118:121], v[60:61], v[86:87], v[118:121]\n"
        "s_waitcnt lgkmcnt(6)\n"
        "v_mfma_f32_4x4x4_16b_bf16 v[102:105], v[62:63], v[88:89], v[102:105]\n"
        "v_mfma_f32_4x4x4_16b_bf16 v[106:109], v[64:65], v[88:89], v[106:109]\n"
        "v_mfma_f32_4x4x4_16b_bf16 v[110:113], v[66:67], v[88:89], v[110:113]\n"
        "v_mfma_f32_4x4x4_16b_bf16 v[114:117], v[68:69], v[88:89], v[114:117]\n"
        "v_mfma_f32_4x4x4_16b_bf16 v[118:121], v[70:71], v[88:89], v[118:121]\n"
        "s_waitcnt lgkmcnt(5)\n"
        "v_mfma_f32_4x4x4_16b_bf16 v[102:105], v[72:73], v[90:91], v[102:105]\n"
        "v_mfma_f32_4x4x4_16b_bf16 v[106:109], v[74:75], v[90:91], v[106:109]\n"
        "v_mfma_f32_4x4x4_16b_bf16 v[110:113], v[76:77], v[90:91], v[110:113]\n"
        "v_mfma_f32_4x4x4_16b_bf16 v[114:117], v[78:79], v[90:91], v[114:117]\n"
        "v_mfma_f32_4x4x4_16b_bf16 v[118:121], v[80:81], v[90:91], v[118:121]\n"
        "s_nop 0\n"
        "s_nop 0\n"
        "v_exp_f32 v142, -v102\n"
        "v_exp_f32 v143, -v103\n"
        "v_exp_f32 v144, -v104\n"
        "v_exp_f32 v145, -v105\n"
        "v_exp_f32 v146, -v106\n"
        "v_exp_f32 v147, -v107\n"
        "s_waitcnt vmcnt(5)\n"
        "s_barrier\n"
        "s_memrealtime s[36:37]\n"
        "v_exp_f32 v148, -v108\n"
        "s_add_u32 m0, s0, 20480\n"
        "v_exp_f32 v149, -v109\n"
        "buffer_load_dwordx4 v251, s[44:47], s58 offen lds\n"
        "v_exp_f32 v150, -v110\n"
        "s_add_u32 m0, s14, 20480\n"
        "v_exp_f32 v151, -v111\n"
        "buffer_load_dwordx4 v248, s[44:47], s58 offen lds\n"
        "v_exp_f32 v152, -v112\n"
        "buffer_load_dwordx4 v249, s[44:47], s58 offen offset:1024 lds\n"
        "v_exp_f32 v153, -v113\n"
        "s_add_u32 m0, s14, 30720\n"
        "v_exp_f32 v154, -v114\n"
        "buffer_load_dwordx4 v248, s[44:47], s59 offen lds\n"
        "v_exp_f32 v155, -v115\n"
        "buffer_load_dwordx4 v249, s[44:47], s59 offen offset:1024 lds\n"
        "v_exp_f32 v156, -v116\n"
        "v_exp_f32 v157, -v117\n"
        "v_exp_f32 v158, -v118\n"
        "v_exp_f32 v159, -v119\n"
        "v_exp_f32 v160, -v120\n"
        "v_exp_f32 v161, -v121\n"
        "v_exp_f32 v162, v102\n"
        "v_exp_f32 v163, v103\n"
        "v_exp_f32 v164, v104\n"
        "v_exp_f32 v165, v105\n"
        "v_exp_f32 v166, v106\n"
        "v_exp_f32 v167, v107\n"
        "v_exp_f32 v168, v108\n"
        "v_exp_f32 v169, v109\n"
        "v_exp_f32 v170, v110\n"
        "v_exp_f32 v171, v111\n"
        "v_exp_f32 v172, v112\n"
        "v_exp_f32 v173, v113\n"
        "v_exp_f32 v174, v114\n"
        "v_exp_f32 v175, v115\n"
        "v_exp_f32 v176, v116\n"
        "v_exp_f32 v177, v117\n"
        "v_exp_f32 v178, v118\n"
        "v_exp_f32 v179, v119\n"
        "s_waitcnt vmcnt(5)\n"
        "s_barrier\n"
        "v_exp_f32_e64 v180, v120\n"
        "s_add_u32 m0, s0, 40960\n"
        "v_exp_f32_e64 v181, v121\n"
        "buffer_load_dwordx4 v251, s[44:47], s60 offen lds\n"
        "v_add_f32 v25, v142, v143\n"
        "v_add_f32 v24, v162, v163\n"
        "s_add_u32 m0, s14, 40960\n"
        "v_add_f32 v25, v25, v144\n"
        "v_add_f32 v24, v24, v164\n"
        "buffer_load_dwordx4 v248, s[44:47], s60 offen lds\n"
        "v_add_f32 v25, v25, v145\n"
        "v_add_f32 v24, v24, v165\n"
        "buffer_load_dwordx4 v249, s[44:47], s60 offen offset:1024 lds\n"
        "v_add_f32 v25, v25, v146\n"
        "v_add_f32 v24, v24, v166\n"
        "s_add_u32 m0, s14, 51200\n"
        "v_add_f32 v25, v25, v147\n"
        "v_add_f32 v24, v24, v167\n"
        "buffer_load_dwordx4 v248, s[44:47], s61 offen lds\n"
        "v_add_f32 v25, v25, v148\n"
        "v_add_f32 v24, v24, v168\n"
        "buffer_load_dwordx4 v249, s[44:47], s61 offen offset:1024 lds\n"
        "v_add_f32 v25, v25, v149\n"
        "v_add_f32 v24, v24, v169\n"
        "v_add_f32 v25, v25, v150\n"
        "v_add_f32 v24, v24, v170\n"
        "v_add_f32 v25, v25, v151\n"
        "v_add_f32 v24, v24, v171\n"
        "v_add_f32 v25, v25, v152\n"
        "v_add_f32 v24, v24, v172\n"
        "v_add_f32 v25, v25, v153\n"
        "v_add_f32 v24, v24, v173\n"
        "v_add_f32 v25, v25, v154\n"
        "v_add_f32 v24, v24, v174\n"
        "v_add_f32 v25, v25, v155\n"
        "v_add_f32 v24, v24, v175\n"
        "v_add_f32 v25, v25, v156\n"
        "v_add_f32 v24, v24, v176\n"
        "v_add_f32 v25, v25, v157\n"
        "v_add_f32 v24, v24, v177\n"
        "v_add_f32 v25, v25, v158\n"
        "v_add_f32 v24, v24, v178\n"
        "v_add_f32 v25, v25, v159\n"
        "v_add_f32 v24, v24, v179\n"
        "v_add_f32 v25, v25, v160\n"
        "v_add_f32 v24, v24, v180\n"
        "v_add_f32 v25, v25, v161\n"
        "v_add_f32 v24, v24, v181\n"
        "v_max_f32_e64 v27, v24, v25\n"
        "v_cmp_lt_f32 vcc, 0x53800000, v27\n"
        "s_nop 0\n"
        "s_cbranch_vccnz .Lks254\n"
        ".Lkj255:\n"
        "v_rcp_f32 v24, v24\n"
        "v_rcp_f32 v25, v25\n"
        "v_mul_f32 v162, v24, v162\n"
        "v_mul_f32 v163, v24, v163\n"
        "v_cvt_pk_bf16_f32 v102, v162, v163\n"
        "v_mul_f32 v164, v24, v164\n"
        "v_mul_f32 v165, v24, v165\n"
        "v_cvt_pk_bf16_f32 v103, v164, v165\n"
        "v_mul_f32 v166, v24, v166\n"
        "v_mul_f32 v167, v24, v167\n"
        "v_cvt_pk_bf16_f32 v104, v166, v167\n"
        "v_mul_f32 v168, v24, v168\n"
        "v_mul_f32 v169, v24, v169\n"
        "v_cvt_pk_bf16_f32 v105, v168, v169\n"
        "v_mul_f32 v170, v24, v170\n"
        "v_mul_f32 v171, v24, v171\n"
        "v_cvt_pk_bf16_f32 v106, v170, v171\n"
        "s_waitcnt vmcnt(5)\n"
        "s_barrier\n"
        "s_memrealtime s[62:63]\n"
        "v_mov_b32 v200, v6\n"
        "v_mov_b32 v201, v7\n"
        "v_mul_f32 v172, v24, v172\n"
        "v_mul_f32 v173, v24, v173\n"
        "s_add_u32 m0, s15, 61440\n"
        "v_cvt_pk_bf16_f32 v107, v172, v173\n"
        "v_mul_f32_e64 v174, v24, v174\n"
        "buffer_load_dwordx4 v250, s[48:51], s52 offen lds\n"
        "v_mul_f32_e64 v175, v24, v175\n"
        "v_cvt_pk_bf16_f32 v108, v174, v175\n"
        "s_add_u32 m0, s14, 61440\n"
        "v_mul_f32 v176, v24, v176\n"
        "v_mul_f32 v177, v24, v177\n"
        "buffer_load_dwordx4 v248, s[48:51], s52 offen lds\n"
        "v_cvt_pk_bf16_f32 v109, v176, v177\n"
        "v_mul_f32_e64 v178, v24, v178\n"
        "buffer_load_dwordx4 v249, s[48:51], s52 offen offset:1024 lds\n"
        "v_mul_f32_e64 v179, v24, v179\n"
        "v_cvt_pk_bf16_f32 v110, v178, v179\n"
        "s_add_u32 m0, s15, 0\n"
        "v_mul_f32 v180, v24, v180\n"
        "v_mul_f32_e64 v181, v24, v181\n"
        "buffer_load_dwordx4 v250, s[48:51], s53 offen lds\n"
        "v_cvt_pk_bf16_f32 v111, v180, v181\n"
        "v_mul_f32 v142, v25, v142\n"
        "s_add_u32 m0, s14, 0\n"
        "v_mul_f32_e64 v143, v25, v143\n"
        "v_cvt_pk_bf16_f32 v112, v142, v143\n"
        "buffer_load_dwordx4 v248, s[48:51], s53 offen lds\n"
        "v_mul_f32 v144, v25, v144\n"
        "v_mul_f32 v145, v25, v145\n"
        "buffer_load_dwordx4 v249, s[48:51], s53 offen offset:1024 lds\n"
        "v_cvt_pk_bf16_f32 v113, v144, v145\n"
        "v_mul_f32 v146, v25, v146\n"
        "v_mul_f32 v147, v25, v147\n"
        "v_cvt_pk_bf16_f32 v114, v146, v147\n"
        "s_waitcnt lgkmcnt(0)\n"
        "s_sub_u32 s36, s62, s36\n"
        "s_mul_i32 s36, s36, 42\n"
        "s_lshr_b32 s36, s36, 4\n"
        "s_min_u32 s36, s36, 256\n"
        "s_add_u32 s62, s62, s36\n"
        "s_sub_u32 s62, s62, 256\n"
        "v_mov_b32 v202, s62\n"
        "s_mov_b64 exec, 0xff\n"
        "buffer_store_dwordx4 v[200:203], v5, s[4:7], s67 offen sc1\n"
        "s_mov_b64 exec, -1\n"
        "v_mul_f32 v148, v25, v148\n"
        "v_mul_f32_e64 v149, v25, v149\n"
        "v_cvt_pk_bf16_f32 v115, v148, v149\n"
        "v_mul_f32 v150, v25, v150\n"
        "v_mul_f32 v151, v25, v151\n"
        "v_cvt_pk_bf16_f32 v116, v150, v151\n"
        "v_mul_f32 v152, v25, v152\n"
        "v_mul_f32 v153, v25, v153\n"
        "v_cvt_pk_bf16_f32 v117, v152, v153\n"
        "v_mul_f32 v154, v25, v154\n"
        "v_mul_f32 v155, v25, v155\n"
        "v_cvt_pk_bf16_f32 v118, v154, v155\n"
        "v_mul_f32_e64 v156, v25, v156\n"
        "v_mfma_f32_4x4x4_16b_bf16 v[122:125], v[32:33], v[92:93], 0\n"
        "v_mfma_f32_4x4x4_16b_bf16 v[126:129], v[34:35], v[92:93], 0\n"
        "v_mfma_f32_4x4x4_16b_bf16 v[130:133], v[36:37], v[92:93], 0\n"
        "v_mfma_f32_4x4x4_16b_bf16 v[134:137], v[38:39], v[92:93], 0\n"
        "v_mfma_f32_4x4x4_16b_bf16 v[138:141], v[40:41], v[92:93], 0\n"
        "v_mfma_f32_4x4x4_16b_bf16 v[122:125], v[42:43], v[94:95], v[122:125]\n"
        "v_mfma_f32_4x4x4_16b_bf16 v[126:129], v[44:45], v[94:95], v[126:129]\n"
        "v_mfma_f32_4x4x4_16b_bf16 v[130:133], v[46:47], v[94:95], v[130:133]\n"
        "v_mfma_f32_4x4x4_16b_bf16 v[134:137], v[48:49], v[94:95], v[134:137]\n"
        "v_mfma_f32_4x4x4_16b_bf16 v[138:141], v[50:51], v[94:95], v[138:141]\n"
        "v_mfma_f32_4x4x4_16b_bf16 v[122:125], v[52:53], v[96:97], v[122:125]\n"
        "v_mfma_f32_4x4x4_16b_bf16 v[126:129], v[54:55], v[96:97], v[126:129]\n"
        "v_mfma_f32_4x4x4_16b_bf16 v[130:133], v[56:57], v[96:97], v[130:133]\n"
        "v_mfma_f32_4x4x4_16b_bf16 v[134:137], v[58:59], v[96:97], v[134:137]\n"
        "v_mfma_f32_4x4x4_16b_bf16 v[138:141], v[60:61], v[96:97], v[138:141]\n"
        "v_mfma_f32_4x4x4_16b_bf16 v[122:125], v[62:63], v[98:99], v[122:125]\n"
        "v_mfma_f32_4x4x4_16b_bf16 v[126:129], v[64:65], v[98:99], v[126:129]\n"
        "v_mfma_f32_4x4x4_16b_bf16 v[130:133], v[66:67], v[98:99], v[130:133]\n"
        "v_mfma_f32_4x4x4_16b_bf16 v[134:137], v[68:69], v[98:99], v[134:137]\n"
        "v_mfma_f32_4x4x4_16b_bf16 v[138:141], v[70:71], v[98:99], v[138:141]\n"
        "v_mfma_f32_4x4x4_16b_bf16 v[122:125], v[72:73], v[100:101], v[122:125]\n"
        "v_mfma_f32_4x4x4_16b_bf16 v[126:129], v[74:75], v[100:101], v[126:129]\n"
        "v_mfma_f32_4x4x4_16b_bf16 v[130:133], v[76:77], v[100:101], v[130:133]\n"
        "v_mfma_f32_4x4x4_16b_bf16 v[134:137], v[78:79], v[100:101], v[134:137]\n"
        "v_mfma_f32_4x4x4_16b_bf16 v[138:141], v[80:81], v[100:101], v[138:141]\n"
        "v_mul_f32_e64 v157, v25, v157\n"
        "v_cvt_pk_bf16_f32 v119, v156, v157\n"
        "v_mul_f32 v158, v25, v158\n"
        "v_mul_f32 v159, v25, v159\n"
        "v_cvt_pk_bf16_f32 v120, v158, v159\n"
        "v_mul_f32 v160, v25, v160\n"
        "v_mul_f32 v161, v25, v161\n"
        "v_cvt_pk_bf16_f32 v121, v160, v161\n"
        "v_exp_f32 v142, -v122\n"
        "v_exp_f32 v143, -v123\n"
        "v_exp_f32 v144, -v124\n"
        "v_exp_f32 v145, -v125\n"
        "v_exp_f32 v146, -v126\n"
        "v_exp_f32 v147, -v127\n"
        "v_exp_f32 v148, -v128\n"
        "v_exp_f32 v149, -v129\n"
        "v_exp_f32 v150, -v130\n"
        "v_exp_f32 v151, -v131\n"
        "s_waitcnt vmcnt(7)\n"
        "s_barrier\n"
        "v_exp_f32 v152, -v132\n"
        "s_add_u32 m0, s0, 10240\n"
        "v_exp_f32 v153, -v133\n"
        "buffer_load_dwordx4 v251, s[48:51], s54 offen lds\n"
        "v_exp_f32 v154, -v134\n"
        "s_add_u32 m0, s14, 10240\n"
        "v_exp_f32 v155, -v135\n"
        "buffer_load_dwordx4 v248, s[48:51], s54 offen lds\n"
        "v_exp_f32 v156, -v136\n"
        "buffer_load_dwordx4 v249, s[48:51], s54 offen offset:1024 lds\n"
        "v_exp_f32 v157, -v137\n"
        "s_add_u32 m0, s14, 20480\n"
        "v_exp_f32 v158, -v138\n"
        "buffer_load_dwordx4 v248, s[48:51], s55 offen lds\n"
        "ds_write_b128 v9, v[102:105] offset:0\n"
        "v_exp_f32 v159, -v139\n"
        "buffer_load_dwordx4 v249, s[48:51], s55 offen offset:1024 lds\n"
        "v_exp_f32 v160, -v140\n"
        "ds_write_b128 v9, v[106:109] offset:16\n"
        "v_exp_f32 v161, -v141\n"
        "v_exp_f32_e64 v162, v122\n"
        "ds_write_b128 v9, v[110:113] offset:32\n"
        "v_exp_f32 v163, v123\n"
        "v_exp_f32 v164, v124\n"
        "v_exp_f32 v165, v125\n"
        "v_exp_f32 v166, v126\n"
        "v_exp_f32 v167, v127\n"
        "v_exp_f32 v168, v128\n"
        "v_exp_f32 v169, v129\n"
        "v_exp_f32 v170, v130\n"
        "v_exp_f32 v171, v131\n"
        "v_exp_f32 v172, v132\n"
        "v_exp_f32 v173, v133\n"
        "v_exp_f32 v174, v134\n"
        "v_exp_f32 v175, v135\n"
        "v_exp_f32 v176, v136\n"
        "v_exp_f32 v177, v137\n"
        "v_exp_f32 v178, v138\n"
        "v_exp_f32 v179, v139\n"
        "v_exp_f32 v180, v140\n"
        "v_exp_f32_e64 v181, v141\n"
        "buffer_store_dwordx4 v[192:195], v5, s[4:7], s18 offen offset:0 sc1\n"
        "v_add_f32 v25, v142, v143\n"
        "v_add_f32 v24, v162, v163\n"
        "v_add_f32 v25, v25, v144\n"
        "v_add_f32 v24, v24, v164\n"
        "v_add_f32 v25, v25, v145\n"
        "v_add_f32 v24, v24, v165\n"
        "buffer_store_dwordx4 v[192:195], v5, s[4:7], s18 offen offset:1024 sc1\n"
        "v_add_f32 v25, v25, v146\n"
        "v_add_f32 v24, v24, v166\n"
        "v_add_f32 v25, v25, v147\n"
        "v_add_f32 v24, v24, v167\n"
        "v_add_f32 v25, v25, v148\n"
        "v_add_f32 v24, v24, v168\n"
        "buffer_store_dwordx4 v[192:195], v5, s[4:7], s18 offen offset:2048 sc1\n"
        "v_add_f32 v25, v25, v149\n"
        "v_add_f32 v24, v24, v169\n"
        "v_add_f32 v25, v25, v150\n"
        "v_add_f32 v24, v24, v170\n"
        "v_add_f32 v25, v25, v151\n"
        "v_add_f32 v24, v24, v171\n"
        "v_add_f32 v25, v25, v152\n"
        "v_add_f32 v24, v24, v172\n"
        "v_add_f32 v25, v25, v153\n"
        "v_add_f32 v24, v24, v173\n"
        "v_add_f32 v25, v25, v154\n"
        "v_add_f32 v24, v24, v174\n"
        "v_add_f32 v25, v25, v155\n"
        "v_add_f32 v24, v24, v175\n"
        "v_add_f32 v25, v25, v156\n"
        "v_add_f32 v24, v24, v176\n"
        "v_add_f32 v25, v25, v157\n"
        "v_add_f32 v24, v24, v177\n"
        "v_add_f32 v25, v25, v158\n"
        "v_add_f32 v24, v24, v178\n"
        "v_add_f32 v25, v25, v159\n"
        "v_add_f32 v24, v24, v179\n"
        "v_add_f32 v25, v25, v160\n"
        "s_waitcnt vmcnt(12)\n"
        "s_barrier\n"
        "v_add_f32 v24, v24, v180\n"
        "v_add_f32 v25, v25, v161\n"
        "v_add_f32 v24, v24, v181\n"
        "v_max_f32_e64 v27, v24, v25\n"
        "buffer_store_dwordx4 v[192:195], v5, s[4:7], s18 offen offset:3072 sc1\n"
        "v_cmp_lt_f32 vcc, 0x53800000, v27\n"
        "s_nop 0\n"
        "s_cbranch_vccnz .Lks256\n"
        ".Lkj257:\n"
        "v_rcp_f32 v24, v24\n"
        "v_rcp_f32 v25, v25\n"
        "v_mul_f32_e64 v162, v24, v162\n"
        "buffer_store_dwordx4 v[192:195], v5, s[4:7], s19 offen offset:0 sc1\n"
        "v_mul_f32_e64 v163, v24, v163\n"
        "v_cvt_pk_bf16_f32 v122, v162, v163\n"
        "v_mul_f32 v164, v24, v164\n"
        "v_mul_f32 v165, v24, v165\n"
        "ds_write_b128 v9, v[114:117] offset:48\n"
        "v_cvt_pk_bf16_f32 v123, v164, v165\n"
        "v_mul_f32 v166, v24, v166\n"
        "v_mul_f32 v167, v24, v167\n"
        "v_cvt_pk_bf16_f32 v124, v166, v167\n"
        "ds_write_b128 v9, v[118:121] offset:64\n"
        "v_mul_f32 v168, v24, v168\n"
        "v_mul_f32 v169, v24, v169\n"
        "v_cvt_pk_bf16_f32 v125, v168, v169\n"
        "ds_write_b128 v9, v[122:125] offset:5376\n"
        "v_mul_f32 v170, v24, v170\n"
        "v_mul_f32 v171, v24, v171\n"
        "v_cvt_pk_bf16_f32 v126, v170, v171\n"
        "v_mul_f32 v172, v24, v172\n"
        "v_mul_f32 v173, v24, v173\n"
        "v_cvt_pk_bf16_f32 v127, v172, v173\n"
        "v_mul_f32 v174, v24, v174\n"
        "v_mul_f32 v175, v24, v175\n"
        "v_cvt_pk_bf16_f32 v128, v174, v175\n"
        "v_mul_f32 v176, v24, v176\n"
        "v_mul_f32 v177, v24, v177\n"
        "v_cvt_pk_bf16_f32 v129, v176, v177\n"
        "ds_write_b128 v9, v[126:129] offset:5392\n"
        "v_mul_f32 v178, v24, v178\n"
        "v_mul_f32 v179, v24, v179\n"
        "v_cvt_pk_bf16_f32 v130, v178, v179\n"
        "v_mul_f32 v180, v24, v180\n"
        "v_mul_f32 v181, v24, v181\n"
        "v_cvt_pk_bf16_f32 v131, v180, v181\n"
        "v_mul_f32 v142, v25, v142\n"
        "v_mul_f32 v143, v25, v143\n"
        "v_cvt_pk_bf16_f32 v132, v142, v143\n"
        "v_mul_f32 v144, v25, v144\n"
        "v_mul_f32 v145, v25, v145\n"
        "v_cvt_pk_bf16_f32 v133, v144, v145\n"
        "ds_write_b128 v9, v[130:133] offset:5408\n"
        "v_mul_f32 v146, v25, v146\n"
        "v_mul_f32 v147, v25, v147\n"
        "v_cvt_pk_bf16_f32 v134, v146, v147\n"
        "v_mul_f32 v148, v25, v148\n"
        "v_mul_f32 v149, v25, v149\n"
        "v_cvt_pk_bf16_f32 v135, v148, v149\n"
        "v_mul_f32 v150, v25, v150\n"
        "v_mul_f32 v151, v25, v151\n"
        "v_cvt_pk_bf16_f32 v136, v150, v151\n"
        "v_mul_f32 v152, v25, v152\n"
        "v_mul_f32 v153, v25, v153\n"
        "v_cvt_pk_bf16_f32 v137, v152, v153\n"
        "ds_write_b128 v9, v[134:137] offset:5424\n"
        "v_mul_f32 v154, v25, v154\n"
        "v_mul_f32 v155, v25, v155\n"
        "v_cvt_pk_bf16_f32 v138, v154, v155\n"
        "v_mul_f32 v156, v25, v156\n"
        "v_mul_f32 v157, v25, v157\n"
        "v_cvt_pk_bf16_f32 v139, v156, v157\n"
        "v_mul_f32 v158, v25, v158\n"
        "v_mul_f32 v159, v25, v159\n"
        "v_cvt_pk_bf16_f32 v140, v158, v159\n"
        "v_mul_f32 v160, v25, v160\n"
        "v_mul_f32 v161, v25, v161\n"
        "v_cvt_pk_bf16_f32 v141, v160, v161\n"
        "ds_write_b128 v9, v[138:141] offset:5440\n"
        "s_waitcnt vmcnt(7) lgkmcnt(0)\n"
        "s_barrier\n"
        "ds_read_b64_tr_b16 v[82:83], v11 offset:0\n"
        "s_add_u32 m0, s0, 30720\n"
        "ds_read_b64_tr_b16 v[84:85], v11 offset:176\n"
        "ds_read_b64_tr_b16 v[32:33], v10 offset:0\n"
        "buffer_load_dwordx4 v251, s[48:51], s56 offen lds\n"
        "ds_read_b64_tr_b16 v[34:35], v10 offset:336\n"
        "ds_read_b64_tr_b16 v[36:37], v10 offset:32\n"
        "s_add_u32 m0, s14, 30720\n"
        "ds_read_b64_tr_b16 v[38:39], v10 offset:368\n"
        "ds_read_b64_tr_b16 v[40:41], v10 offset:64\n"
        "buffer_load_dwordx4 v248, s[48:51], s56 offen lds\n"
        "ds_read_b64_tr_b16 v[42:43], v10 offset:400\n"
        "ds_read_b64_tr_b16 v[86:87], v12 offset:0\n"
        "ds_read_b64_tr_b16 v[88:89], v12 offset:176\n"
        "buffer_load_dwordx4 v249, s[48:51], s56 offen offset:1024 lds\n"
        "ds_read_b64_tr_b16 v[90:91], v11 offset:16384\n"
        "ds_read_b64_tr_b16 v[92:93], v11 offset:16560\n"
        "s_add_u32 m0, s14, 40960\n"
        "ds_read_b64_tr_b16 v[44:45], v10 offset:16384\n"
        "ds_read_b64_tr_b16 v[46:47], v10 offset:16720\n"
        "buffer_load_dwordx4 v248, s[48:51], s57 offen lds\n"
        "s_waitcnt lgkmcnt(10)\n"
        "v_mfma_f32_16x16x32_bf16 v[114:117], v[32:35], v[82:85], 0\n"
        "ds_read_b64_tr_b16 v[48:49], v10 offset:16416\n"
        "ds_read_b64_tr_b16 v[50:51], v10 offset:16752\n"
        "buffer_load_dwordx4 v249, s[48:51], s57 offen offset:1024 lds\n"
        "s_waitcnt lgkmcnt(10)\n"
        "v_mfma_f32_16x16x32_bf16 v[118:121], v[36:39], v[82:85], 0\n"
        "ds_read_b64_tr_b16 v[52:53], v10 offset:16448\n"
        "s_add_u32 m0, s15, 51200\n"
        "ds_read_b64_tr_b16 v[54:55], v10 offset:16784\n"
        "s_waitcnt lgkmcnt(10)\n"
        "v_mfma_f32_16x16x32_bf16 v[122:125], v[40:43], v[82:85], 0\n"
        "buffer_load_dwordx4 v250, s[48:51], s58 offen lds\n"
        "ds_read_b64_tr_b16 v[94:95], v12 offset:16384\n"
        "ds_read_b64_tr_b16 v[96:97], v12 offset:16560\n"
        "s_waitcnt lgkmcnt(10)\n"
        "v_mfma_f32_16x16x32_bf16 v[126:129], v[32:35], v[86:89], 0\n"
        "s_add_u32 m0, s14, 51200\n"
        "ds_read_b64_tr_b16 v[98:99], v11 offset:32768\n"
        "ds_read_b64_tr_b16 v[100:101], v11 offset:32944\n"
        "buffer_load_dwordx4 v248, s[48:51], s58 offen lds\n"
        "v_mfma_f32_16x16x32_bf16 v[130:133], v[36:39], v[86:89], 0\n"
        "ds_read_b64_tr_b16 v[56:57], v10 offset:32768\n"
        "buffer_load_dwordx4 v249, s[48:51], s58 offen offset:1024 lds\n"
        "ds_read_b64_tr_b16 v[58:59], v10 offset:33104\n"
        "v_mfma_f32_16x16x32_bf16 v[134:137], v[40:43], v[86:89], 0\n"
        "ds_read_b64_tr_b16 v[60:61], v10 offset:32800\n"
        "ds_read_b64_tr_b16 v[62:63], v10 offset:33136\n"
        "s_waitcnt lgkmcnt(12)\n"
        "v_mfma_f32_16x16x32_bf16 v[114:117], v[44:47], v[90:93], v[114:117]\n"
        "ds_read_b64_tr_b16 v[64:65], v10 offset:32832\n"
        "s_waitcnt lgkmcnt(11)\n"
        "v_mfma_f32_16x16x32_bf16 v[118:121], v[48:51], v[90:93], v[118:121]\n"
        "ds_read_b64_tr_b16 v[66:67], v10 offset:33168\n"
        "ds_read_b64_tr_b16 v[102:103], v12 offset:32768\n"
        "s_waitcnt lgkmcnt(11)\n"
        "v_mfma_f32_16x16x32_bf16 v[122:125], v[52:55], v[90:93], v[122:125]\n"
        "ds_read_b64_tr_b16 v[104:105], v12 offset:32944\n"
        "ds_read_b64_tr_b16 v[106:107], v11 offset:49152\n"
        "s_waitcnt lgkmcnt(11)\n"
        "v_mfma_f32_16x16x32_bf16 v[126:129], v[44:47], v[94:97], v[126:129]\n"
        "ds_read_b64_tr_b16 v[108:109], v11 offset:49328\n"
        "ds_read_b64_tr_b16 v[68:69], v10 offset:49152\n"
        "v_mfma_f32_16x16x32_bf16 v[130:133], v[48:51], v[94:97], v[130:133]\n"
        "ds_read_b64_tr_b16 v[70:71], v10 offset:49488\n"
        "ds_read_b64_tr_b16 v[72:73], v10 offset:49184\n"
        "v_mfma_f32_16x16x32_bf16 v[134:137], v[52:55], v[94:97], v[134:137]\n"
        "ds_read_b64_tr_b16 v[74:75], v10 offset:49520\n"
        "ds_read_b64_tr_b16 v[76:77], v10 offset:49216\n"
        "s_waitcnt lgkmcnt(13)\n"
        "v_mfma_f32_16x16x32_bf16 v[114:117], v[56:59], v[98:101], v[114:117]\n"
        "ds_read_b64_tr_b16 v[78:79], v10 offset:49552\n"
        "ds_read_b64_tr_b16 v[110:111], v12 offset:49152\n"
        "s_waitcnt lgkmcnt(13)\n"
        "v_mfma_f32_16x16x32_bf16 v[118:121], v[60:63], v[98:101], v[118:121]\n"
        "ds_read_b64_tr_b16 v[112:113], v12 offset:49328\n"
        "s_waitcnt lgkmcnt(12)\n"
        "v_mfma_f32_16x16x32_bf16 v[122:125], v[64:67], v[98:101], v[122:125]\n"
        "s_waitcnt lgkmcnt(10)\n"
        "v_mfma_f32_16x16x32_bf16 v[126:129], v[56:59], v[102:105], v[126:129]\n"
        "v_mfma_f32_16x16x32_bf16 v[130:133], v[60:63], v[102:105], v[130:133]\n"
        "v_mfma_f32_16x16x32_bf16 v[134:137], v[64:67], v[102:105], v[134:137]\n"
        "s_waitcnt lgkmcnt(6)\n"
        "v_mfma_f32_16x16x32_bf16 v[114:117], v[68:71], v[106:109], v[114:117]\n"
        "s_waitcnt lgkmcnt(4)\n"
        "v_mfma_f32_16x16x32_bf16 v[118:121], v[72:75], v[106:109], v[118:121]\n"
        "s_waitcnt lgkmcnt(2)\n"
        "v_mfma_f32_16x16x32_bf16 v[122:125], v[76:79], v[106:109], v[122:125]\n"
        "s_waitcnt lgkmcnt(0)\n"
        "v_mfma_f32_16x16x32_bf16 v[126:129], v[68:71], v[110:113], v[126:129]\n"
        "v_mfma_f32_16x16x32_bf16 v[130:133], v[72:75], v[110:113], v[130:133]\n"
        "v_mfma_f32_16x16x32_bf16 v[134:137], v[76:79], v[110:113], v[134:137]\n"
        "v_cvt_pk_bf16_f32 v138, v114, v115\n"
        "v_cvt_pk_bf16_f32 v139, v116, v117\n"
        "v_cvt_pk_bf16_f32 v140, v118, v119\n"
        "v_cvt_pk_bf16_f32 v141, v120, v121\n"
        "v_cvt_pk_bf16_f32 v146, v122, v123\n"
        "v_cvt_pk_bf16_f32 v147, v124, v125\n"
        "buffer_store_dwordx4 v[138:141], v5, s[4:7], s8 offen offset:0 sc1\n"
        "v_cvt_pk_bf16_f32 v142, v126, v127\n"
        "v_cvt_pk_bf16_f32 v143, v128, v129\n"
        "v_cvt_pk_bf16_f32 v144, v130, v131\n"
        "v_cvt_pk_bf16_f32 v145, v132, v133\n"
        "v_cvt_pk_bf16_f32 v148, v134, v135\n"
        "v_cvt_pk_bf16_f32 v149, v136, v137\n"
        "s_mov_b32 exec_lo, 0xff00ff\n"
        "s_mov_b32 exec_hi, 0xff00ff\n"
        "buffer_store_dwordx4 v[142:145], v13, s[4:7], s8 offen offset:1024 sc1\n"
        "s_mov_b32 exec_lo, -1\n"
        "s_mov_b32 exec_hi, 0\n"
        "buffer_store_dwordx4 v[146:149], v5, s[4:7], s8 offen offset:1536 sc1\n"
        "s_mov_b32 exec_hi, -1\n"
        ".Ls1:\n"
        "s_mul_i32 s62, s35, 2816\n"
        "s_and_b32 s38, s35, 1\n"
        "s_lshl3_add_u32 s62, s38, s62\n"
        "s_add_u32 s62, s62, 152320\n"
        "s_xor_b32 s38, s62, 8\n"
        "s_lshl_b32 s36, s35, 8\n"
        "s_add_u32 s36, s36, s38\n"
        "s_mul_i32 s37, s35, 96\n"
        "s_add_u32 s37, s37, s38\n"
        "s_add_u32 s37, s37, 1024\n"
        "v_and_b32 v40, 0xf0, v5\n"
        "v_and_b32 v41, 0x70, v5\n"
        "v_lshrrev_b32 v42, 3, v0\n"
        "v_mul_u32_u24 v42, 96, v42\n"
        "v_add_u32_e64 v16, s62, v5\n"
        "v_add_u32 v17, 1152, v16\n"
        "v_add3_u32 v42, v41, v42, s62\n"
        "v_add_u32 v18, 141376, v197\n"
        "v_mov_b32_e64 v19, v18\n"
        "s_mov_b32 exec_lo, 0xffff\n"
        "s_mov_b32 exec_hi, 0x0\n"
        "v_add_u32 v17, s36, v40\n"
        "s_mov_b32 exec_lo, 0x1f1f1f1f\n"
        "v_add_u32 v18, 1024, v42\n"
        "v_add_u32 v19, 2336, v42\n"
        "s_mov_b32 exec_lo, 0x1f\n"
        "v_add_u32 v19, s37, v41\n"
        "s_mov_b64 exec, -1\n"
        "v_add_u32 v40, 1408, v40\n"
        "s_add_u32 s0, s62, 768\n"
        "s_add_u32 s62, s62, 1312\n"
        "s_mov_b32 exec_hi, 0xffff\n"
        "s_mov_b32 exec_lo, 0\n"
        "v_add_u32 v18, s0, v40\n"
        "s_mov_b32 exec_hi, 0x1f\n"
        "v_add_u32_e64 v19, s62, v40\n"
        "s_mov_b32 exec_hi, 0xffff0000\n"
        "v_add_u32_e64 v18, s36, v40\n"
        "s_mov_b32 exec_hi, 0x1f0000\n"
        "v_add_u32_e64 v19, s37, v40\n"
        "s_mov_b64 exec, -1\n"
        "s_waitcnt vmcnt(3)\n"
        "ds_write_b128 v15, v[182:185] offset:0\n"
        "ds_write_b128 v15, v[186:189] offset:1024\n"
        "ds_write_b64 v196, v[6:7]\n"
        "s_barrier\n"
        "s_bitcmp1_b32 s35, 0\n"
        "s_cbranch_scc1 .Lm258\n"
        "s_add_u32 m0, s15, 61440\n"
        "s_nop 0\n"
        "buffer_load_dwordx4 v250, s[48:51], s59 offen lds\n"
        ".Lm258:\n"
        "s_lshl_b32 s64, s17, 7\n"
        "s_add_u32 s40, s10, s64\n"
        "s_addc_u32 s41, s11, 0\n"
        "s_and_b32 s41, s41, 0xffff\n"
        "s_sub_u32 s42, 819200, s64\n"
        "s_mov_b32 s43, 0x20000\n"
        "v_lshrrev_b32 v13, 3, v0\n"
        "v_and_b32_e64 v14, 2, v13\n"
        "s_add_u32 m0, s14, 61440\n"
        "s_nop 0\n"
        "buffer_load_dwordx4 v248, s[48:51], s59 offen lds\n"
        "v_lshlrev_b32 v14, 1, v14\n"
        "v_and_b32 v12, 7, v0\n"
        "v_xor_b32 v14, v14, v12\n"
        "v_lshlrev_b32_e64 v14, 4, v14\n"
        "buffer_load_dwordx4 v249, s[48:51], s59 offen offset:1024 lds\n"
        "s_movk_i32 s3, 1280\n"
        "v_mad_u32_u24 v8, v13, s3, v14\n"
        "v_and_b32 v9, 3, v13\n"
        "v_mul_u32_u24_e64 v9, 20, v9\n"
        "s_bitcmp0_b32 s35, 0\n"
        "s_cbranch_scc1 .Lm259\n"
        "s_add_u32 m0, s15, 0\n"
        "s_nop 0\n"
        "buffer_load_dwordx4 v250, s[48:51], s60 offen lds\n"
        ".Lm259:\n"
        "v_lshrrev_b32_e64 v10, 5, v0\n"
        "v_lshl_add_u32 v9, v10, 1, v9\n"
        "v_mad_u32_u24 v9, v9, s3, v14\n"
        "s_mul_i32 s12, s35, 10240\n"
        "s_add_u32 m0, s14, 0\n"
        "s_nop 0\n"
        "buffer_load_dwordx4 v248, s[48:51], s60 offen lds\n"
        "s_add_u32 s3, s35, s17\n"
        "s_and_b32 s3, s3, 3\n"
        "s_xor_b32 s63, s29, 0\n"
        "buffer_load_dwordx4 v249, s[48:51], s60 offen offset:1024 lds\n"
        "s_and_b32 s63, s63, 1\n"
        "s_lshl2_add_u32 s3, s63, s3\n"
        "s_mul_i32 s65, s3, 102400\n"
        "s_add_u32 s3, s35, s17\n"
        "s_bitcmp1_b32 s35, 0\n"
        "s_cbranch_scc1 .Lm260\n"
        "s_add_u32 m0, s15, 71680\n"
        "s_nop 0\n"
        "buffer_load_dwordx4 v250, s[48:51], s61 offen lds\n"
        ".Lm260:\n"
        "s_and_b32 s3, s3, 3\n"
        "s_xor_b32 s63, s29, 1\n"
        "s_and_b32 s63, s63, 1\n"
        "s_lshl2_add_u32 s3, s63, s3\n"
        "s_add_u32 m0, s14, 71680\n"
        "s_nop 0\n"
        "buffer_load_dwordx4 v248, s[48:51], s61 offen lds\n"
        "s_mul_i32 s66, s3, 102400\n"
        "s_sub_u32 s3, s9, 1507328\n"
        "v_lshl_add_u32 v204, v0, 7, s3\n"
        "s_mov_b64 s[22:23], 0xffff\n"
        "buffer_load_dwordx4 v249, s[48:51], s61 offen offset:1024 lds\n"
        "v_cndmask_b32 v20, v20, v204, s[22:23]\n"
        "s_mov_b32 s20, 0x40000\n"
        "s_mov_b32 s21, 0x7f817f81\n"
        "s_add_u32 m0, s12, 81920\n"
        "s_add_u32 s63, s65, 0\n"
        "buffer_load_dwordx4 v8, s[40:43], s63 offen lds\n"
        "v_lshrrev_b32 v252, 3, v0\n"
        "v_and_b32_e64 v254, 7, v0\n"
        "s_lshl_b32 s13, s16, 2\n"
        "s_add_u32 s13, s13, s35\n"
        "s_mul_i32 s14, s13, 40960\n"
        "s_add_u32 s14, s14, 2097152\n"
        "s_xor_b32 s39, s29, 0\n"
        "s_and_b32 s39, s39, 1\n"
        "s_add_u32 s3, s17, 0\n"
        "s_and_b32 s3, s3, 3\n"
        "s_lshl2_add_u32 s3, s39, s3\n"
        "s_mul_i32 s3, s3, 5120\n"
        "s_add_u32 s52, s3, s14\n"
        "s_add_u32 s3, s17, 1\n"
        "s_and_b32 s3, s3, 3\n"
        "s_lshl2_add_u32 s3, s39, s3\n"
        "s_mul_i32 s3, s3, 5120\n"
        "s_add_u32 s53, s3, s14\n"
        "s_add_u32 m0, s12, 82944\n"
        "s_add_u32 s63, s65, 10240\n"
        "buffer_load_dwordx4 v8, s[40:43], s63 offen lds\n"
        "s_add_u32 s3, s17, 2\n"
        "s_and_b32 s3, s3, 3\n"
        "s_lshl2_add_u32 s3, s39, s3\n"
        "s_mul_i32 s3, s3, 5120\n"
        "s_add_u32 s54, s3, s14\n"
        "s_add_u32 s3, s17, 3\n"
        "s_and_b32 s3, s3, 3\n"
        "s_lshl2_add_u32 s3, s39, s3\n"
        "s_mul_i32 s3, s3, 5120\n"
        "s_add_u32 s55, s3, s14\n"
        "s_xor_b32 s39, s29, 1\n"
        "s_and_b32 s39, s39, 1\n"
        "s_add_u32 s3, s17, 4\n"
        "s_and_b32 s3, s3, 3\n"
        "s_lshl2_add_u32 s3, s39, s3\n"
        "s_mul_i32 s3, s3, 5120\n"
        "s_add_u32 s56, s3, s14\n"
        "s_add_u32 m0, s12, 83968\n"
        "s_add_u32 s63, s65, 25600\n"
        "buffer_load_dwordx4 v8, s[40:43], s63 offen lds\n"
        "s_add_u32 s3, s17, 5\n"
        "s_and_b32 s3, s3, 3\n"
        "s_lshl2_add_u32 s3, s39, s3\n"
        "s_mul_i32 s3, s3, 5120\n"
        "s_add_u32 s57, s3, s14\n"
        "s_add_u32 s3, s17, 6\n"
        "s_and_b32 s3, s3, 3\n"
        "s_lshl2_add_u32 s3, s39, s3\n"
        "s_mul_i32 s3, s3, 5120\n"
        "s_add_u32 s58, s3, s14\n"
        "s_add_u32 s3, s17, 7\n"
        "s_and_b32 s3, s3, 3\n"
        "s_lshl2_add_u32 s3, s39, s3\n"
        "s_mul_i32 s3, s3, 5120\n"
        "s_add_u32 s59, s3, s14\n"
        "s_mul_i32 s3, s13, 81920\n"
        "s_add_u32 s3, s64, s3\n"
        "s_add_u32 s44, s24, s3\n"
        "s_addc_u32 s45, s25, 0\n"
        "s_and_b32 s45, s45, 0xffff\n"
        "s_sub_u32 s46, 5242880, s3\n"
        "s_mov_b32 s47, 0x20000\n"
        "s_add_u32 m0, s12, 84992\n"
        "s_add_u32 s63, s65, 35840\n"
        "buffer_load_dwordx4 v8, s[40:43], s63 offen lds\n"
        "s_movk_i32 s3, 2560\n"
        "v_lshlrev_b32_e64 v254, 4, v254\n"
        "v_mad_u32_u24 v12, v252, s3, v254\n"
        "s_mul_i32 s3, s35, 4608\n"
        "s_add_u32 s3, s3, 51200\n"
        "v_mul_u32_u24 v252, 144, v252\n"
        "v_add3_u32 v11, v252, v254, s3\n"
        "v_and_b32_e64 v252, 31, v0\n"
        "v_mul_u32_u24 v252, 144, v252\n"
        "v_lshrrev_b32 v253, 5, v0\n"
        "v_lshlrev_b32 v254, 3, v253\n"
        "v_add3_u32 v10, v252, v254, s3\n"
        "v_bfe_u32 v252, v0, 2, 2\n"
        "v_lshl_add_u32 v253, v253, 2, v252\n"
        "v_and_b32 v252, 2, v252\n"
        "v_lshlrev_b32 v252, 5, v252\n"
        "v_lshl_add_u32 v252, v253, 7, v252\n"
        "s_add_u32 m0, s12, 86016\n"
        "s_add_u32 s63, s65, 51200\n"
        "buffer_load_dwordx4 v8, s[40:43], s63 offen lds\n"
        "v_bfe_u32 v253, v0, 4, 1\n"
        "v_lshl_add_u32 v252, v253, 5, v252\n"
        "v_and_b32_e64 v253, 3, v0\n"
        "v_lshl_add_u32 v252, v253, 3, v252\n"
        "v_xor_b32_e64 v253, 0, v252\n"
        "v_add_u32 v24, 81920, v253\n"
        "v_add_u32 v26, 10240, v253\n"
        "v_xor_b32_e64 v253, 64, v252\n"
        "v_add_u32 v25, 81920, v253\n"
        "v_add_u32 v27, 10240, v253\n"
        "v_add_u32 v13, 1024, v5\n"
        "s_lshl1_add_u32 s3, s13, s39\n"
        "s_lshl_b32 s3, s3, 9\n"
        "s_add_u32 s3, s3, 196608\n"
        "v_and_b32_e64 v252, 3, v0\n"
        "v_lshl_add_u32 v14, v252, 7, s3\n"
        "v_xor_b32 v21, 512, v14\n"
        "s_waitcnt vmcnt(5) lgkmcnt(0)\n"
        "s_barrier\n"
        "s_add_u32 m0, s12, 87040\n"
        "s_add_u32 s63, s65, 61440\n"
        "buffer_load_dwordx4 v8, s[40:43], s63 offen lds\n"
        "s_add_u32 m0, s12, 88064\n"
        "s_add_u32 s63, s65, 76800\n"
        "buffer_load_dwordx4 v8, s[40:43], s63 offen lds\n"
        "s_add_u32 m0, s12, 89088\n"
        "s_add_u32 s63, s65, 87040\n"
        "buffer_load_dwordx4 v8, s[40:43], s63 offen lds\n"
        "buffer_load_dwordx4 v[176:179], v20, s[4:7], 0 offen sc1\n"
        "s_add_u32 m0, s12, 90112\n"
        "s_add_u32 s63, s65, 20480\n"
        "buffer_load_dwordx4 v9, s[40:43], s63 offen lds\n"
        "s_add_u32 m0, s12, 91136\n"
        "s_add_u32 s63, s65, 21760\n"
        "buffer_load_dwordx4 v9, s[40:43], s63 offen lds\n"
        "s_add_u32 m0, s12, 10240\n"
        "s_add_u32 s63, s66, 0\n"
        "buffer_load_dwordx4 v8, s[40:43], s63 offen lds\n"
        "s_add_u32 m0, s12, 11264\n"
        "s_add_u32 s63, s66, 10240\n"
        "buffer_load_dwordx4 v8, s[40:43], s63 offen lds\n"
        "s_add_u32 m0, s12, 12288\n"
        "s_add_u32 s63, s66, 25600\n"
        "buffer_load_dwordx4 v8, s[40:43], s63 offen lds\n"
        "s_add_u32 m0, s12, 13312\n"
        "s_add_u32 s63, s66, 35840\n"
        "buffer_load_dwordx4 v8, s[40:43], s63 offen lds\n"
        "s_add_u32 m0, s12, 14336\n"
        "s_add_u32 s63, s66, 51200\n"
        "buffer_load_dwordx4 v8, s[40:43], s63 offen lds\n"
        "s_add_u32 m0, s12, 15360\n"
        "s_add_u32 s63, s66, 61440\n"
        "buffer_load_dwordx4 v8, s[40:43], s63 offen lds\n"
        "s_waitcnt vmcnt(8)\n"
        "v_cmp_ne_u64_e64 vcc, v[176:177], v[6:7]\n"
        "s_nop 0\n"
        "s_cbranch_vccnz .Ll261\n"
        ".Lj262:\n"
        "s_mov_b64 exec, 0xffff\n"
        ".Lt265:\n"
        "s_sleep 1\n"
        "s_memrealtime s[60:61]\n"
        "s_waitcnt lgkmcnt(0)\n"
        "v_sub_u32_e64 v179, s60, v178\n"
        "v_cmp_gt_u32 vcc, 411, v179\n"
        "s_nop 0\n"
        "s_cbranch_vccnz .Lt265\n"
        "s_mov_b64 exec, -1\n"
        ".Lr266:\n"
        "buffer_load_dwordx4 v[32:35], v5, s[4:7], s9 offen offset:0 sc1\n"
        "buffer_load_dwordx4 v[36:39], v5, s[4:7], s9 offen offset:1024 sc1\n"
        "s_sub_i32 s20, s20, 1\n"
        "s_cmp_lt_i32 s20, 0\n"
        "s_waitcnt vmcnt(0)\n"
        "s_cbranch_scc1 .Lo267\n"
        "v_cmp_eq_u32 s[36:37], s21, v32\n"
        "v_cmp_eq_u32 s[38:39], s21, v33\n"
        "v_cmp_eq_u32 s[48:49], s21, v34\n"
        "v_cmp_eq_u32 s[50:51], s21, v35\n"
        "s_or_b64 s[36:37], s[36:37], s[38:39]\n"
        "s_or_b64 s[22:23], s[48:49], s[50:51]\n"
        "v_cmp_eq_u32 s[38:39], s21, v36\n"
        "v_cmp_eq_u32 s[48:49], s21, v37\n"
        "v_cmp_eq_u32 s[50:51], s21, v38\n"
        "v_cmp_eq_u32 vcc, s21, v39\n"
        "s_or_b64 s[36:37], s[36:37], s[22:23]\n"
        "s_or_b64 s[38:39], s[38:39], s[48:49]\n"
        "s_or_b64 s[36:37], s[36:37], s[50:51]\n"
        "s_or_b64 s[36:37], s[36:37], s[38:39]\n"
        "s_or_b64 vcc, vcc, s[36:37]\n"
        "s_cbranch_vccnz .Lr266\n"
        ".Lo267:\n"
        "ds_write_b64 v16, v[32:33]\n"
        "ds_write_b64 v17, v[34:35]\n"
        "ds_write_b64 v18, v[36:37]\n"
        "ds_write_b64 v19, v[38:39]\n"
        "v_mov_b32_e64 v3, 5\n"
        "ds_write_b32 v2, v3 offset:32\n"
        ".Lw268:\n"
        "ds_read_b32 v4, v1 offset:32\n"
        "s_waitcnt lgkmcnt(0)\n"
        "v_cmp_gt_u32 vcc, 5, v4\n"
        "s_nop 0\n"
        "s_cbranch_vccz .Ld269\n"
        "s_sleep 1\n"
        "s_sub_i32 s68, s68, 1\n"
        "s_cmp_gt_i32 s68, 0\n"
        "s_cbranch_scc1 .Lw268\n"
        ".Ld269:\n"
        "s_add_u32 m0, s12, 16384\n"
        "s_add_u32 s63, s66, 76800\n"
        "buffer_load_dwordx4 v8, s[40:43], s63 offen lds\n"
        "s_add_u32 m0, s12, 17408\n"
        "s_add_u32 s63, s66, 87040\n"
        "buffer_load_dwordx4 v8, s[40:43], s63 offen lds\n"
        "s_add_u32 m0, s12, 18432\n"
        "s_add_u32 s63, s66, 20480\n"
        "buffer_load_dwordx4 v9, s[40:43], s63 offen lds\n"
        "s_add_u32 m0, s12, 19456\n"
        "s_add_u32 s63, s66, 21760\n"
        "buffer_load_dwordx4 v9, s[40:43], s63 offen lds\n"
        "s_waitcnt vmcnt(0)\n"
        ".La3:\n"
        "v_mov_b32 v3, 6\n"
        "ds_write_b32 v2, v3 offset:32\n"
        ".Lw270:\n"
        "ds_read_b32 v4, v1 offset:32\n"
        "s_waitcnt lgkmcnt(0)\n"
        "v_cmp_gt_u32 vcc, 6, v4\n"
        "s_nop 0\n"
        "s_cbranch_vccz .Ld271\n"
        "s_sleep 1\n"
        "s_sub_i32 s68, s68, 1\n"
        "s_cmp_gt_i32 s68, 0\n"
        "s_cbranch_scc1 .Lw270\n"
        ".Ld271:\n"
        "s_mov_b32 s15, 0x4000\n"
        "s_mov_b32 s20, 0x40000\n"
        "s_mov_b32 s21, 0x7f817f81\n"
        ".Lw273:\n"
        "ds_read_b32 v4, v1 offset:16\n"
        "s_waitcnt lgkmcnt(0)\n"
        "v_cmp_gt_u32 vcc, 1, v4\n"
        "s_nop 0\n"
        "s_cbranch_vccz .Ld274\n"
        "s_sleep 1\n"
        "s_sub_i32 s68, s68, 1\n"
        "s_cmp_gt_i32 s68, 0\n"
        "s_cbranch_scc1 .Lw273\n"
        ".Ld274:\n"
        "s_sleep 9\n"
        "s_mov_b32 s3, 0x40000\n"
        ".Lp275:\n"
        "buffer_load_dwordx2 v[28:29], v21, s[4:7], 0 offen sc1\n"
        "s_waitcnt vmcnt(0)\n"
        "v_cmp_ne_u64_e64 vcc, v[28:29], v[6:7]\n"
        "s_nop 0\n"
        "s_cbranch_vccz .Ld276\n"
        "s_sleep 1\n"
        "s_sub_u32 s3, s3, 1\n"
        "s_cmp_eq_u32 s3, 0\n"
        "s_cbranch_scc0 .Lp275\n"
        ".Ld276:\n"
        ".La0:\n"
        ".Lr272:\n"
        "buffer_load_dwordx4 v[32:35], v5, s[4:7], s52 offen offset:0 sc1\n"
        "buffer_load_dwordx4 v[36:39], v13, s[4:7], s52 offen offset:0 sc1\n"
        "buffer_load_dwordx4 v[52:55], v5, s[4:7], s53 offen offset:0 sc1\n"
        "buffer_load_dwordx4 v[56:59], v13, s[4:7], s53 offen offset:0 sc1\n"
        "buffer_load_dwordx4 v[72:75], v5, s[4:7], s54 offen offset:0 sc1\n"
        "buffer_load_dwordx4 v[76:79], v13, s[4:7], s54 offen offset:0 sc1\n"
        "buffer_load_dwordx4 v[92:95], v5, s[4:7], s55 offen offset:0 sc1\n"
        "buffer_load_dwordx4 v[96:99], v13, s[4:7], s55 offen offset:0 sc1\n"
        "ds_read_b64_tr_b16 v[224:225], v24 offset:0\n"
        "ds_read_b64_tr_b16 v[226:227], v24 offset:2048\n"
        "ds_read_b64_tr_b16 v[228:229], v25 offset:0\n"
        "ds_read_b64_tr_b16 v[230:231], v25 offset:2048\n"
        "ds_read_b64_tr_b16 v[232:233], v24 offset:1024\n"
        "ds_read_b64_tr_b16 v[234:235], v24 offset:3072\n"
        "ds_read_b64_tr_b16 v[236:237], v25 offset:1024\n"
        "ds_read_b64_tr_b16 v[238:239], v25 offset:3072\n"
        "ds_read_b64_tr_b16 v[240:241], v24 offset:10240\n"
        "ds_read_b64_tr_b16 v[242:243], v24 offset:12288\n"
        "ds_read_b64_tr_b16 v[244:245], v25 offset:10240\n"
        "ds_read_b64_tr_b16 v[246:247], v25 offset:12288\n"
        "s_waitcnt vmcnt(6)\n"
        "v_cmp_u_f32 vcc, v32, v36\n"
        "s_nop 0\n"
        "s_cbranch_vccnz .Lv277\n"
        ".Lb278:\n"
        "s_waitcnt lgkmcnt(10)\n"
        "v_mfma_f32_32x32x16_bf16 v[192:207], v[224:227], v[32:35], 0\n"
        "ds_read_b64_tr_b16 v[248:249], v24 offset:11264\n"
        "buffer_load_dwordx4 v[40:43], v13, s[4:7], s52 offen offset:1024 sc1\n"
        "ds_read_b64_tr_b16 v[250:251], v24 offset:13312\n"
        "s_waitcnt lgkmcnt(10)\n"
        "v_mfma_f32_32x32x16_bf16 v[208:223], v[228:231], v[32:35], 0\n"
        "ds_read_b64_tr_b16 v[224:225], v25 offset:11264\n"
        "ds_read_b64_tr_b16 v[226:227], v25 offset:13312\n"
        "s_waitcnt lgkmcnt(8)\n"
        "v_mfma_f32_32x32x16_bf16 v[192:207], v[232:235], v[36:39], v[192:207]\n"
        "ds_read_b64_tr_b16 v[228:229], v24 offset:20480\n"
        "buffer_load_dwordx4 v[44:47], v13, s[4:7], s52 offen offset:2048 sc1\n"
        "ds_read_b64_tr_b16 v[230:231], v24 offset:22528\n"
        "v_mfma_f32_32x32x16_bf16 v[208:223], v[236:239], v[36:39], v[208:223]\n"
        "ds_read_b64_tr_b16 v[232:233], v25 offset:20480\n"
        "s_waitcnt vmcnt(6)\n"
        "v_cmp_u_f32_e64 vcc, v52, v56\n"
        "ds_read_b64_tr_b16 v[234:235], v25 offset:22528\n"
        "s_cbranch_vccnz .Lv279\n"
        ".Lb280:\n"
        "s_waitcnt lgkmcnt(8)\n"
        "v_mfma_f32_32x32x16_bf16 v[192:207], v[240:243], v[52:55], v[192:207]\n"
        "ds_read_b64_tr_b16 v[236:237], v24 offset:21504\n"
        "buffer_load_dwordx4 v[60:63], v13, s[4:7], s53 offen offset:1024 sc1\n"
        "ds_read_b64_tr_b16 v[238:239], v24 offset:23552\n"
        "v_mfma_f32_32x32x16_bf16 v[208:223], v[244:247], v[52:55], v[208:223]\n"
        "ds_read_b64_tr_b16 v[240:241], v25 offset:21504\n"
        "ds_read_b64_tr_b16 v[242:243], v25 offset:23552\n"
        "s_waitcnt lgkmcnt(8)\n"
        "v_mfma_f32_32x32x16_bf16 v[192:207], v[248:251], v[56:59], v[192:207]\n"
        "ds_read_b64_tr_b16 v[244:245], v24 offset:30720\n"
        "buffer_load_dwordx2 v[28:29], v14, s[4:7], 0 offen sc1\n"
        "buffer_load_dwordx4 v[64:67], v13, s[4:7], s53 offen offset:2048 sc1\n"
        "ds_read_b64_tr_b16 v[246:247], v24 offset:32768\n"
        "v_mfma_f32_32x32x16_bf16 v[208:223], v[224:227], v[56:59], v[208:223]\n"
        "ds_read_b64_tr_b16 v[248:249], v25 offset:30720\n"
        "s_waitcnt vmcnt(7)\n"
        "v_cmp_u_f32_e64 vcc, v72, v76\n"
        "ds_read_b64_tr_b16 v[250:251], v25 offset:32768\n"
        "s_cbranch_vccnz .Lv281\n"
        ".Lb282:\n"
        "s_waitcnt lgkmcnt(8)\n"
        "v_mfma_f32_32x32x16_bf16 v[192:207], v[228:231], v[72:75], v[192:207]\n"
        "ds_read_b64_tr_b16 v[224:225], v24 offset:31744\n"
        "buffer_load_dwordx4 v[80:83], v13, s[4:7], s54 offen offset:1024 sc1\n"
        "ds_read_b64_tr_b16 v[226:227], v24 offset:33792\n"
        "v_mfma_f32_32x32x16_bf16 v[208:223], v[232:235], v[72:75], v[208:223]\n"
        "ds_read_b64_tr_b16 v[228:229], v25 offset:31744\n"
        "ds_read_b64_tr_b16 v[230:231], v25 offset:33792\n"
        "s_waitcnt lgkmcnt(8)\n"
        "v_mfma_f32_32x32x16_bf16 v[192:207], v[236:239], v[76:79], v[192:207]\n"
        "ds_read_b64_tr_b16 v[232:233], v24 offset:4096\n"
        "buffer_load_dwordx4 v[84:87], v13, s[4:7], s54 offen offset:2048 sc1\n"
        "ds_read_b64_tr_b16 v[234:235], v24 offset:6144\n"
        "v_mfma_f32_32x32x16_bf16 v[208:223], v[240:243], v[76:79], v[208:223]\n"
        "ds_read_b64_tr_b16 v[236:237], v25 offset:4096\n"
        "s_waitcnt vmcnt(7)\n"
        "v_cmp_u_f32_e64 vcc, v92, v96\n"
        "ds_read_b64_tr_b16 v[238:239], v25 offset:6144\n"
        "s_cbranch_vccnz .Lv283\n"
        ".Lb284:\n"
        "s_waitcnt lgkmcnt(8)\n"
        "v_mfma_f32_32x32x16_bf16 v[192:207], v[244:247], v[92:95], v[192:207]\n"
        "ds_read_b64_tr_b16 v[240:241], v24 offset:5120\n"
        "buffer_load_dwordx4 v[100:103], v13, s[4:7], s55 offen offset:1024 sc1\n"
        "ds_read_b64_tr_b16 v[242:243], v24 offset:7168\n"
        "v_mfma_f32_32x32x16_bf16 v[208:223], v[248:251], v[92:95], v[208:223]\n"
        "ds_read_b64_tr_b16 v[244:245], v25 offset:5120\n"
        "ds_read_b64_tr_b16 v[246:247], v25 offset:7168\n"
        "s_waitcnt lgkmcnt(8)\n"
        "v_mfma_f32_32x32x16_bf16 v[192:207], v[224:227], v[96:99], v[192:207]\n"
        "ds_read_b64_tr_b16 v[248:249], v24 offset:14336\n"
        "buffer_load_dwordx4 v[104:107], v13, s[4:7], s55 offen offset:2048 sc1\n"
        "ds_read_b64_tr_b16 v[250:251], v24 offset:16384\n"
        "v_mfma_f32_32x32x16_bf16 v[208:223], v[228:231], v[96:99], v[208:223]\n"
        "ds_read_b64_tr_b16 v[224:225], v25 offset:14336\n"
        "s_waitcnt vmcnt(7)\n"
        "v_cmp_u_f32_e64 vcc, v40, v44\n"
        "ds_read_b64_tr_b16 v[226:227], v25 offset:16384\n"
        "s_cbranch_vccnz .Lv285\n"
        ".Lb286:\n"
        "s_waitcnt lgkmcnt(8)\n"
        "v_mfma_f32_32x32x16_bf16 v[192:207], v[232:235], v[40:43], v[192:207]\n"
        "ds_read_b64_tr_b16 v[228:229], v24 offset:15360\n"
        "buffer_load_dwordx4 v[48:51], v13, s[4:7], s52 offen offset:3072 sc1\n"
        "ds_read_b64_tr_b16 v[230:231], v24 offset:17408\n"
        "v_mfma_f32_32x32x16_bf16 v[208:223], v[236:239], v[40:43], v[208:223]\n"
        "ds_read_b64_tr_b16 v[232:233], v25 offset:15360\n"
        "ds_read_b64_tr_b16 v[234:235], v25 offset:17408\n"
        "s_waitcnt lgkmcnt(8)\n"
        "v_mfma_f32_32x32x16_bf16 v[192:207], v[240:243], v[44:47], v[192:207]\n"
        "ds_read_b64_tr_b16 v[236:237], v24 offset:24576\n"
        "buffer_load_dwordx4 v[68:71], v13, s[4:7], s53 offen offset:3072 sc1\n"
        "ds_read_b64_tr_b16 v[238:239], v24 offset:26624\n"
        "v_mfma_f32_32x32x16_bf16 v[208:223], v[244:247], v[44:47], v[208:223]\n"
        "ds_read_b64_tr_b16 v[240:241], v25 offset:24576\n"
        "s_waitcnt vmcnt(6)\n"
        "v_cmp_u_f32_e64 vcc, v60, v64\n"
        "ds_read_b64_tr_b16 v[242:243], v25 offset:26624\n"
        "s_cbranch_vccnz .Lv287\n"
        ".Lb288:\n"
        "s_waitcnt lgkmcnt(8)\n"
        "v_mfma_f32_32x32x16_bf16 v[192:207], v[248:251], v[60:63], v[192:207]\n"
        "ds_read_b64_tr_b16 v[244:245], v24 offset:25600\n"
        "buffer_load_dwordx4 v[88:91], v13, s[4:7], s54 offen offset:3072 sc1\n"
        "ds_read_b64_tr_b16 v[246:247], v24 offset:27648\n"
        "v_mfma_f32_32x32x16_bf16 v[208:223], v[224:227], v[60:63], v[208:223]\n"
        "ds_read_b64_tr_b16 v[248:249], v25 offset:25600\n"
        "ds_read_b64_tr_b16 v[250:251], v25 offset:27648\n"
        "s_waitcnt lgkmcnt(8)\n"
        "v_mfma_f32_32x32x16_bf16 v[192:207], v[228:231], v[64:67], v[192:207]\n"
        "ds_read_b64_tr_b16 v[224:225], v24 offset:34816\n"
        "buffer_load_dwordx4 v[108:111], v13, s[4:7], s55 offen offset:3072 sc1\n"
        "ds_read_b64_tr_b16 v[226:227], v24 offset:36864\n"
        "v_mfma_f32_32x32x16_bf16 v[208:223], v[232:235], v[64:67], v[208:223]\n"
        "ds_read_b64_tr_b16 v[228:229], v25 offset:34816\n"
        "s_waitcnt vmcnt(6)\n"
        "v_cmp_u_f32_e64 vcc, v80, v84\n"
        "ds_read_b64_tr_b16 v[230:231], v25 offset:36864\n"
        "s_cbranch_vccnz .Lv289\n"
        ".Lb290:\n"
        "s_waitcnt lgkmcnt(8)\n"
        "v_mfma_f32_32x32x16_bf16 v[192:207], v[236:239], v[80:83], v[192:207]\n"
        "ds_read_b64_tr_b16 v[232:233], v24 offset:35840\n"
        "v_cmp_ne_u64_e64 vcc, v[28:29], v[6:7]\n"
        "s_nop 0\n"
        "s_cbranch_vccnz .Lq291\n"
        ".Lb292:\n"
        "buffer_load_dwordx4 v[112:115], v5, s[4:7], s56 offen offset:0 sc1\n"
        "ds_read_b64_tr_b16 v[234:235], v24 offset:37888\n"
        "v_mfma_f32_32x32x16_bf16 v[208:223], v[240:243], v[80:83], v[208:223]\n"
        "ds_read_b64_tr_b16 v[236:237], v25 offset:35840\n"
        "ds_read_b64_tr_b16 v[238:239], v25 offset:37888\n"
        "s_waitcnt lgkmcnt(8)\n"
        "v_mfma_f32_32x32x16_bf16 v[192:207], v[244:247], v[84:87], v[192:207]\n"
        "ds_read_b64_tr_b16 v[240:241], v24 offset:8192\n"
        "buffer_load_dwordx4 v[116:119], v13, s[4:7], s56 offen offset:0 sc1\n"
        "ds_read_b64_tr_b16 v[242:243], v24 offset:9216\n"
        "v_mfma_f32_32x32x16_bf16 v[208:223], v[248:251], v[84:87], v[208:223]\n"
        "ds_read_b64_tr_b16 v[244:245], v25 offset:8192\n"
        "s_waitcnt vmcnt(6)\n"
        "v_cmp_u_f32_e64 vcc, v100, v104\n"
        "ds_read_b64_tr_b16 v[246:247], v25 offset:9216\n"
        "s_cbranch_vccnz .Lv293\n"
        ".Lb294:\n"
        "s_waitcnt lgkmcnt(8)\n"
        "v_mfma_f32_32x32x16_bf16 v[192:207], v[224:227], v[100:103], v[192:207]\n"
        "ds_read_b64_tr_b16 v[248:249], v24 offset:18432\n"
        "buffer_load_dwordx4 v[132:135], v5, s[4:7], s57 offen offset:0 sc1\n"
        "ds_read_b64_tr_b16 v[250:251], v24 offset:19456\n"
        "v_mfma_f32_32x32x16_bf16 v[208:223], v[228:231], v[100:103], v[208:223]\n"
        "ds_read_b64_tr_b16 v[224:225], v25 offset:18432\n"
        "ds_read_b64_tr_b16 v[226:227], v25 offset:19456\n"
        "s_waitcnt lgkmcnt(8)\n"
        "v_mfma_f32_32x32x16_bf16 v[192:207], v[232:235], v[104:107], v[192:207]\n"
        "ds_read_b64_tr_b16 v[228:229], v24 offset:28672\n"
        "buffer_load_dwordx4 v[136:139], v13, s[4:7], s57 offen offset:0 sc1\n"
        "ds_read_b64_tr_b16 v[230:231], v24 offset:29696\n"
        "v_mfma_f32_32x32x16_bf16 v[208:223], v[236:239], v[104:107], v[208:223]\n"
        "ds_read_b64_tr_b16 v[232:233], v25 offset:28672\n"
        "s_waitcnt vmcnt(6)\n"
        "v_cmp_u_f32_e64 vcc, v48, v68\n"
        "ds_read_b64_tr_b16 v[234:235], v25 offset:29696\n"
        "s_cbranch_vccnz .Lv295\n"
        ".Lb296:\n"
        "s_waitcnt lgkmcnt(8)\n"
        "v_mfma_f32_32x32x16_bf16 v[192:207], v[240:243], v[48:51], v[192:207]\n"
        "ds_read_b64_tr_b16 v[236:237], v24 offset:38912\n"
        "buffer_load_dwordx4 v[152:155], v5, s[4:7], s58 offen offset:0 sc1\n"
        "ds_read_b64_tr_b16 v[238:239], v24 offset:39936\n"
        "v_mfma_f32_32x32x16_bf16 v[208:223], v[244:247], v[48:51], v[208:223]\n"
        "ds_read_b64_tr_b16 v[240:241], v25 offset:38912\n"
        "ds_read_b64_tr_b16 v[242:243], v25 offset:39936\n"
        "s_waitcnt lgkmcnt(8)\n"
        "v_mfma_f32_32x32x16_bf16 v[192:207], v[248:251], v[68:71], v[192:207]\n"
        "ds_read_b64_tr_b16 v[244:245], v26 offset:0\n"
        "buffer_load_dwordx4 v[156:159], v13, s[4:7], s58 offen offset:0 sc1\n"
        "ds_read_b64_tr_b16 v[246:247], v26 offset:2048\n"
        "v_mfma_f32_32x32x16_bf16 v[208:223], v[224:227], v[68:71], v[208:223]\n"
        "ds_read_b64_tr_b16 v[248:249], v27 offset:0\n"
        "s_waitcnt vmcnt(6)\n"
        "v_cmp_u_f32_e64 vcc, v88, v108\n"
        "ds_read_b64_tr_b16 v[250:251], v27 offset:2048\n"
        "s_cbranch_vccnz .Lv297\n"
        ".Lb298:\n"
        "s_waitcnt lgkmcnt(8)\n"
        "v_mfma_f32_32x32x16_bf16 v[192:207], v[228:231], v[88:91], v[192:207]\n"
        "ds_read_b64_tr_b16 v[224:225], v26 offset:1024\n"
        "buffer_load_dwordx4 v[172:175], v5, s[4:7], s59 offen offset:0 sc1\n"
        "ds_read_b64_tr_b16 v[226:227], v26 offset:3072\n"
        "v_mfma_f32_32x32x16_bf16 v[208:223], v[232:235], v[88:91], v[208:223]\n"
        "ds_read_b64_tr_b16 v[228:229], v27 offset:1024\n"
        "ds_read_b64_tr_b16 v[230:231], v27 offset:3072\n"
        "s_waitcnt lgkmcnt(8)\n"
        "v_mfma_f32_32x32x16_bf16 v[192:207], v[236:239], v[108:111], v[192:207]\n"
        "ds_read_b64_tr_b16 v[232:233], v26 offset:10240\n"
        "buffer_load_dwordx4 v[176:179], v13, s[4:7], s59 offen offset:0 sc1\n"
        "ds_read_b64_tr_b16 v[234:235], v26 offset:12288\n"
        "v_mfma_f32_32x32x16_bf16 v[208:223], v[240:243], v[108:111], v[208:223]\n"
        "ds_read_b64_tr_b16 v[236:237], v27 offset:10240\n"
        "s_waitcnt vmcnt(6)\n"
        "v_cmp_u_f32_e64 vcc, v112, v116\n"
        "ds_read_b64_tr_b16 v[238:239], v27 offset:12288\n"
        "s_cbranch_vccnz .Lv299\n"
        ".Lb300:\n"
        "s_waitcnt lgkmcnt(8)\n"
        "v_mfma_f32_32x32x16_bf16 v[192:207], v[244:247], v[112:115], v[192:207]\n"
        "ds_read_b64_tr_b16 v[240:241], v26 offset:11264\n"
        "buffer_load_dwordx4 v[120:123], v13, s[4:7], s56 offen offset:1024 sc1\n"
        "ds_read_b64_tr_b16 v[242:243], v26 offset:13312\n"
        "v_mfma_f32_32x32x16_bf16 v[208:223], v[248:251], v[112:115], v[208:223]\n"
        "ds_read_b64_tr_b16 v[244:245], v27 offset:11264\n"
        "ds_read_b64_tr_b16 v[246:247], v27 offset:13312\n"
        "s_waitcnt lgkmcnt(8)\n"
        "v_mfma_f32_32x32x16_bf16 v[192:207], v[224:227], v[116:119], v[192:207]\n"
        "ds_read_b64_tr_b16 v[248:249], v26 offset:20480\n"
        "buffer_load_dwordx4 v[124:127], v13, s[4:7], s56 offen offset:2048 sc1\n"
        "ds_read_b64_tr_b16 v[250:251], v26 offset:22528\n"
        "v_mfma_f32_32x32x16_bf16 v[208:223], v[228:231], v[116:119], v[208:223]\n"
        "ds_read_b64_tr_b16 v[224:225], v27 offset:20480\n"
        "s_waitcnt vmcnt(6)\n"
        "v_cmp_u_f32_e64 vcc, v132, v136\n"
        "ds_read_b64_tr_b16 v[226:227], v27 offset:22528\n"
        "s_cbranch_vccnz .Lv301\n"
        ".Lb302:\n"
        "s_waitcnt lgkmcnt(8)\n"
        "v_mfma_f32_32x32x16_bf16 v[192:207], v[232:235], v[132:135], v[192:207]\n"
        "ds_read_b64_tr_b16 v[228:229], v26 offset:21504\n"
        "buffer_load_dwordx4 v[140:143], v13, s[4:7], s57 offen offset:1024 sc1\n"
        "ds_read_b64_tr_b16 v[230:231], v26 offset:23552\n"
        "v_mfma_f32_32x32x16_bf16 v[208:223], v[236:239], v[132:135], v[208:223]\n"
        "ds_read_b64_tr_b16 v[232:233], v27 offset:21504\n"
        "ds_read_b64_tr_b16 v[234:235], v27 offset:23552\n"
        "s_waitcnt lgkmcnt(8)\n"
        "v_mfma_f32_32x32x16_bf16 v[192:207], v[240:243], v[136:139], v[192:207]\n"
        "ds_read_b64_tr_b16 v[236:237], v26 offset:30720\n"
        "buffer_load_dwordx4 v[144:147], v13, s[4:7], s57 offen offset:2048 sc1\n"
        "ds_read_b64_tr_b16 v[238:239], v26 offset:32768\n"
        "v_mfma_f32_32x32x16_bf16 v[208:223], v[244:247], v[136:139], v[208:223]\n"
        "ds_read_b64_tr_b16 v[240:241], v27 offset:30720\n"
        "s_waitcnt vmcnt(6)\n"
        "v_cmp_u_f32_e64 vcc, v152, v156\n"
        "ds_read_b64_tr_b16 v[242:243], v27 offset:32768\n"
        "s_cbranch_vccnz .Lv303\n"
        ".Lb304:\n"
        "s_waitcnt lgkmcnt(8)\n"
        "v_mfma_f32_32x32x16_bf16 v[192:207], v[248:251], v[152:155], v[192:207]\n"
        "ds_read_b64_tr_b16 v[244:245], v26 offset:31744\n"
        "buffer_load_dwordx4 v[160:163], v13, s[4:7], s58 offen offset:1024 sc1\n"
        "ds_read_b64_tr_b16 v[246:247], v26 offset:33792\n"
        "v_mfma_f32_32x32x16_bf16 v[208:223], v[224:227], v[152:155], v[208:223]\n"
        "ds_read_b64_tr_b16 v[248:249], v27 offset:31744\n"
        "ds_read_b64_tr_b16 v[250:251], v27 offset:33792\n"
        "s_waitcnt lgkmcnt(8)\n"
        "v_mfma_f32_32x32x16_bf16 v[192:207], v[228:231], v[156:159], v[192:207]\n"
        "ds_read_b64_tr_b16 v[224:225], v26 offset:4096\n"
        "buffer_load_dwordx4 v[164:167], v13, s[4:7], s58 offen offset:2048 sc1\n"
        "ds_read_b64_tr_b16 v[226:227], v26 offset:6144\n"
        "v_mfma_f32_32x32x16_bf16 v[208:223], v[232:235], v[156:159], v[208:223]\n"
        "ds_read_b64_tr_b16 v[228:229], v27 offset:4096\n"
        "s_waitcnt vmcnt(6)\n"
        "v_cmp_u_f32_e64 vcc, v172, v176\n"
        "ds_read_b64_tr_b16 v[230:231], v27 offset:6144\n"
        "s_cbranch_vccnz .Lv305\n"
        ".Lb306:\n"
        "s_waitcnt lgkmcnt(8)\n"
        "v_mfma_f32_32x32x16_bf16 v[192:207], v[236:239], v[172:175], v[192:207]\n"
        "ds_read_b64_tr_b16 v[232:233], v26 offset:5120\n"
        "buffer_load_dwordx4 v[180:183], v13, s[4:7], s59 offen offset:1024 sc1\n"
        "ds_read_b64_tr_b16 v[234:235], v26 offset:7168\n"
        "v_mfma_f32_32x32x16_bf16 v[208:223], v[240:243], v[172:175], v[208:223]\n"
        "ds_read_b64_tr_b16 v[236:237], v27 offset:5120\n"
        "ds_read_b64_tr_b16 v[238:239], v27 offset:7168\n"
        "s_waitcnt lgkmcnt(8)\n"
        "v_mfma_f32_32x32x16_bf16 v[192:207], v[244:247], v[176:179], v[192:207]\n"
        "ds_read_b64_tr_b16 v[240:241], v26 offset:14336\n"
        "buffer_load_dwordx4 v[184:187], v13, s[4:7], s59 offen offset:2048 sc1\n"
        "ds_read_b64_tr_b16 v[242:243], v26 offset:16384\n"
        "v_mfma_f32_32x32x16_bf16 v[208:223], v[248:251], v[176:179], v[208:223]\n"
        "ds_read_b64_tr_b16 v[244:245], v27 offset:14336\n"
        "s_waitcnt vmcnt(6)\n"
        "v_cmp_u_f32_e64 vcc, v120, v124\n"
        "ds_read_b64_tr_b16 v[246:247], v27 offset:16384\n"
        "s_cbranch_vccnz .Lv307\n"
        ".Lb308:\n"
        "s_waitcnt lgkmcnt(8)\n"
        "v_mfma_f32_32x32x16_bf16 v[192:207], v[224:227], v[120:123], v[192:207]\n"
        "ds_read_b64_tr_b16 v[248:249], v26 offset:15360\n"
        "buffer_load_dwordx4 v[128:131], v13, s[4:7], s56 offen offset:3072 sc1\n"
        "ds_read_b64_tr_b16 v[250:251], v26 offset:17408\n"
        "v_mfma_f32_32x32x16_bf16 v[208:223], v[228:231], v[120:123], v[208:223]\n"
        "ds_read_b64_tr_b16 v[224:225], v27 offset:15360\n"
        "ds_read_b64_tr_b16 v[226:227], v27 offset:17408\n"
        "s_waitcnt lgkmcnt(8)\n"
        "v_mfma_f32_32x32x16_bf16 v[192:207], v[232:235], v[124:127], v[192:207]\n"
        "ds_read_b64_tr_b16 v[228:229], v26 offset:24576\n"
        "buffer_load_dwordx4 v[148:151], v13, s[4:7], s57 offen offset:3072 sc1\n"
        "ds_read_b64_tr_b16 v[230:231], v26 offset:26624\n"
        "v_mfma_f32_32x32x16_bf16 v[208:223], v[236:239], v[124:127], v[208:223]\n"
        "ds_read_b64_tr_b16 v[232:233], v27 offset:24576\n"
        "s_waitcnt vmcnt(6)\n"
        "v_cmp_u_f32_e64 vcc, v140, v144\n"
        "ds_read_b64_tr_b16 v[234:235], v27 offset:26624\n"
        "s_cbranch_vccnz .Lv309\n"
        ".Lb310:\n"
        "s_waitcnt lgkmcnt(8)\n"
        "v_mfma_f32_32x32x16_bf16 v[192:207], v[240:243], v[140:143], v[192:207]\n"
        "ds_read_b64_tr_b16 v[236:237], v26 offset:25600\n"
        "buffer_load_dwordx4 v[168:171], v13, s[4:7], s58 offen offset:3072 sc1\n"
        "ds_read_b64_tr_b16 v[238:239], v26 offset:27648\n"
        "v_mfma_f32_32x32x16_bf16 v[208:223], v[244:247], v[140:143], v[208:223]\n"
        "ds_read_b64_tr_b16 v[240:241], v27 offset:25600\n"
        "ds_read_b64_tr_b16 v[242:243], v27 offset:27648\n"
        "s_waitcnt lgkmcnt(8)\n"
        "v_mfma_f32_32x32x16_bf16 v[192:207], v[248:251], v[144:147], v[192:207]\n"
        "ds_read_b64_tr_b16 v[244:245], v26 offset:34816\n"
        "buffer_load_dwordx4 v[188:191], v13, s[4:7], s59 offen offset:3072 sc1\n"
        "ds_read_b64_tr_b16 v[246:247], v26 offset:36864\n"
        "v_mfma_f32_32x32x16_bf16 v[208:223], v[224:227], v[144:147], v[208:223]\n"
        "ds_read_b64_tr_b16 v[248:249], v27 offset:34816\n"
        "s_waitcnt vmcnt(6)\n"
        "v_cmp_u_f32_e64 vcc, v160, v164\n"
        "ds_read_b64_tr_b16 v[250:251], v27 offset:36864\n"
        "s_cbranch_vccnz .Lv311\n"
        ".Lb312:\n"
        "s_waitcnt lgkmcnt(8)\n"
        "v_mfma_f32_32x32x16_bf16 v[192:207], v[228:231], v[160:163], v[192:207]\n"
        "ds_read_b64_tr_b16 v[224:225], v26 offset:35840\n"
        "ds_read_b64_tr_b16 v[226:227], v26 offset:37888\n"
        "v_mfma_f32_32x32x16_bf16 v[208:223], v[232:235], v[160:163], v[208:223]\n"
        "ds_read_b64_tr_b16 v[228:229], v27 offset:35840\n"
        "ds_read_b64_tr_b16 v[230:231], v27 offset:37888\n"
        "s_waitcnt lgkmcnt(8)\n"
        "v_mfma_f32_32x32x16_bf16 v[192:207], v[236:239], v[164:167], v[192:207]\n"
        "ds_read_b64_tr_b16 v[232:233], v26 offset:8192\n"
        "ds_read_b64_tr_b16 v[234:235], v26 offset:9216\n"
        "v_mfma_f32_32x32x16_bf16 v[208:223], v[240:243], v[164:167], v[208:223]\n"
        "ds_read_b64_tr_b16 v[236:237], v27 offset:8192\n"
        "s_waitcnt vmcnt(4)\n"
        "v_cmp_u_f32_e64 vcc, v180, v184\n"
        "ds_read_b64_tr_b16 v[238:239], v27 offset:9216\n"
        "s_cbranch_vccnz .Lv313\n"
        ".Lb314:\n"
        "s_waitcnt lgkmcnt(8)\n"
        "v_mfma_f32_32x32x16_bf16 v[192:207], v[244:247], v[180:183], v[192:207]\n"
        "ds_read_b64_tr_b16 v[240:241], v26 offset:18432\n"
        "ds_read_b64_tr_b16 v[242:243], v26 offset:19456\n"
        "v_mfma_f32_32x32x16_bf16 v[208:223], v[248:251], v[180:183], v[208:223]\n"
        "ds_read_b64_tr_b16 v[244:245], v27 offset:18432\n"
        "ds_read_b64_tr_b16 v[246:247], v27 offset:19456\n"
        "s_waitcnt lgkmcnt(8)\n"
        "v_mfma_f32_32x32x16_bf16 v[192:207], v[224:227], v[184:187], v[192:207]\n"
        "ds_read_b64_tr_b16 v[248:249], v26 offset:28672\n"
        "ds_read_b64_tr_b16 v[250:251], v26 offset:29696\n"
        "v_mfma_f32_32x32x16_bf16 v[208:223], v[228:231], v[184:187], v[208:223]\n"
        "ds_read_b64_tr_b16 v[224:225], v27 offset:28672\n"
        "s_waitcnt vmcnt(2)\n"
        "v_cmp_u_f32_e64 vcc, v128, v148\n"
        "ds_read_b64_tr_b16 v[226:227], v27 offset:29696\n"
        "s_cbranch_vccnz .Lv315\n"
        ".Lb316:\n"
        "s_waitcnt lgkmcnt(8)\n"
        "v_mfma_f32_32x32x16_bf16 v[192:207], v[232:235], v[128:131], v[192:207]\n"
        "ds_read_b64_tr_b16 v[228:229], v26 offset:38912\n"
        "ds_read_b64_tr_b16 v[230:231], v26 offset:39936\n"
        "v_mfma_f32_32x32x16_bf16 v[208:223], v[236:239], v[128:131], v[208:223]\n"
        "ds_read_b64_tr_b16 v[232:233], v27 offset:38912\n"
        "s_waitcnt vmcnt(0)\n"
        "v_cmp_u_f32 vcc, v168, v188\n"
        "ds_read_b64_tr_b16 v[234:235], v27 offset:39936\n"
        "s_cbranch_vccnz .Lv317\n"
        ".Lb318:\n"
        "s_waitcnt lgkmcnt(10)\n"
        "v_mfma_f32_32x32x16_bf16 v[192:207], v[240:243], v[148:151], v[192:207]\n"
        "s_waitcnt lgkmcnt(6)\n"
        "v_mfma_f32_32x32x16_bf16 v[192:207], v[248:251], v[168:171], v[192:207]\n"
        "s_waitcnt lgkmcnt(2)\n"
        "v_mfma_f32_32x32x16_bf16 v[192:207], v[228:231], v[188:191], v[192:207]\n"
        "v_mfma_f32_32x32x16_bf16 v[208:223], v[244:247], v[148:151], v[208:223]\n"
        "v_mfma_f32_32x32x16_bf16 v[208:223], v[224:227], v[168:171], v[208:223]\n"
        "s_nop 9\n"
        "v_cvt_pk_bf16_f32 v16, v192, v193\n"
        "v_cvt_pk_bf16_f32 v17, v194, v195\n"
        "ds_write_b64 v10, v[16:17] offset:0\n"
        "v_cvt_pk_bf16_f32 v18, v196, v197\n"
        "v_cvt_pk_bf16_f32 v19, v198, v199\n"
        "s_waitcnt lgkmcnt(1)\n"
        "v_mfma_f32_32x32x16_bf16 v[208:223], v[232:235], v[188:191], v[208:223]\n"
        "ds_write_b64 v10, v[18:19] offset:16\n"
        "v_cvt_pk_bf16_f32 v20, v200, v201\n"
        "v_cvt_pk_bf16_f32 v21, v202, v203\n"
        "ds_write_b64 v10, v[20:21] offset:32\n"
        "v_cvt_pk_bf16_f32 v22, v204, v205\n"
        "v_cvt_pk_bf16_f32 v23, v206, v207\n"
        "v_cmp_u_f32_e64 vcc, v192, v193\n"
        "ds_write_b64 v10, v[22:23] offset:48\n"
        "s_nop 3\n"
        "s_nop 0\n"
        "v_cvt_pk_bf16_f32 v16, v208, v209\n"
        "v_cvt_pk_bf16_f32 v17, v210, v211\n"
        "ds_write_b64 v10, v[16:17] offset:64\n"
        "v_cvt_pk_bf16_f32 v18, v212, v213\n"
        "v_cvt_pk_bf16_f32 v19, v214, v215\n"
        "ds_write_b64 v10, v[18:19] offset:80\n"
        "v_cvt_pk_bf16_f32 v20, v216, v217\n"
        "v_cvt_pk_bf16_f32 v21, v218, v219\n"
        "ds_write_b64 v10, v[20:21] offset:96\n"
        "v_cvt_pk_bf16_f32 v22, v220, v221\n"
        "v_cvt_pk_bf16_f32 v23, v222, v223\n"
        "ds_write_b64 v10, v[22:23] offset:112\n"
        "s_cbranch_vccnz .Lc319\n"
        ".Lo320:\n"
        "ds_read_b128 v[32:35], v11 offset:0\n"
        "ds_read_b128 v[36:39], v11 offset:1152\n"
        "ds_read_b128 v[40:43], v11 offset:2304\n"
        "ds_read_b128 v[44:47], v11 offset:3456\n"
        "s_mov_b32 s3, 0\n"
        "s_waitcnt lgkmcnt(3)\n"
        "buffer_store_dwordx4 v[32:35], v12, s[44:47], s3 offen sc1\n"
        "s_mov_b32 s3, 20480\n"
        "s_waitcnt lgkmcnt(2)\n"
        "buffer_store_dwordx4 v[36:39], v12, s[44:47], s3 offen sc1\n"
        "s_mov_b32 s3, 40960\n"
        "s_waitcnt lgkmcnt(1)\n"
        "buffer_store_dwordx4 v[40:43], v12, s[44:47], s3 offen sc1\n"
        "s_mov_b32 s3, 61440\n"
        "s_waitcnt lgkmcnt(0)\n"
        "buffer_store_dwordx4 v[44:47], v12, s[44:47], s3 offen sc1\n"
        ".La1:\n"
        "s_endpgm\n"
        ".LroleG:\n"
        "s_barrier\n"
        "v_and_b32 v0, 63, v0\n"
        "s_and_b32 s35, s28, 3\n"
        "v_lshlrev_b32 v8, 4, v0\n"
        "v_mov_b32 v112, 0\n"
        "v_mov_b32 v113, 0\n"
        "v_mov_b32 v114, 0\n"
        "v_mov_b32_e64 v115, 0\n"
        "s_mul_i32 s63, s35, 2816\n"
        "s_add_u32 s63, s63, 152320\n"
        "v_add_u32_e64 v22, s63, v8\n"
        "ds_write_b128 v22, v[112:115] offset:0\n"
        "ds_write_b128 v22, v[112:115] offset:1024\n"
        "ds_write_b128 v22, v[112:115] offset:1792\n"
        "v_lshrrev_b32_e64 v18, 4, v0\n"
        "v_bfe_u32 v19, v0, 2, 2\n"
        "v_and_b32 v20, 3, v0\n"
        "v_and_b32 v21, 15, v0\n"
        "v_lshlrev_b32_e64 v12, 2, v20\n"
        "v_add_u32 v12, 163584, v12\n"
        "ds_write_b32 v12, v112 offset:32\n"
        "ds_write_b32 v12, v112\n"
        "ds_write_b32 v12, v112 offset:16\n"
        "v_mov_b32 v23, 141312\n"
        "ds_write_b64 v23, v[112:113]\n"
        "v_cmp_eq_u32_e64 vcc, s35, v20\n"
        "v_mov_b32 v112, 0x3f803f80\n"
        "v_mov_b32 v113, 0x3f803f80\n"
        "v_cndmask_b32_e64 v114, v114, v112, vcc\n"
        "v_mov_b32 v115, v114\n"
        "s_and_b32 s62, s35, 1\n"
        "s_lshl_b32 s62, s62, 3\n"
        "s_add_u32 s3, s63, s62\n"
        "s_add_u32 s3, s3, 1104\n"
        "v_and_b32_e64 v22, 1, v19\n"
        "v_mul_u32_u24 v22, 1408, v22\n"
        "v_mul_u32_u24 v23, 96, v20\n"
        "v_add3_u32 v22, v22, v23, s3\n"
        "v_xor_b32_e64 v23, 8, v22\n"
        "ds_write_b64 v22, v[112:113]\n"
        "ds_write_b64 v23, v[114:115]\n"
        "s_lshl_b32 s66, s35, 14\n"
        "s_add_u32 s66, s66, 71680\n"
        "v_lshrrev_b32 v24, 2, v0\n"
        "v_mul_u32_u24 v22, 160, v24\n"
        "v_lshl_add_u32 v1, v20, 3, v22\n"
        "v_mul_u32_u24_e64 v23, 40, v20\n"
        "v_add3_u32 v2, v22, v23, 32\n"
        "v_lshrrev_b32 v22, 1, v21\n"
        "v_xor_b32 v22, v18, v22\n"
        "v_lshlrev_b32_e64 v22, 4, v22\n"
        "v_lshl_add_u32 v22, v21, 7, v22\n"
        "v_add_u32_e64 v3, s66, v22\n"
        "v_xor_b32 v4, 64, v3\n"
        "s_waitcnt lgkmcnt(0)\n"
        "v_add_u32 v13, 40960, v1\n"
        "v_add_u32 v14, 40960, v2\n"
        "s_mov_b32 s36, s26\n"
        "s_and_b32 s37, s27, 0xffff\n"
        "s_mov_b32 s38, 8388608\n"
        "s_mov_b32 s39, 0x20000\n"
        "v_lshlrev_b32 v9, 3, v0\n"
        "v_mov_b32 v10, 163632\n"
        "s_lshl_b32 s3, s35, 2\n"
        "s_add_u32 s3, s3, 163584\n"
        "v_mov_b32 v94, s3\n"
        "v_mov_b32_e64 v95, 1\n"
        "s_add_u32 s3, s66, 10752\n"
        "v_mul_u32_u24 v22, 176, v21\n"
        "v_add_u32_e64 v22, s3, v22\n"
        "v_lshl_add_u32 v16, v18, 3, v22\n"
        "v_mul_u32_u24_e64 v23, 40, v18\n"
        "v_add3_u32 v17, v22, v23, 32\n"
        "v_add_u32 v5, 144128, v8\n"
        "v_add_u32 v6, 152320, v8\n"
        "v_min_u32 v22, 2, v20\n"
        "v_add_u32 v22, 3, v22\n"
        "v_cmp_eq_u32 s[22:23], 0, v20\n"
        "v_mul_u32_u24 v23, 96, v18\n"
        "v_add_u32 v23, 153344, v23\n"
        "v_cndmask_b32 v22, v22, v19, s[22:23]\n"
        "v_lshl_add_u32 v7, v22, 4, v23\n"
        "v_lshlrev_b32_e64 v22, 4, v21\n"
        "v_lshl_add_u32 v0, v18, 9, v22\n"
        "s_lshr_b32 s3, s29, 1\n"
        "s_lshl_b32 s3, s3, 2\n"
        "s_lshr_b32 s33, s30, 2\n"
        "s_add_u32 s33, s33, s3\n"
        "s_and_b32 s3, s29, 1\n"
        "s_lshl_b32 s3, s3, 2\n"
        "s_and_b32 s34, s30, 3\n"
        "s_add_u32 s34, s34, s3\n"
        "s_lshl_b32 s64, s33, 2\n"
        "s_add_u32 s64, s64, s35\n"
        "s_lshl_b32 s64, s64, 3\n"
        "s_add_u32 s64, s64, s34\n"
        "s_mul_i32 s65, s64, 5120\n"
        "s_add_u32 s65, s65, 2097152\n"
        "s_add_u32 s66, s65, 4096\n"
        "s_lshl_b32 s64, s64, 7\n"
        "s_add_u32 s64, s64, 196608\n"
        "s_mov_b32 s67, 0x42200000\n"
        "s_mov_b32 s16, 0x358637bd\n"
        "s_waitcnt vmcnt(0) lgkmcnt(0)\n"
        "s_barrier\n"
        "ds_read_b128 v[96:99], v3 offset:0\n"
        "ds_read_b128 v[100:103], v3 offset:2048\n"
        "ds_read_b64_tr_b16 v[26:27], v1 offset:0\n"
        "ds_read_b64_tr_b16 v[28:29], v1 offset:2560\n"
        "ds_read_b128 v[104:107], v4 offset:0\n"
        "ds_read_b128 v[108:111], v4 offset:2048\n"
        "ds_read_b64_tr_b16 v[30:31], v1 offset:40\n"
        "ds_read_b64_tr_b16 v[32:33], v1 offset:2600\n"
        "ds_read_b64_tr_b16 v[34:35], v1 offset:80\n"
        "ds_read_b64_tr_b16 v[36:37], v1 offset:2640\n"
        "ds_read_b64_tr_b16 v[38:39], v1 offset:120\n"
        "ds_read_b64_tr_b16 v[40:41], v1 offset:2680\n"
        "ds_read_b64_tr_b16 v[42:43], v2 offset:0\n"
        "ds_read_b64_tr_b16 v[44:45], v2 offset:2560\n"
        "ds_read_b64_tr_b16 v[46:47], v1 offset:5120\n"
        "ds_read_b64_tr_b16 v[48:49], v1 offset:7680\n"
        "s_waitcnt lgkmcnt(8)\n"
        "v_mfma_f32_16x16x32_bf16 v[54:57], v[26:29], v[96:99], 0\n"
        "ds_read_b64_tr_b16 v[50:51], v1 offset:5160\n"
        "v_mfma_f32_16x16x32_bf16 v[58:61], v[26:29], v[100:103], 0\n"
        "ds_read_b64_tr_b16 v[52:53], v1 offset:7720\n"
        "v_mfma_f32_16x16x32_bf16 v[62:65], v[30:33], v[96:99], 0\n"
        "ds_read_b64_tr_b16 v[26:27], v1 offset:5200\n"
        "v_mfma_f32_16x16x32_bf16 v[66:69], v[30:33], v[100:103], 0\n"
        "ds_read_b64_tr_b16 v[28:29], v1 offset:7760\n"
        "s_waitcnt lgkmcnt(8)\n"
        "v_mfma_f32_16x16x32_bf16 v[70:73], v[34:37], v[96:99], 0\n"
        "ds_read_b64_tr_b16 v[30:31], v1 offset:5240\n"
        "v_mfma_f32_16x16x32_bf16 v[74:77], v[34:37], v[100:103], 0\n"
        "ds_read_b64_tr_b16 v[32:33], v1 offset:7800\n"
        "v_mfma_f32_16x16x32_bf16 v[78:81], v[38:41], v[96:99], 0\n"
        "ds_read_b64_tr_b16 v[34:35], v2 offset:5120\n"
        "v_mfma_f32_16x16x32_bf16 v[82:85], v[38:41], v[100:103], 0\n"
        "ds_read_b64_tr_b16 v[36:37], v2 offset:7680\n"
        "s_barrier\n"
        "s_waitcnt lgkmcnt(8)\n"
        "v_mfma_f32_16x16x32_bf16 v[86:89], v[42:45], v[96:99], 0\n"
        "ds_read_b64_tr_b16 v[38:39], v1 offset:10240\n"
        "ds_read_b128 v[112:115], v3 offset:4096\n"
        "v_mfma_f32_16x16x32_bf16 v[90:93], v[42:45], v[100:103], 0\n"
        "ds_read_b64_tr_b16 v[40:41], v1 offset:12800\n"
        "ds_read_b128 v[116:119], v3 offset:6144\n"
        "v_mfma_f32_16x16x32_bf16 v[54:57], v[46:49], v[104:107], v[54:57]\n"
        "ds_read_b64_tr_b16 v[42:43], v1 offset:10280\n"
        "ds_read_b128 v[120:123], v4 offset:4096\n"
        "v_mfma_f32_16x16x32_bf16 v[58:61], v[46:49], v[108:111], v[58:61]\n"
        "ds_read_b64_tr_b16 v[44:45], v1 offset:12840\n"
        "ds_read_b128 v[124:127], v4 offset:6144\n"
        "s_waitcnt lgkmcnt(12)\n"
        "v_mfma_f32_16x16x32_bf16 v[62:65], v[50:53], v[104:107], v[62:65]\n"
        "ds_read_b64_tr_b16 v[46:47], v1 offset:10320\n"
        "v_mfma_f32_16x16x32_bf16 v[66:69], v[50:53], v[108:111], v[66:69]\n"
        "ds_read_b64_tr_b16 v[48:49], v1 offset:12880\n"
        "v_mfma_f32_16x16x32_bf16 v[70:73], v[26:29], v[104:107], v[70:73]\n"
        "ds_read_b64_tr_b16 v[50:51], v1 offset:10360\n"
        "v_mfma_f32_16x16x32_bf16 v[74:77], v[26:29], v[108:111], v[74:77]\n"
        "ds_read_b64_tr_b16 v[52:53], v1 offset:12920\n"
        "s_waitcnt lgkmcnt(12)\n"
        "v_mfma_f32_16x16x32_bf16 v[78:81], v[30:33], v[104:107], v[78:81]\n"
        "ds_read_b64_tr_b16 v[26:27], v2 offset:10240\n"
        "v_mfma_f32_16x16x32_bf16 v[82:85], v[30:33], v[108:111], v[82:85]\n"
        "ds_read_b64_tr_b16 v[28:29], v2 offset:12800\n"
        "v_mfma_f32_16x16x32_bf16 v[86:89], v[34:37], v[104:107], v[86:89]\n"
        "ds_read_b64_tr_b16 v[30:31], v1 offset:15360\n"
        "v_mfma_f32_16x16x32_bf16 v[90:93], v[34:37], v[108:111], v[90:93]\n"
        "ds_read_b64_tr_b16 v[32:33], v1 offset:17920\n"
        "s_waitcnt lgkmcnt(9)\n"
        "v_mfma_f32_16x16x32_bf16 v[54:57], v[38:41], v[112:115], v[54:57]\n"
        "ds_read_b64_tr_b16 v[34:35], v1 offset:15400\n"
        "v_mfma_f32_16x16x32_bf16 v[58:61], v[38:41], v[116:119], v[58:61]\n"
        "ds_read_b64_tr_b16 v[36:37], v1 offset:17960\n"
        "v_mfma_f32_16x16x32_bf16 v[62:65], v[42:45], v[112:115], v[62:65]\n"
        "ds_read_b64_tr_b16 v[38:39], v1 offset:15440\n"
        "v_mfma_f32_16x16x32_bf16 v[66:69], v[42:45], v[116:119], v[66:69]\n"
        "ds_read_b64_tr_b16 v[40:41], v1 offset:18000\n"
        "s_waitcnt lgkmcnt(8)\n"
        "v_mfma_f32_16x16x32_bf16 v[70:73], v[46:49], v[112:115], v[70:73]\n"
        "ds_read_b64_tr_b16 v[42:43], v1 offset:15480\n"
        "v_mfma_f32_16x16x32_bf16 v[74:77], v[46:49], v[116:119], v[74:77]\n"
        "ds_read_b64_tr_b16 v[44:45], v1 offset:18040\n"
        "v_mfma_f32_16x16x32_bf16 v[78:81], v[50:53], v[112:115], v[78:81]\n"
        "ds_read_b64_tr_b16 v[46:47], v2 offset:15360\n"
        "v_mfma_f32_16x16x32_bf16 v[82:85], v[50:53], v[116:119], v[82:85]\n"
        "ds_read_b64_tr_b16 v[48:49], v2 offset:17920\n"
        "s_barrier\n"
        "s_waitcnt lgkmcnt(8)\n"
        "v_mfma_f32_16x16x32_bf16 v[86:89], v[26:29], v[112:115], v[86:89]\n"
        "ds_read_b64_tr_b16 v[50:51], v1 offset:20480\n"
        "ds_read_b128 v[128:131], v3 offset:8192\n"
        "v_mfma_f32_16x16x32_bf16 v[90:93], v[26:29], v[116:119], v[90:93]\n"
        "ds_read_b64_tr_b16 v[52:53], v1 offset:23040\n"
        "ds_read_b128 v[132:135], v3 offset:10240\n"
        "v_mfma_f32_16x16x32_bf16 v[54:57], v[30:33], v[120:123], v[54:57]\n"
        "ds_read_b64_tr_b16 v[26:27], v1 offset:20520\n"
        "ds_read_b128 v[136:139], v4 offset:8192\n"
        "v_mfma_f32_16x16x32_bf16 v[58:61], v[30:33], v[124:127], v[58:61]\n"
        "ds_read_b64_tr_b16 v[28:29], v1 offset:23080\n"
        "ds_read_b128 v[140:143], v4 offset:10240\n"
        "s_waitcnt lgkmcnt(12)\n"
        "v_mfma_f32_16x16x32_bf16 v[62:65], v[34:37], v[120:123], v[62:65]\n"
        "ds_read_b64_tr_b16 v[30:31], v1 offset:20560\n"
        "v_mfma_f32_16x16x32_bf16 v[66:69], v[34:37], v[124:127], v[66:69]\n"
        "ds_read_b64_tr_b16 v[32:33], v1 offset:23120\n"
        "v_mfma_f32_16x16x32_bf16 v[70:73], v[38:41], v[120:123], v[70:73]\n"
        "ds_read_b64_tr_b16 v[34:35], v1 offset:20600\n"
        "v_mfma_f32_16x16x32_bf16 v[74:77], v[38:41], v[124:127], v[74:77]\n"
        "ds_read_b64_tr_b16 v[36:37], v1 offset:23160\n"
        "s_waitcnt lgkmcnt(12)\n"
        "v_mfma_f32_16x16x32_bf16 v[78:81], v[42:45], v[120:123], v[78:81]\n"
        "ds_read_b64_tr_b16 v[38:39], v2 offset:20480\n"
        "v_mfma_f32_16x16x32_bf16 v[82:85], v[42:45], v[124:127], v[82:85]\n"
        "ds_read_b64_tr_b16 v[40:41], v2 offset:23040\n"
        "v_mfma_f32_16x16x32_bf16 v[86:89], v[46:49], v[120:123], v[86:89]\n"
        "ds_read_b64_tr_b16 v[42:43], v1 offset:25600\n"
        "v_mfma_f32_16x16x32_bf16 v[90:93], v[46:49], v[124:127], v[90:93]\n"
        "ds_read_b64_tr_b16 v[44:45], v1 offset:28160\n"
        "s_waitcnt lgkmcnt(9)\n"
        "v_mfma_f32_16x16x32_bf16 v[54:57], v[50:53], v[128:131], v[54:57]\n"
        "ds_read_b64_tr_b16 v[46:47], v1 offset:25640\n"
        "v_mfma_f32_16x16x32_bf16 v[58:61], v[50:53], v[132:135], v[58:61]\n"
        "ds_read_b64_tr_b16 v[48:49], v1 offset:28200\n"
        "v_mfma_f32_16x16x32_bf16 v[62:65], v[26:29], v[128:131], v[62:65]\n"
        "ds_read_b64_tr_b16 v[50:51], v1 offset:25680\n"
        "v_mfma_f32_16x16x32_bf16 v[66:69], v[26:29], v[132:135], v[66:69]\n"
        "ds_read_b64_tr_b16 v[52:53], v1 offset:28240\n"
        "s_waitcnt lgkmcnt(8)\n"
        "v_mfma_f32_16x16x32_bf16 v[70:73], v[30:33], v[128:131], v[70:73]\n"
        "ds_read_b64_tr_b16 v[26:27], v1 offset:25720\n"
        "v_mfma_f32_16x16x32_bf16 v[74:77], v[30:33], v[132:135], v[74:77]\n"
        "ds_read_b64_tr_b16 v[28:29], v1 offset:28280\n"
        "v_mfma_f32_16x16x32_bf16 v[78:81], v[34:37], v[128:131], v[78:81]\n"
        "ds_read_b64_tr_b16 v[30:31], v2 offset:25600\n"
        "v_mfma_f32_16x16x32_bf16 v[82:85], v[34:37], v[132:135], v[82:85]\n"
        "ds_read_b64_tr_b16 v[32:33], v2 offset:28160\n"
        "s_barrier\n"
        "s_waitcnt lgkmcnt(8)\n"
        "v_mfma_f32_16x16x32_bf16 v[86:89], v[38:41], v[128:131], v[86:89]\n"
        "ds_read_b64_tr_b16 v[34:35], v1 offset:30720\n"
        "ds_read_b128 v[144:147], v3 offset:12288\n"
        "v_mfma_f32_16x16x32_bf16 v[90:93], v[38:41], v[132:135], v[90:93]\n"
        "ds_read_b64_tr_b16 v[36:37], v1 offset:33280\n"
        "ds_read_b128 v[148:151], v3 offset:14336\n"
        "v_mfma_f32_16x16x32_bf16 v[54:57], v[42:45], v[136:139], v[54:57]\n"
        "ds_read_b64_tr_b16 v[38:39], v1 offset:30760\n"
        "ds_read_b128 v[152:155], v4 offset:12288\n"
        "v_mfma_f32_16x16x32_bf16 v[58:61], v[42:45], v[140:143], v[58:61]\n"
        "ds_read_b64_tr_b16 v[40:41], v1 offset:33320\n"
        "ds_read_b128 v[156:159], v4 offset:14336\n"
        "s_waitcnt lgkmcnt(12)\n"
        "v_mfma_f32_16x16x32_bf16 v[62:65], v[46:49], v[136:139], v[62:65]\n"
        "ds_read_b64_tr_b16 v[42:43], v1 offset:30800\n"
        "v_mfma_f32_16x16x32_bf16 v[66:69], v[46:49], v[140:143], v[66:69]\n"
        "ds_read_b64_tr_b16 v[44:45], v1 offset:33360\n"
        "v_mfma_f32_16x16x32_bf16 v[70:73], v[50:53], v[136:139], v[70:73]\n"
        "ds_read_b64_tr_b16 v[46:47], v1 offset:30840\n"
        "v_mfma_f32_16x16x32_bf16 v[74:77], v[50:53], v[140:143], v[74:77]\n"
        "ds_read_b64_tr_b16 v[48:49], v1 offset:33400\n"
        "s_waitcnt lgkmcnt(12)\n"
        "v_mfma_f32_16x16x32_bf16 v[78:81], v[26:29], v[136:139], v[78:81]\n"
        "ds_read_b64_tr_b16 v[50:51], v2 offset:30720\n"
        "v_mfma_f32_16x16x32_bf16 v[82:85], v[26:29], v[140:143], v[82:85]\n"
        "ds_read_b64_tr_b16 v[52:53], v2 offset:33280\n"
        "v_mfma_f32_16x16x32_bf16 v[86:89], v[30:33], v[136:139], v[86:89]\n"
        "ds_read_b64_tr_b16 v[26:27], v1 offset:35840\n"
        "v_mfma_f32_16x16x32_bf16 v[90:93], v[30:33], v[140:143], v[90:93]\n"
        "ds_read_b64_tr_b16 v[28:29], v1 offset:38400\n"
        "s_waitcnt lgkmcnt(9)\n"
        "v_mfma_f32_16x16x32_bf16 v[54:57], v[34:37], v[144:147], v[54:57]\n"
        "ds_read_b64_tr_b16 v[30:31], v1 offset:35880\n"
        "v_mfma_f32_16x16x32_bf16 v[58:61], v[34:37], v[148:151], v[58:61]\n"
        "ds_read_b64_tr_b16 v[32:33], v1 offset:38440\n"
        "v_mfma_f32_16x16x32_bf16 v[62:65], v[38:41], v[144:147], v[62:65]\n"
        "ds_read_b64_tr_b16 v[34:35], v1 offset:35920\n"
        "v_mfma_f32_16x16x32_bf16 v[66:69], v[38:41], v[148:151], v[66:69]\n"
        "ds_read_b64_tr_b16 v[36:37], v1 offset:38480\n"
        "s_waitcnt lgkmcnt(8)\n"
        "v_mfma_f32_16x16x32_bf16 v[70:73], v[42:45], v[144:147], v[70:73]\n"
        "ds_read_b64_tr_b16 v[38:39], v1 offset:35960\n"
        "v_mfma_f32_16x16x32_bf16 v[74:77], v[42:45], v[148:151], v[74:77]\n"
        "ds_read_b64_tr_b16 v[40:41], v1 offset:38520\n"
        "v_mfma_f32_16x16x32_bf16 v[78:81], v[46:49], v[144:147], v[78:81]\n"
        "ds_read_b64_tr_b16 v[42:43], v2 offset:35840\n"
        "v_mfma_f32_16x16x32_bf16 v[82:85], v[46:49], v[148:151], v[82:85]\n"
        "ds_read_b64_tr_b16 v[44:45], v2 offset:38400\n"
        "s_barrier\n"
        "s_waitcnt lgkmcnt(8)\n"
        "v_mfma_f32_16x16x32_bf16 v[86:89], v[50:53], v[144:147], v[86:89]\n"
        "ds_read_b64_tr_b16 v[46:47], v13 offset:0\n"
        "ds_read_b128 v[160:163], v3 offset:0\n"
        "v_mfma_f32_16x16x32_bf16 v[90:93], v[50:53], v[148:151], v[90:93]\n"
        "ds_read_b64_tr_b16 v[48:49], v13 offset:2560\n"
        "ds_read_b128 v[164:167], v3 offset:2048\n"
        "v_mfma_f32_16x16x32_bf16 v[54:57], v[26:29], v[152:155], v[54:57]\n"
        "ds_read_b64_tr_b16 v[50:51], v13 offset:40\n"
        "ds_read_b128 v[168:171], v4 offset:0\n"
        "v_mfma_f32_16x16x32_bf16 v[58:61], v[26:29], v[156:159], v[58:61]\n"
        "ds_read_b64_tr_b16 v[52:53], v13 offset:2600\n"
        "ds_read_b128 v[172:175], v4 offset:2048\n"
        "s_waitcnt lgkmcnt(12)\n"
        "v_mfma_f32_16x16x32_bf16 v[62:65], v[30:33], v[152:155], v[62:65]\n"
        "ds_read_b64_tr_b16 v[26:27], v13 offset:80\n"
        "v_mfma_f32_16x16x32_bf16 v[66:69], v[30:33], v[156:159], v[66:69]\n"
        "ds_read_b64_tr_b16 v[28:29], v13 offset:2640\n"
        "v_mfma_f32_16x16x32_bf16 v[70:73], v[34:37], v[152:155], v[70:73]\n"
        "ds_read_b64_tr_b16 v[30:31], v13 offset:120\n"
        "v_mfma_f32_16x16x32_bf16 v[74:77], v[34:37], v[156:159], v[74:77]\n"
        "ds_read_b64_tr_b16 v[32:33], v13 offset:2680\n"
        "s_waitcnt lgkmcnt(12)\n"
        "v_mfma_f32_16x16x32_bf16 v[78:81], v[38:41], v[152:155], v[78:81]\n"
        "ds_read_b64_tr_b16 v[34:35], v14 offset:0\n"
        "v_mfma_f32_16x16x32_bf16 v[82:85], v[38:41], v[156:159], v[82:85]\n"
        "ds_read_b64_tr_b16 v[36:37], v14 offset:2560\n"
        "v_mfma_f32_16x16x32_bf16 v[86:89], v[42:45], v[152:155], v[86:89]\n"
        "ds_read_b64_tr_b16 v[38:39], v13 offset:5120\n"
        "v_mfma_f32_16x16x32_bf16 v[90:93], v[42:45], v[156:159], v[90:93]\n"
        "ds_read_b64_tr_b16 v[40:41], v13 offset:7680\n"
        "s_waitcnt lgkmcnt(9)\n"
        "v_mfma_f32_16x16x32_bf16 v[54:57], v[46:49], v[160:163], v[54:57]\n"
        "ds_read_b64_tr_b16 v[42:43], v13 offset:5160\n"
        "v_mfma_f32_16x16x32_bf16 v[58:61], v[46:49], v[164:167], v[58:61]\n"
        "ds_read_b64_tr_b16 v[44:45], v13 offset:7720\n"
        "v_mfma_f32_16x16x32_bf16 v[62:65], v[50:53], v[160:163], v[62:65]\n"
        "ds_read_b64_tr_b16 v[46:47], v13 offset:5200\n"
        "v_mfma_f32_16x16x32_bf16 v[66:69], v[50:53], v[164:167], v[66:69]\n"
        "ds_read_b64_tr_b16 v[48:49], v13 offset:7760\n"
        "s_waitcnt lgkmcnt(8)\n"
        "v_mfma_f32_16x16x32_bf16 v[70:73], v[26:29], v[160:163], v[70:73]\n"
        "ds_read_b64_tr_b16 v[50:51], v13 offset:5240\n"
        "v_mfma_f32_16x16x32_bf16 v[74:77], v[26:29], v[164:167], v[74:77]\n"
        "ds_read_b64_tr_b16 v[52:53], v13 offset:7800\n"
        "v_mfma_f32_16x16x32_bf16 v[78:81], v[30:33], v[160:163], v[78:81]\n"
        "ds_read_b64_tr_b16 v[26:27], v14 offset:5120\n"
        "v_mfma_f32_16x16x32_bf16 v[82:85], v[30:33], v[164:167], v[82:85]\n"
        "ds_read_b64_tr_b16 v[28:29], v14 offset:7680\n"
        "s_barrier\n"
        "s_waitcnt lgkmcnt(8)\n"
        "v_mfma_f32_16x16x32_bf16 v[86:89], v[34:37], v[160:163], v[86:89]\n"
        "ds_read_b64_tr_b16 v[30:31], v13 offset:10240\n"
        "ds_read_b128 v[176:179], v3 offset:4096\n"
        "v_mfma_f32_16x16x32_bf16 v[90:93], v[34:37], v[164:167], v[90:93]\n"
        "ds_read_b64_tr_b16 v[32:33], v13 offset:12800\n"
        "ds_read_b128 v[180:183], v3 offset:6144\n"
        "v_mfma_f32_16x16x32_bf16 v[54:57], v[38:41], v[168:171], v[54:57]\n"
        "ds_read_b64_tr_b16 v[34:35], v13 offset:10280\n"
        "ds_read_b128 v[184:187], v4 offset:4096\n"
        "v_mfma_f32_16x16x32_bf16 v[58:61], v[38:41], v[172:175], v[58:61]\n"
        "ds_read_b64_tr_b16 v[36:37], v13 offset:12840\n"
        "ds_read_b128 v[188:191], v4 offset:6144\n"
        "s_waitcnt lgkmcnt(12)\n"
        "v_mfma_f32_16x16x32_bf16 v[62:65], v[42:45], v[168:171], v[62:65]\n"
        "ds_read_b64_tr_b16 v[38:39], v13 offset:10320\n"
        "v_mfma_f32_16x16x32_bf16 v[66:69], v[42:45], v[172:175], v[66:69]\n"
        "ds_read_b64_tr_b16 v[40:41], v13 offset:12880\n"
        "v_mfma_f32_16x16x32_bf16 v[70:73], v[46:49], v[168:171], v[70:73]\n"
        "ds_read_b64_tr_b16 v[42:43], v13 offset:10360\n"
        "v_mfma_f32_16x16x32_bf16 v[74:77], v[46:49], v[172:175], v[74:77]\n"
        "ds_read_b64_tr_b16 v[44:45], v13 offset:12920\n"
        "s_waitcnt lgkmcnt(12)\n"
        "v_mfma_f32_16x16x32_bf16 v[78:81], v[50:53], v[168:171], v[78:81]\n"
        "ds_read_b64_tr_b16 v[46:47], v14 offset:10240\n"
        "v_mfma_f32_16x16x32_bf16 v[82:85], v[50:53], v[172:175], v[82:85]\n"
        "ds_read_b64_tr_b16 v[48:49], v14 offset:12800\n"
        "v_mfma_f32_16x16x32_bf16 v[86:89], v[26:29], v[168:171], v[86:89]\n"
        "ds_read_b64_tr_b16 v[50:51], v13 offset:15360\n"
        "v_mfma_f32_16x16x32_bf16 v[90:93], v[26:29], v[172:175], v[90:93]\n"
        "ds_read_b64_tr_b16 v[52:53], v13 offset:17920\n"
        "s_waitcnt lgkmcnt(9)\n"
        "v_mfma_f32_16x16x32_bf16 v[54:57], v[30:33], v[176:179], v[54:57]\n"
        "ds_read_b64_tr_b16 v[26:27], v13 offset:15400\n"
        "v_mfma_f32_16x16x32_bf16 v[58:61], v[30:33], v[180:183], v[58:61]\n"
        "ds_read_b64_tr_b16 v[28:29], v13 offset:17960\n"
        "v_mfma_f32_16x16x32_bf16 v[62:65], v[34:37], v[176:179], v[62:65]\n"
        "ds_read_b64_tr_b16 v[30:31], v13 offset:15440\n"
        "v_mfma_f32_16x16x32_bf16 v[66:69], v[34:37], v[180:183], v[66:69]\n"
        "ds_read_b64_tr_b16 v[32:33], v13 offset:18000\n"
        "s_waitcnt lgkmcnt(8)\n"
        "v_mfma_f32_16x16x32_bf16 v[70:73], v[38:41], v[176:179], v[70:73]\n"
        "ds_read_b64_tr_b16 v[34:35], v13 offset:15480\n"
        "v_mfma_f32_16x16x32_bf16 v[74:77], v[38:41], v[180:183], v[74:77]\n"
        "ds_read_b64_tr_b16 v[36:37], v13 offset:18040\n"
        "v_mfma_f32_16x16x32_bf16 v[78:81], v[42:45], v[176:179], v[78:81]\n"
        "ds_read_b64_tr_b16 v[38:39], v14 offset:15360\n"
        "v_mfma_f32_16x16x32_bf16 v[82:85], v[42:45], v[180:183], v[82:85]\n"
        "ds_read_b64_tr_b16 v[40:41], v14 offset:17920\n"
        "s_barrier\n"
        "s_waitcnt lgkmcnt(8)\n"
        "v_mfma_f32_16x16x32_bf16 v[86:89], v[46:49], v[176:179], v[86:89]\n"
        "ds_read_b64_tr_b16 v[42:43], v13 offset:20480\n"
        "ds_read_b128 v[192:195], v3 offset:8192\n"
        "v_mfma_f32_16x16x32_bf16 v[90:93], v[46:49], v[180:183], v[90:93]\n"
        "ds_read_b64_tr_b16 v[44:45], v13 offset:23040\n"
        "ds_read_b128 v[196:199], v3 offset:10240\n"
        "v_mfma_f32_16x16x32_bf16 v[54:57], v[50:53], v[184:187], v[54:57]\n"
        "ds_read_b64_tr_b16 v[46:47], v13 offset:20520\n"
        "ds_read_b128 v[200:203], v4 offset:8192\n"
        "v_mfma_f32_16x16x32_bf16 v[58:61], v[50:53], v[188:191], v[58:61]\n"
        "ds_read_b64_tr_b16 v[48:49], v13 offset:23080\n"
        "ds_read_b128 v[204:207], v4 offset:10240\n"
        "s_waitcnt lgkmcnt(12)\n"
        "v_mfma_f32_16x16x32_bf16 v[62:65], v[26:29], v[184:187], v[62:65]\n"
        "ds_read_b64_tr_b16 v[50:51], v13 offset:20560\n"
        "v_mfma_f32_16x16x32_bf16 v[66:69], v[26:29], v[188:191], v[66:69]\n"
        "ds_read_b64_tr_b16 v[52:53], v13 offset:23120\n"
        "v_mfma_f32_16x16x32_bf16 v[70:73], v[30:33], v[184:187], v[70:73]\n"
        "ds_read_b64_tr_b16 v[26:27], v13 offset:20600\n"
        "v_mfma_f32_16x16x32_bf16 v[74:77], v[30:33], v[188:191], v[74:77]\n"
        "ds_read_b64_tr_b16 v[28:29], v13 offset:23160\n"
        "s_waitcnt lgkmcnt(12)\n"
        "v_mfma_f32_16x16x32_bf16 v[78:81], v[34:37], v[184:187], v[78:81]\n"
        "ds_read_b64_tr_b16 v[30:31], v14 offset:20480\n"
        "v_mfma_f32_16x16x32_bf16 v[82:85], v[34:37], v[188:191], v[82:85]\n"
        "ds_read_b64_tr_b16 v[32:33], v14 offset:23040\n"
        "v_mfma_f32_16x16x32_bf16 v[86:89], v[38:41], v[184:187], v[86:89]\n"
        "ds_read_b64_tr_b16 v[34:35], v13 offset:25600\n"
        "v_mfma_f32_16x16x32_bf16 v[90:93], v[38:41], v[188:191], v[90:93]\n"
        "ds_read_b64_tr_b16 v[36:37], v13 offset:28160\n"
        "s_waitcnt lgkmcnt(9)\n"
        "v_mfma_f32_16x16x32_bf16 v[54:57], v[42:45], v[192:195], v[54:57]\n"
        "ds_read_b64_tr_b16 v[38:39], v13 offset:25640\n"
        "v_mfma_f32_16x16x32_bf16 v[58:61], v[42:45], v[196:199], v[58:61]\n"
        "ds_read_b64_tr_b16 v[40:41], v13 offset:28200\n"
        "v_mfma_f32_16x16x32_bf16 v[62:65], v[46:49], v[192:195], v[62:65]\n"
        "ds_read_b64_tr_b16 v[42:43], v13 offset:25680\n"
        "v_mfma_f32_16x16x32_bf16 v[66:69], v[46:49], v[196:199], v[66:69]\n"
        "ds_read_b64_tr_b16 v[44:45], v13 offset:28240\n"
        "s_waitcnt lgkmcnt(8)\n"
        "v_mfma_f32_16x16x32_bf16 v[70:73], v[50:53], v[192:195], v[70:73]\n"
        "ds_read_b64_tr_b16 v[46:47], v13 offset:25720\n"
        "v_mfma_f32_16x16x32_bf16 v[74:77], v[50:53], v[196:199], v[74:77]\n"
        "ds_read_b64_tr_b16 v[48:49], v13 offset:28280\n"
        "v_mfma_f32_16x16x32_bf16 v[78:81], v[26:29], v[192:195], v[78:81]\n"
        "ds_read_b64_tr_b16 v[50:51], v14 offset:25600\n"
        "v_mfma_f32_16x16x32_bf16 v[82:85], v[26:29], v[196:199], v[82:85]\n"
        "ds_read_b64_tr_b16 v[52:53], v14 offset:28160\n"
        "s_barrier\n"
        "s_waitcnt lgkmcnt(8)\n"
        "v_mfma_f32_16x16x32_bf16 v[86:89], v[30:33], v[192:195], v[86:89]\n"
        "ds_read_b64_tr_b16 v[26:27], v1 offset:0\n"
        "ds_read_b128 v[208:211], v3 offset:12288\n"
        "v_mfma_f32_16x16x32_bf16 v[90:93], v[30:33], v[196:199], v[90:93]\n"
        "ds_read_b64_tr_b16 v[28:29], v1 offset:2560\n"
        "ds_read_b128 v[212:215], v3 offset:14336\n"
        "v_mfma_f32_16x16x32_bf16 v[54:57], v[34:37], v[200:203], v[54:57]\n"
        "ds_read_b64_tr_b16 v[30:31], v1 offset:40\n"
        "ds_read_b128 v[216:219], v4 offset:12288\n"
        "v_mfma_f32_16x16x32_bf16 v[58:61], v[34:37], v[204:207], v[58:61]\n"
        "ds_read_b64_tr_b16 v[32:33], v1 offset:2600\n"
        "ds_read_b128 v[220:223], v4 offset:14336\n"
        "s_waitcnt lgkmcnt(12)\n"
        "v_mfma_f32_16x16x32_bf16 v[62:65], v[38:41], v[200:203], v[62:65]\n"
        "ds_read_b64_tr_b16 v[34:35], v1 offset:80\n"
        "v_mfma_f32_16x16x32_bf16 v[66:69], v[38:41], v[204:207], v[66:69]\n"
        "ds_read_b64_tr_b16 v[36:37], v1 offset:2640\n"
        "v_mfma_f32_16x16x32_bf16 v[70:73], v[42:45], v[200:203], v[70:73]\n"
        "ds_read_b64_tr_b16 v[38:39], v1 offset:120\n"
        "v_mfma_f32_16x16x32_bf16 v[74:77], v[42:45], v[204:207], v[74:77]\n"
        "ds_read_b64_tr_b16 v[40:41], v1 offset:2680\n"
        "s_waitcnt lgkmcnt(12)\n"
        "v_mfma_f32_16x16x32_bf16 v[78:81], v[46:49], v[200:203], v[78:81]\n"
        "ds_read_b64_tr_b16 v[42:43], v2 offset:0\n"
        "v_mfma_f32_16x16x32_bf16 v[82:85], v[46:49], v[204:207], v[82:85]\n"
        "ds_read_b64_tr_b16 v[44:45], v2 offset:2560\n"
        "v_mfma_f32_16x16x32_bf16 v[86:89], v[50:53], v[200:203], v[86:89]\n"
        "ds_read_b64_tr_b16 v[46:47], v1 offset:5120\n"
        "v_mfma_f32_16x16x32_bf16 v[90:93], v[50:53], v[204:207], v[90:93]\n"
        "ds_read_b64_tr_b16 v[48:49], v1 offset:7680\n"
        "s_waitcnt lgkmcnt(9)\n"
        "v_mfma_f32_16x16x32_bf16 v[54:57], v[26:29], v[208:211], v[54:57]\n"
        "ds_read_b64_tr_b16 v[50:51], v1 offset:5160\n"
        "v_mfma_f32_16x16x32_bf16 v[58:61], v[26:29], v[212:215], v[58:61]\n"
        "ds_read_b64_tr_b16 v[52:53], v1 offset:7720\n"
        "v_mfma_f32_16x16x32_bf16 v[62:65], v[30:33], v[208:211], v[62:65]\n"
        "ds_read_b64_tr_b16 v[26:27], v1 offset:5200\n"
        "v_mfma_f32_16x16x32_bf16 v[66:69], v[30:33], v[212:215], v[66:69]\n"
        "ds_read_b64_tr_b16 v[28:29], v1 offset:7760\n"
        "s_waitcnt lgkmcnt(8)\n"
        "v_mfma_f32_16x16x32_bf16 v[70:73], v[34:37], v[208:211], v[70:73]\n"
        "ds_read_b64_tr_b16 v[30:31], v1 offset:5240\n"
        "v_mfma_f32_16x16x32_bf16 v[74:77], v[34:37], v[212:215], v[74:77]\n"
        "ds_read_b64_tr_b16 v[32:33], v1 offset:7800\n"
        "v_mfma_f32_16x16x32_bf16 v[78:81], v[38:41], v[208:211], v[78:81]\n"
        "ds_read_b64_tr_b16 v[34:35], v2 offset:5120\n"
        "v_mfma_f32_16x16x32_bf16 v[82:85], v[38:41], v[212:215], v[82:85]\n"
        "ds_read_b64_tr_b16 v[36:37], v2 offset:7680\n"
        "s_barrier\n"
        "s_waitcnt lgkmcnt(8)\n"
        "v_mfma_f32_16x16x32_bf16 v[86:89], v[42:45], v[208:211], v[86:89]\n"
        "ds_read_b64_tr_b16 v[38:39], v1 offset:10240\n"
        "ds_read_b128 v[224:227], v3 offset:0\n"
        "v_mfma_f32_16x16x32_bf16 v[90:93], v[42:45], v[212:215], v[90:93]\n"
        "ds_read_b64_tr_b16 v[40:41], v1 offset:12800\n"
        "ds_read_b128 v[228:231], v3 offset:2048\n"
        "v_mfma_f32_16x16x32_bf16 v[54:57], v[46:49], v[216:219], v[54:57]\n"
        "ds_read_b64_tr_b16 v[42:43], v1 offset:10280\n"
        "ds_read_b128 v[232:235], v4 offset:0\n"
        "v_mfma_f32_16x16x32_bf16 v[58:61], v[46:49], v[220:223], v[58:61]\n"
        "ds_read_b64_tr_b16 v[44:45], v1 offset:12840\n"
        "ds_read_b128 v[236:239], v4 offset:2048\n"
        "s_waitcnt lgkmcnt(12)\n"
        "v_mfma_f32_16x16x32_bf16 v[62:65], v[50:53], v[216:219], v[62:65]\n"
        "ds_read_b64_tr_b16 v[46:47], v1 offset:10320\n"
        "v_mfma_f32_16x16x32_bf16 v[66:69], v[50:53], v[220:223], v[66:69]\n"
        "ds_read_b64_tr_b16 v[48:49], v1 offset:12880\n"
        "v_mfma_f32_16x16x32_bf16 v[70:73], v[26:29], v[216:219], v[70:73]\n"
        "ds_read_b64_tr_b16 v[50:51], v1 offset:10360\n"
        "v_mfma_f32_16x16x32_bf16 v[74:77], v[26:29], v[220:223], v[74:77]\n"
        "ds_read_b64_tr_b16 v[52:53], v1 offset:12920\n"
        "s_waitcnt lgkmcnt(12)\n"
        "v_mfma_f32_16x16x32_bf16 v[78:81], v[30:33], v[216:219], v[78:81]\n"
        "ds_read_b64_tr_b16 v[26:27], v2 offset:10240\n"
        "v_mfma_f32_16x16x32_bf16 v[82:85], v[30:33], v[220:223], v[82:85]\n"
        "ds_read_b64_tr_b16 v[28:29], v2 offset:12800\n"
        "v_mfma_f32_16x16x32_bf16 v[86:89], v[34:37], v[216:219], v[86:89]\n"
        "ds_read_b64_tr_b16 v[30:31], v1 offset:15360\n"
        "v_mfma_f32_16x16x32_bf16 v[90:93], v[34:37], v[220:223], v[90:93]\n"
        "ds_read_b64_tr_b16 v[32:33], v1 offset:17920\n"
        "s_waitcnt lgkmcnt(9)\n"
        "v_mfma_f32_16x16x32_bf16 v[54:57], v[38:41], v[224:227], v[54:57]\n"
        "ds_read_b64_tr_b16 v[34:35], v1 offset:15400\n"
        "v_mfma_f32_16x16x32_bf16 v[58:61], v[38:41], v[228:231], v[58:61]\n"
        "ds_read_b64_tr_b16 v[36:37], v1 offset:17960\n"
        "v_mfma_f32_16x16x32_bf16 v[62:65], v[42:45], v[224:227], v[62:65]\n"
        "ds_read_b64_tr_b16 v[38:39], v1 offset:15440\n"
        "v_mfma_f32_16x16x32_bf16 v[66:69], v[42:45], v[228:231], v[66:69]\n"
        "ds_read_b64_tr_b16 v[40:41], v1 offset:18000\n"
        "s_waitcnt lgkmcnt(8)\n"
        "v_mfma_f32_16x16x32_bf16 v[70:73], v[46:49], v[224:227], v[70:73]\n"
        "ds_read_b64_tr_b16 v[42:43], v1 offset:15480\n"
        "v_mfma_f32_16x16x32_bf16 v[74:77], v[46:49], v[228:231], v[74:77]\n"
        "ds_read_b64_tr_b16 v[44:45], v1 offset:18040\n"
        "v_mfma_f32_16x16x32_bf16 v[78:81], v[50:53], v[224:227], v[78:81]\n"
        "ds_read_b64_tr_b16 v[46:47], v2 offset:15360\n"
        "v_mfma_f32_16x16x32_bf16 v[82:85], v[50:53], v[228:231], v[82:85]\n"
        "ds_read_b64_tr_b16 v[48:49], v2 offset:17920\n"
        "s_barrier\n"
        "s_waitcnt lgkmcnt(8)\n"
        "v_mfma_f32_16x16x32_bf16 v[86:89], v[26:29], v[224:227], v[86:89]\n"
        "ds_read_b64_tr_b16 v[50:51], v1 offset:20480\n"
        "ds_read_b128 v[240:243], v3 offset:4096\n"
        "v_mfma_f32_16x16x32_bf16 v[90:93], v[26:29], v[228:231], v[90:93]\n"
        "ds_read_b64_tr_b16 v[52:53], v1 offset:23040\n"
        "ds_read_b128 v[244:247], v3 offset:6144\n"
        "v_mfma_f32_16x16x32_bf16 v[54:57], v[30:33], v[232:235], v[54:57]\n"
        "ds_read_b64_tr_b16 v[26:27], v1 offset:20520\n"
        "ds_read_b128 v[248:251], v4 offset:4096\n"
        "v_mfma_f32_16x16x32_bf16 v[58:61], v[30:33], v[236:239], v[58:61]\n"
        "ds_read_b64_tr_b16 v[28:29], v1 offset:23080\n"
        "ds_read_b128 v[252:255], v4 offset:6144\n"
        "s_waitcnt lgkmcnt(12)\n"
        "v_mfma_f32_16x16x32_bf16 v[62:65], v[34:37], v[232:235], v[62:65]\n"
        "ds_read_b64_tr_b16 v[30:31], v1 offset:20560\n"
        "v_mfma_f32_16x16x32_bf16 v[66:69], v[34:37], v[236:239], v[66:69]\n"
        "ds_read_b64_tr_b16 v[32:33], v1 offset:23120\n"
        "v_mfma_f32_16x16x32_bf16 v[70:73], v[38:41], v[232:235], v[70:73]\n"
        "ds_read_b64_tr_b16 v[34:35], v1 offset:20600\n"
        "v_mfma_f32_16x16x32_bf16 v[74:77], v[38:41], v[236:239], v[74:77]\n"
        "ds_read_b64_tr_b16 v[36:37], v1 offset:23160\n"
        "s_waitcnt lgkmcnt(12)\n"
        "v_mfma_f32_16x16x32_bf16 v[78:81], v[42:45], v[232:235], v[78:81]\n"
        "ds_read_b64_tr_b16 v[38:39], v2 offset:20480\n"
        "v_mfma_f32_16x16x32_bf16 v[82:85], v[42:45], v[236:239], v[82:85]\n"
        "ds_read_b64_tr_b16 v[40:41], v2 offset:23040\n"
        "v_mfma_f32_16x16x32_bf16 v[86:89], v[46:49], v[232:235], v[86:89]\n"
        "ds_read_b64_tr_b16 v[42:43], v1 offset:25600\n"
        "v_mfma_f32_16x16x32_bf16 v[90:93], v[46:49], v[236:239], v[90:93]\n"
        "ds_read_b64_tr_b16 v[44:45], v1 offset:28160\n"
        "s_waitcnt lgkmcnt(9)\n"
        "v_mfma_f32_16x16x32_bf16 v[54:57], v[50:53], v[240:243], v[54:57]\n"
        "ds_read_b64_tr_b16 v[46:47], v1 offset:25640\n"
        "v_mfma_f32_16x16x32_bf16 v[58:61], v[50:53], v[244:247], v[58:61]\n"
        "ds_read_b64_tr_b16 v[48:49], v1 offset:28200\n"
        "v_mfma_f32_16x16x32_bf16 v[62:65], v[26:29], v[240:243], v[62:65]\n"
        "ds_read_b64_tr_b16 v[50:51], v1 offset:25680\n"
        "v_mfma_f32_16x16x32_bf16 v[66:69], v[26:29], v[244:247], v[66:69]\n"
        "ds_read_b64_tr_b16 v[52:53], v1 offset:28240\n"
        "s_waitcnt lgkmcnt(8)\n"
        "v_mfma_f32_16x16x32_bf16 v[70:73], v[30:33], v[240:243], v[70:73]\n"
        "ds_read_b64_tr_b16 v[26:27], v1 offset:25720\n"
        "v_mfma_f32_16x16x32_bf16 v[74:77], v[30:33], v[244:247], v[74:77]\n"
        "ds_read_b64_tr_b16 v[28:29], v1 offset:28280\n"
        "v_mfma_f32_16x16x32_bf16 v[78:81], v[34:37], v[240:243], v[78:81]\n"
        "ds_read_b64_tr_b16 v[30:31], v2 offset:25600\n"
        "v_mfma_f32_16x16x32_bf16 v[82:85], v[34:37], v[244:247], v[82:85]\n"
        "ds_read_b64_tr_b16 v[32:33], v2 offset:28160\n"
        "s_barrier\n"
        "s_waitcnt lgkmcnt(8)\n"
        "v_mfma_f32_16x16x32_bf16 v[86:89], v[38:41], v[240:243], v[86:89]\n"
        "ds_read_b64_tr_b16 v[34:35], v1 offset:30720\n"
        "v_mfma_f32_16x16x32_bf16 v[90:93], v[38:41], v[244:247], v[90:93]\n"
        "ds_read_b64_tr_b16 v[36:37], v1 offset:33280\n"
        "v_mfma_f32_16x16x32_bf16 v[54:57], v[42:45], v[248:251], v[54:57]\n"
        "ds_read_b64_tr_b16 v[38:39], v1 offset:30760\n"
        "v_mfma_f32_16x16x32_bf16 v[58:61], v[42:45], v[252:255], v[58:61]\n"
        "ds_read_b64_tr_b16 v[40:41], v1 offset:33320\n"
        "s_waitcnt lgkmcnt(8)\n"
        "v_mfma_f32_16x16x32_bf16 v[62:65], v[46:49], v[248:251], v[62:65]\n"
        "ds_read_b64_tr_b16 v[42:43], v1 offset:30800\n"
        "v_mfma_f32_16x16x32_bf16 v[66:69], v[46:49], v[252:255], v[66:69]\n"
        "ds_read_b64_tr_b16 v[44:45], v1 offset:33360\n"
        "v_mfma_f32_16x16x32_bf16 v[70:73], v[50:53], v[248:251], v[70:73]\n"
        "ds_read_b64_tr_b16 v[46:47], v1 offset:30840\n"
        "v_cvt_pk_bf16_f32 v18, v54, v55\n"
        "v_cvt_pk_bf16_f32 v19, v56, v57\n"
        "v_mfma_f32_16x16x32_bf16 v[74:77], v[50:53], v[252:255], v[74:77]\n"
        "ds_read_b64_tr_b16 v[48:49], v1 offset:33400\n"
        "ds_write_b64 v16, v[18:19] offset:0\n"
        "v_cvt_pk_bf16_f32 v20, v58, v59\n"
        "v_cvt_pk_bf16_f32 v21, v60, v61\n"
        "s_waitcnt lgkmcnt(9)\n"
        "s_nop 0\n"
        "v_mfma_f32_16x16x32_bf16 v[78:81], v[26:29], v[248:251], v[78:81]\n"
        "ds_read_b64_tr_b16 v[50:51], v2 offset:30720\n"
        "ds_write_b64 v16, v[20:21] offset:2816\n"
        "v_cvt_pk_bf16_f32 v22, v62, v63\n"
        "v_cvt_pk_bf16_f32 v23, v64, v65\n"
        "v_mfma_f32_16x16x32_bf16 v[82:85], v[26:29], v[252:255], v[82:85]\n"
        "ds_read_b64_tr_b16 v[52:53], v2 offset:33280\n"
        "ds_write_b64 v16, v[22:23] offset:40\n"
        "v_cvt_pk_bf16_f32 v24, v66, v67\n"
        "v_cvt_pk_bf16_f32 v25, v68, v69\n"
        "v_mfma_f32_16x16x32_bf16 v[86:89], v[30:33], v[248:251], v[86:89]\n"
        "ds_read_b64_tr_b16 v[26:27], v1 offset:35840\n"
        "ds_write_b64 v16, v[24:25] offset:2856\n"
        "v_cvt_pk_bf16_f32 v18, v70, v71\n"
        "v_cvt_pk_bf16_f32 v19, v72, v73\n"
        "v_mfma_f32_16x16x32_bf16 v[90:93], v[30:33], v[252:255], v[90:93]\n"
        "ds_read_b64_tr_b16 v[28:29], v1 offset:38400\n"
        "ds_write_b64 v16, v[18:19] offset:80\n"
        "v_cvt_pk_bf16_f32 v20, v74, v75\n"
        "v_cvt_pk_bf16_f32 v21, v76, v77\n"
        "s_waitcnt lgkmcnt(13)\n"
        "s_nop 0\n"
        "v_mfma_f32_16x16x32_bf16 v[54:57], v[34:37], v[96:99], 0\n"
        "ds_read_b64_tr_b16 v[30:31], v1 offset:35880\n"
        "ds_write_b64 v16, v[20:21] offset:2896\n"
        "v_cvt_pk_bf16_f32 v22, v78, v79\n"
        "v_cvt_pk_bf16_f32 v23, v80, v81\n"
        "v_mfma_f32_16x16x32_bf16 v[58:61], v[34:37], v[100:103], 0\n"
        "ds_read_b64_tr_b16 v[32:33], v1 offset:38440\n"
        "ds_write_b64 v16, v[22:23] offset:120\n"
        "v_cvt_pk_bf16_f32 v24, v82, v83\n"
        "v_cvt_pk_bf16_f32 v25, v84, v85\n"
        "v_mfma_f32_16x16x32_bf16 v[62:65], v[38:41], v[96:99], 0\n"
        "ds_read_b64_tr_b16 v[34:35], v1 offset:35920\n"
        "ds_write_b64 v16, v[24:25] offset:2936\n"
        "v_cvt_pk_bf16_f32 v18, v86, v87\n"
        "v_cvt_pk_bf16_f32 v19, v88, v89\n"
        "v_mfma_f32_16x16x32_bf16 v[66:69], v[38:41], v[100:103], 0\n"
        "ds_read_b64_tr_b16 v[36:37], v1 offset:38480\n"
        "ds_write_b64 v17, v[18:19] offset:0\n"
        "v_cvt_pk_bf16_f32 v20, v90, v91\n"
        "v_cvt_pk_bf16_f32 v21, v92, v93\n"
        "s_waitcnt lgkmcnt(15)\n"
        "v_mfma_f32_16x16x32_bf16 v[70:73], v[42:45], v[96:99], 0\n"
        "ds_read_b64_tr_b16 v[38:39], v1 offset:35960\n"
        "ds_write_b64 v17, v[20:21] offset:2816\n"
        "v_mfma_f32_16x16x32_bf16 v[74:77], v[42:45], v[100:103], 0\n"
        "ds_read_b64_tr_b16 v[40:41], v1 offset:38520\n"
        "v_mfma_f32_16x16x32_bf16 v[78:81], v[46:49], v[96:99], 0\n"
        "ds_read_b64_tr_b16 v[42:43], v2 offset:35840\n"
        "v_mfma_f32_16x16x32_bf16 v[82:85], v[46:49], v[100:103], 0\n"
        "ds_read_b64_tr_b16 v[44:45], v2 offset:38400\n"
        "s_waitcnt lgkmcnt(3)\n"
        "s_barrier\n"
        "v_mfma_f32_16x16x32_bf16 v[86:89], v[50:53], v[96:99], 0\n"
        "ds_read_b64_tr_b16 v[46:47], v13 offset:0\n"
        "v_mfma_f32_16x16x32_bf16 v[90:93], v[50:53], v[100:103], 0\n"
        "ds_read_b64_tr_b16 v[48:49], v13 offset:2560\n"
        "v_mfma_f32_16x16x32_bf16 v[54:57], v[26:29], v[104:107], v[54:57]\n"
        "ds_read_b64_tr_b16 v[50:51], v13 offset:40\n"
        "v_mfma_f32_16x16x32_bf16 v[58:61], v[26:29], v[108:111], v[58:61]\n"
        "ds_read_b64_tr_b16 v[52:53], v13 offset:2600\n"
        "v_mfma_f32_16x16x32_bf16 v[62:65], v[30:33], v[104:107], v[62:65]\n"
        "ds_read_b64_tr_b16 v[26:27], v13 offset:80\n"
        "v_mfma_f32_16x16x32_bf16 v[66:69], v[30:33], v[108:111], v[66:69]\n"
        "ds_read_b64_tr_b16 v[28:29], v13 offset:2640\n"
        "v_mfma_f32_16x16x32_bf16 v[70:73], v[34:37], v[104:107], v[70:73]\n"
        "ds_read_b64_tr_b16 v[30:31], v13 offset:120\n"
        "v_mfma_f32_16x16x32_bf16 v[74:77], v[34:37], v[108:111], v[74:77]\n"
        "ds_read_b64_tr_b16 v[32:33], v13 offset:2680\n"
        "s_waitcnt lgkmcnt(8)\n"
        "v_mfma_f32_16x16x32_bf16 v[78:81], v[38:41], v[104:107], v[78:81]\n"
        "ds_read_b64_tr_b16 v[34:35], v14 offset:0\n"
        "v_mfma_f32_16x16x32_bf16 v[82:85], v[38:41], v[108:111], v[82:85]\n"
        "ds_read_b64_tr_b16 v[36:37], v14 offset:2560\n"
        "v_mfma_f32_16x16x32_bf16 v[86:89], v[42:45], v[104:107], v[86:89]\n"
        "ds_read_b64_tr_b16 v[38:39], v13 offset:5120\n"
        "v_mfma_f32_16x16x32_bf16 v[90:93], v[42:45], v[108:111], v[90:93]\n"
        "ds_read_b64_tr_b16 v[40:41], v13 offset:7680\n"
        "s_waitcnt lgkmcnt(8)\n"
        "v_mfma_f32_16x16x32_bf16 v[54:57], v[46:49], v[112:115], v[54:57]\n"
        "ds_read_b64_tr_b16 v[42:43], v13 offset:5160\n"
        "v_mfma_f32_16x16x32_bf16 v[58:61], v[46:49], v[116:119], v[58:61]\n"
        "ds_read_b64_tr_b16 v[44:45], v13 offset:7720\n"
        "v_mfma_f32_16x16x32_bf16 v[62:65], v[50:53], v[112:115], v[62:65]\n"
        "ds_read_b64_tr_b16 v[46:47], v13 offset:5200\n"
        "v_mfma_f32_16x16x32_bf16 v[66:69], v[50:53], v[116:119], v[66:69]\n"
        "ds_read_b64_tr_b16 v[48:49], v13 offset:7760\n"
        "s_waitcnt lgkmcnt(8)\n"
        "v_mfma_f32_16x16x32_bf16 v[70:73], v[26:29], v[112:115], v[70:73]\n"
        "ds_read_b64_tr_b16 v[50:51], v13 offset:5240\n"
        "v_mfma_f32_16x16x32_bf16 v[74:77], v[26:29], v[116:119], v[74:77]\n"
        "ds_read_b64_tr_b16 v[52:53], v13 offset:7800\n"
        "v_mfma_f32_16x16x32_bf16 v[78:81], v[30:33], v[112:115], v[78:81]\n"
        "ds_read_b64_tr_b16 v[26:27], v14 offset:5120\n"
        "v_mfma_f32_16x16x32_bf16 v[82:85], v[30:33], v[116:119], v[82:85]\n"
        "ds_read_b64_tr_b16 v[28:29], v14 offset:7680\n"
        "s_barrier\n"
        "s_waitcnt lgkmcnt(8)\n"
        "v_mfma_f32_16x16x32_bf16 v[86:89], v[34:37], v[112:115], v[86:89]\n"
        "ds_read_b64_tr_b16 v[30:31], v13 offset:10240\n"
        "v_mfma_f32_16x16x32_bf16 v[90:93], v[34:37], v[116:119], v[90:93]\n"
        "ds_read_b64_tr_b16 v[32:33], v13 offset:12800\n"
        "v_mfma_f32_16x16x32_bf16 v[54:57], v[38:41], v[120:123], v[54:57]\n"
        "ds_read_b64_tr_b16 v[34:35], v13 offset:10280\n"
        "v_mfma_f32_16x16x32_bf16 v[58:61], v[38:41], v[124:127], v[58:61]\n"
        "ds_read_b64_tr_b16 v[36:37], v13 offset:12840\n"
        "s_waitcnt lgkmcnt(8)\n"
        "v_mfma_f32_16x16x32_bf16 v[62:65], v[42:45], v[120:123], v[62:65]\n"
        "ds_read_b64_tr_b16 v[38:39], v13 offset:10320\n"
        "v_mfma_f32_16x16x32_bf16 v[66:69], v[42:45], v[124:127], v[66:69]\n"
        "ds_read_b64_tr_b16 v[40:41], v13 offset:12880\n"
        "v_mfma_f32_16x16x32_bf16 v[70:73], v[46:49], v[120:123], v[70:73]\n"
        "ds_read_b64_tr_b16 v[42:43], v13 offset:10360\n"
        "v_mfma_f32_16x16x32_bf16 v[74:77], v[46:49], v[124:127], v[74:77]\n"
        "ds_read_b64_tr_b16 v[44:45], v13 offset:12920\n"
        "s_waitcnt lgkmcnt(8)\n"
        "v_mfma_f32_16x16x32_bf16 v[78:81], v[50:53], v[120:123], v[78:81]\n"
        "ds_read_b64_tr_b16 v[46:47], v14 offset:10240\n"
        "v_mfma_f32_16x16x32_bf16 v[82:85], v[50:53], v[124:127], v[82:85]\n"
        "ds_read_b64_tr_b16 v[48:49], v14 offset:12800\n"
        "v_mfma_f32_16x16x32_bf16 v[86:89], v[26:29], v[120:123], v[86:89]\n"
        "ds_read_b64_tr_b16 v[50:51], v13 offset:15360\n"
        "v_mfma_f32_16x16x32_bf16 v[90:93], v[26:29], v[124:127], v[90:93]\n"
        "ds_read_b64_tr_b16 v[52:53], v13 offset:17920\n"
        "s_waitcnt lgkmcnt(8)\n"
        "v_mfma_f32_16x16x32_bf16 v[54:57], v[30:33], v[128:131], v[54:57]\n"
        "ds_read_b64_tr_b16 v[26:27], v13 offset:15400\n"
        "v_mfma_f32_16x16x32_bf16 v[58:61], v[30:33], v[132:135], v[58:61]\n"
        "ds_read_b64_tr_b16 v[28:29], v13 offset:17960\n"
        "v_mfma_f32_16x16x32_bf16 v[62:65], v[34:37], v[128:131], v[62:65]\n"
        "ds_read_b64_tr_b16 v[30:31], v13 offset:15440\n"
        "v_mfma_f32_16x16x32_bf16 v[66:69], v[34:37], v[132:135], v[66:69]\n"
        "ds_read_b64_tr_b16 v[32:33], v13 offset:18000\n"
        "s_waitcnt lgkmcnt(8)\n"
        "v_mfma_f32_16x16x32_bf16 v[70:73], v[38:41], v[128:131], v[70:73]\n"
        "ds_read_b64_tr_b16 v[34:35], v13 offset:15480\n"
        "v_mfma_f32_16x16x32_bf16 v[74:77], v[38:41], v[132:135], v[74:77]\n"
        "ds_read_b64_tr_b16 v[36:37], v13 offset:18040\n"
        "v_mfma_f32_16x16x32_bf16 v[78:81], v[42:45], v[128:131], v[78:81]\n"
        "ds_read_b64_tr_b16 v[38:39], v14 offset:15360\n"
        "v_mfma_f32_16x16x32_bf16 v[82:85], v[42:45], v[132:135], v[82:85]\n"
        "ds_read_b64_tr_b16 v[40:41], v14 offset:17920\n"
        "s_waitcnt lgkmcnt(8)\n"
        "v_mfma_f32_16x16x32_bf16 v[86:89], v[46:49], v[128:131], v[86:89]\n"
        "ds_read_b64_tr_b16 v[42:43], v13 offset:20480\n"
        "v_mfma_f32_16x16x32_bf16 v[90:93], v[46:49], v[132:135], v[90:93]\n"
        "ds_read_b64_tr_b16 v[44:45], v13 offset:23040\n"
        "v_mfma_f32_16x16x32_bf16 v[54:57], v[50:53], v[136:139], v[54:57]\n"
        "ds_read_b64_tr_b16 v[46:47], v13 offset:20520\n"
        "v_mfma_f32_16x16x32_bf16 v[58:61], v[50:53], v[140:143], v[58:61]\n"
        "ds_read_b64_tr_b16 v[48:49], v13 offset:23080\n"
        "s_waitcnt lgkmcnt(8)\n"
        "v_mfma_f32_16x16x32_bf16 v[62:65], v[26:29], v[136:139], v[62:65]\n"
        "ds_read_b64_tr_b16 v[50:51], v13 offset:20560\n"
        "v_mfma_f32_16x16x32_bf16 v[66:69], v[26:29], v[140:143], v[66:69]\n"
        "ds_read_b64_tr_b16 v[52:53], v13 offset:23120\n"
        "v_mfma_f32_16x16x32_bf16 v[70:73], v[30:33], v[136:139], v[70:73]\n"
        "ds_read_b64_tr_b16 v[26:27], v13 offset:20600\n"
        "v_mfma_f32_16x16x32_bf16 v[74:77], v[30:33], v[140:143], v[74:77]\n"
        "ds_read_b64_tr_b16 v[28:29], v13 offset:23160\n"
        "s_waitcnt lgkmcnt(8)\n"
        "v_mfma_f32_16x16x32_bf16 v[78:81], v[34:37], v[136:139], v[78:81]\n"
        "ds_read_b64_tr_b16 v[30:31], v14 offset:20480\n"
        "v_mfma_f32_16x16x32_bf16 v[82:85], v[34:37], v[140:143], v[82:85]\n"
        "ds_read_b64_tr_b16 v[32:33], v14 offset:23040\n"
        "v_mfma_f32_16x16x32_bf16 v[86:89], v[38:41], v[136:139], v[86:89]\n"
        "ds_read_b64_tr_b16 v[34:35], v13 offset:25600\n"
        "v_mfma_f32_16x16x32_bf16 v[90:93], v[38:41], v[140:143], v[90:93]\n"
        "ds_read_b64_tr_b16 v[36:37], v13 offset:28160\n"
        "s_waitcnt lgkmcnt(8)\n"
        "v_mfma_f32_16x16x32_bf16 v[54:57], v[42:45], v[144:147], v[54:57]\n"
        "ds_read_b64_tr_b16 v[38:39], v13 offset:25640\n"
        "v_mfma_f32_16x16x32_bf16 v[58:61], v[42:45], v[148:151], v[58:61]\n"
        "ds_read_b64_tr_b16 v[40:41], v13 offset:28200\n"
        "v_mfma_f32_16x16x32_bf16 v[62:65], v[46:49], v[144:147], v[62:65]\n"
        "ds_read_b64_tr_b16 v[42:43], v13 offset:25680\n"
        "v_mfma_f32_16x16x32_bf16 v[66:69], v[46:49], v[148:151], v[66:69]\n"
        "ds_read_b64_tr_b16 v[44:45], v13 offset:28240\n"
        "s_waitcnt lgkmcnt(8)\n"
        "v_mfma_f32_16x16x32_bf16 v[70:73], v[50:53], v[144:147], v[70:73]\n"
        "ds_read_b64_tr_b16 v[46:47], v13 offset:25720\n"
        "v_mfma_f32_16x16x32_bf16 v[74:77], v[50:53], v[148:151], v[74:77]\n"
        "ds_read_b64_tr_b16 v[48:49], v13 offset:28280\n"
        "v_mfma_f32_16x16x32_bf16 v[78:81], v[26:29], v[144:147], v[78:81]\n"
        "ds_read_b64_tr_b16 v[50:51], v14 offset:25600\n"
        "v_mfma_f32_16x16x32_bf16 v[82:85], v[26:29], v[148:151], v[82:85]\n"
        "ds_read_b64_tr_b16 v[52:53], v14 offset:28160\n"
        "s_barrier\n"
        "s_waitcnt lgkmcnt(8)\n"
        "v_mfma_f32_16x16x32_bf16 v[86:89], v[30:33], v[144:147], v[86:89]\n"
        "ds_read_b64_tr_b16 v[26:27], v1 offset:0\n"
        "v_mfma_f32_16x16x32_bf16 v[90:93], v[30:33], v[148:151], v[90:93]\n"
        "ds_read_b64_tr_b16 v[28:29], v1 offset:2560\n"
        "v_mfma_f32_16x16x32_bf16 v[54:57], v[34:37], v[152:155], v[54:57]\n"
        "ds_read_b64_tr_b16 v[30:31], v1 offset:40\n"
        "v_mfma_f32_16x16x32_bf16 v[58:61], v[34:37], v[156:159], v[58:61]\n"
        "ds_read_b64_tr_b16 v[32:33], v1 offset:2600\n"
        "s_waitcnt lgkmcnt(8)\n"
        "v_mfma_f32_16x16x32_bf16 v[62:65], v[38:41], v[152:155], v[62:65]\n"
        "ds_read_b64_tr_b16 v[34:35], v1 offset:80\n"
        "v_mfma_f32_16x16x32_bf16 v[66:69], v[38:41], v[156:159], v[66:69]\n"
        "ds_read_b64_tr_b16 v[36:37], v1 offset:2640\n"
        "v_mfma_f32_16x16x32_bf16 v[70:73], v[42:45], v[152:155], v[70:73]\n"
        "ds_read_b64_tr_b16 v[38:39], v1 offset:120\n"
        "v_mfma_f32_16x16x32_bf16 v[74:77], v[42:45], v[156:159], v[74:77]\n"
        "ds_read_b64_tr_b16 v[40:41], v1 offset:2680\n"
        "s_waitcnt lgkmcnt(8)\n"
        "v_mfma_f32_16x16x32_bf16 v[78:81], v[46:49], v[152:155], v[78:81]\n"
        "ds_read_b64_tr_b16 v[42:43], v2 offset:0\n"
        "v_mfma_f32_16x16x32_bf16 v[82:85], v[46:49], v[156:159], v[82:85]\n"
        "ds_read_b64_tr_b16 v[44:45], v2 offset:2560\n"
        "v_mfma_f32_16x16x32_bf16 v[86:89], v[50:53], v[152:155], v[86:89]\n"
        "ds_read_b64_tr_b16 v[46:47], v1 offset:5120\n"
        "v_mfma_f32_16x16x32_bf16 v[90:93], v[50:53], v[156:159], v[90:93]\n"
        "ds_read_b64_tr_b16 v[48:49], v1 offset:7680\n"
        "s_waitcnt lgkmcnt(8)\n"
        "v_mfma_f32_16x16x32_bf16 v[54:57], v[26:29], v[160:163], v[54:57]\n"
        "ds_read_b64_tr_b16 v[50:51], v1 offset:5160\n"
        "v_mfma_f32_16x16x32_bf16 v[58:61], v[26:29], v[164:167], v[58:61]\n"
        "ds_read_b64_tr_b16 v[52:53], v1 offset:7720\n"
        "v_mfma_f32_16x16x32_bf16 v[62:65], v[30:33], v[160:163], v[62:65]\n"
        "ds_read_b64_tr_b16 v[26:27], v1 offset:5200\n"
        "v_mfma_f32_16x16x32_bf16 v[66:69], v[30:33], v[164:167], v[66:69]\n"
        "ds_read_b64_tr_b16 v[28:29], v1 offset:7760\n"
        "s_waitcnt lgkmcnt(8)\n"
        "v_mfma_f32_16x16x32_bf16 v[70:73], v[34:37], v[160:163], v[70:73]\n"
        "ds_read_b64_tr_b16 v[30:31], v1 offset:5240\n"
        "v_mfma_f32_16x16x32_bf16 v[74:77], v[34:37], v[164:167], v[74:77]\n"
        "ds_read_b64_tr_b16 v[32:33], v1 offset:7800\n"
        "v_mfma_f32_16x16x32_bf16 v[78:81], v[38:41], v[160:163], v[78:81]\n"
        "ds_read_b64_tr_b16 v[34:35], v2 offset:5120\n"
        "v_mfma_f32_16x16x32_bf16 v[82:85], v[38:41], v[164:167], v[82:85]\n"
        "ds_read_b64_tr_b16 v[36:37], v2 offset:7680\n"
        "s_waitcnt lgkmcnt(8)\n"
        "v_mfma_f32_16x16x32_bf16 v[86:89], v[42:45], v[160:163], v[86:89]\n"
        "ds_read_b64_tr_b16 v[38:39], v1 offset:10240\n"
        "v_mfma_f32_16x16x32_bf16 v[90:93], v[42:45], v[164:167], v[90:93]\n"
        "ds_read_b64_tr_b16 v[40:41], v1 offset:12800\n"
        "v_mfma_f32_16x16x32_bf16 v[54:57], v[46:49], v[168:171], v[54:57]\n"
        "ds_read_b64_tr_b16 v[42:43], v1 offset:10280\n"
        "v_mfma_f32_16x16x32_bf16 v[58:61], v[46:49], v[172:175], v[58:61]\n"
        "ds_read_b64_tr_b16 v[44:45], v1 offset:12840\n"
        "s_waitcnt lgkmcnt(8)\n"
        "v_mfma_f32_16x16x32_bf16 v[62:65], v[50:53], v[168:171], v[62:65]\n"
        "ds_read_b64_tr_b16 v[46:47], v1 offset:10320\n"
        "v_mfma_f32_16x16x32_bf16 v[66:69], v[50:53], v[172:175], v[66:69]\n"
        "ds_read_b64_tr_b16 v[48:49], v1 offset:12880\n"
        "v_mfma_f32_16x16x32_bf16 v[70:73], v[26:29], v[168:171], v[70:73]\n"
        "ds_read_b64_tr_b16 v[50:51], v1 offset:10360\n"
        "v_mfma_f32_16x16x32_bf16 v[74:77], v[26:29], v[172:175], v[74:77]\n"
        "ds_read_b64_tr_b16 v[52:53], v1 offset:12920\n"
        "s_waitcnt lgkmcnt(8)\n"
        "v_mfma_f32_16x16x32_bf16 v[78:81], v[30:33], v[168:171], v[78:81]\n"
        "ds_read_b64_tr_b16 v[26:27], v2 offset:10240\n"
        "v_mfma_f32_16x16x32_bf16 v[82:85], v[30:33], v[172:175], v[82:85]\n"
        "ds_read_b64_tr_b16 v[28:29], v2 offset:12800\n"
        "v_mfma_f32_16x16x32_bf16 v[86:89], v[34:37], v[168:171], v[86:89]\n"
        "ds_read_b64_tr_b16 v[30:31], v1 offset:15360\n"
        "v_mfma_f32_16x16x32_bf16 v[90:93], v[34:37], v[172:175], v[90:93]\n"
        "ds_read_b64_tr_b16 v[32:33], v1 offset:17920\n"
        "s_waitcnt lgkmcnt(8)\n"
        "v_mfma_f32_16x16x32_bf16 v[54:57], v[38:41], v[176:179], v[54:57]\n"
        "ds_read_b64_tr_b16 v[34:35], v1 offset:15400\n"
        "v_mfma_f32_16x16x32_bf16 v[58:61], v[38:41], v[180:183], v[58:61]\n"
        "ds_read_b64_tr_b16 v[36:37], v1 offset:17960\n"
        "v_mfma_f32_16x16x32_bf16 v[62:65], v[42:45], v[176:179], v[62:65]\n"
        "ds_read_b64_tr_b16 v[38:39], v1 offset:15440\n"
        "v_mfma_f32_16x16x32_bf16 v[66:69], v[42:45], v[180:183], v[66:69]\n"
        "ds_read_b64_tr_b16 v[40:41], v1 offset:18000\n"
        "s_waitcnt lgkmcnt(8)\n"
        "v_mfma_f32_16x16x32_bf16 v[70:73], v[46:49], v[176:179], v[70:73]\n"
        "ds_read_b64_tr_b16 v[42:43], v1 offset:15480\n"
        "v_mfma_f32_16x16x32_bf16 v[74:77], v[46:49], v[180:183], v[74:77]\n"
        "ds_read_b64_tr_b16 v[44:45], v1 offset:18040\n"
        "v_mfma_f32_16x16x32_bf16 v[78:81], v[50:53], v[176:179], v[78:81]\n"
        "ds_read_b64_tr_b16 v[46:47], v2 offset:15360\n"
        "v_mfma_f32_16x16x32_bf16 v[82:85], v[50:53], v[180:183], v[82:85]\n"
        "ds_read_b64_tr_b16 v[48:49], v2 offset:17920\n"
        "s_barrier\n"
        "s_waitcnt lgkmcnt(8)\n"
        "v_mfma_f32_16x16x32_bf16 v[86:89], v[26:29], v[176:179], v[86:89]\n"
        "ds_read_b64_tr_b16 v[50:51], v1 offset:20480\n"
        "v_mfma_f32_16x16x32_bf16 v[90:93], v[26:29], v[180:183], v[90:93]\n"
        "ds_read_b64_tr_b16 v[52:53], v1 offset:23040\n"
        "v_mfma_f32_16x16x32_bf16 v[54:57], v[30:33], v[184:187], v[54:57]\n"
        "ds_read_b64_tr_b16 v[26:27], v1 offset:20520\n"
        "v_mfma_f32_16x16x32_bf16 v[58:61], v[30:33], v[188:191], v[58:61]\n"
        "ds_read_b64_tr_b16 v[28:29], v1 offset:23080\n"
        "s_waitcnt lgkmcnt(8)\n"
        "v_mfma_f32_16x16x32_bf16 v[62:65], v[34:37], v[184:187], v[62:65]\n"
        "ds_read_b64_tr_b16 v[30:31], v1 offset:20560\n"
        "v_mfma_f32_16x16x32_bf16 v[66:69], v[34:37], v[188:191], v[66:69]\n"
        "ds_read_b64_tr_b16 v[32:33], v1 offset:23120\n"
        "v_mfma_f32_16x16x32_bf16 v[70:73], v[38:41], v[184:187], v[70:73]\n"
        "ds_read_b64_tr_b16 v[34:35], v1 offset:20600\n"
        "v_mfma_f32_16x16x32_bf16 v[74:77], v[38:41], v[188:191], v[74:77]\n"
        "ds_read_b64_tr_b16 v[36:37], v1 offset:23160\n"
        "s_waitcnt lgkmcnt(8)\n"
        "v_mfma_f32_16x16x32_bf16 v[78:81], v[42:45], v[184:187], v[78:81]\n"
        "ds_read_b64_tr_b16 v[38:39], v2 offset:20480\n"
        "v_mfma_f32_16x16x32_bf16 v[82:85], v[42:45], v[188:191], v[82:85]\n"
        "ds_read_b64_tr_b16 v[40:41], v2 offset:23040\n"
        "v_mfma_f32_16x16x32_bf16 v[86:89], v[46:49], v[184:187], v[86:89]\n"
        "ds_read_b64_tr_b16 v[42:43], v1 offset:25600\n"
        "v_mfma_f32_16x16x32_bf16 v[90:93], v[46:49], v[188:191], v[90:93]\n"
        "ds_read_b64_tr_b16 v[44:45], v1 offset:28160\n"
        "s_waitcnt lgkmcnt(8)\n"
        "v_mfma_f32_16x16x32_bf16 v[54:57], v[50:53], v[192:195], v[54:57]\n"
        "ds_read_b64_tr_b16 v[46:47], v1 offset:25640\n"
        "v_mfma_f32_16x16x32_bf16 v[58:61], v[50:53], v[196:199], v[58:61]\n"
        "ds_read_b64_tr_b16 v[48:49], v1 offset:28200\n"
        "v_mfma_f32_16x16x32_bf16 v[62:65], v[26:29], v[192:195], v[62:65]\n"
        "ds_read_b64_tr_b16 v[50:51], v1 offset:25680\n"
        "v_mfma_f32_16x16x32_bf16 v[66:69], v[26:29], v[196:199], v[66:69]\n"
        "ds_read_b64_tr_b16 v[52:53], v1 offset:28240\n"
        "s_waitcnt lgkmcnt(8)\n"
        "v_mfma_f32_16x16x32_bf16 v[70:73], v[30:33], v[192:195], v[70:73]\n"
        "ds_read_b64_tr_b16 v[26:27], v1 offset:25720\n"
        "v_mfma_f32_16x16x32_bf16 v[74:77], v[30:33], v[196:199], v[74:77]\n"
        "ds_read_b64_tr_b16 v[28:29], v1 offset:28280\n"
        "v_mfma_f32_16x16x32_bf16 v[78:81], v[34:37], v[192:195], v[78:81]\n"
        "ds_read_b64_tr_b16 v[30:31], v2 offset:25600\n"
        "v_mfma_f32_16x16x32_bf16 v[82:85], v[34:37], v[196:199], v[82:85]\n"
        "ds_read_b64_tr_b16 v[32:33], v2 offset:28160\n"
        "s_waitcnt lgkmcnt(8)\n"
        "v_mfma_f32_16x16x32_bf16 v[86:89], v[38:41], v[192:195], v[86:89]\n"
        "ds_read_b64_tr_b16 v[34:35], v1 offset:30720\n"
        "v_mfma_f32_16x16x32_bf16 v[90:93], v[38:41], v[196:199], v[90:93]\n"
        "ds_read_b64_tr_b16 v[36:37], v1 offset:33280\n"
        "v_mfma_f32_16x16x32_bf16 v[54:57], v[42:45], v[200:203], v[54:57]\n"
        "ds_read_b64_tr_b16 v[38:39], v1 offset:30760\n"
        "v_mfma_f32_16x16x32_bf16 v[58:61], v[42:45], v[204:207], v[58:61]\n"
        "ds_read_b64_tr_b16 v[40:41], v1 offset:33320\n"
        "s_waitcnt lgkmcnt(8)\n"
        "v_mfma_f32_16x16x32_bf16 v[62:65], v[46:49], v[200:203], v[62:65]\n"
        "ds_read_b64_tr_b16 v[42:43], v1 offset:30800\n"
        "v_mfma_f32_16x16x32_bf16 v[66:69], v[46:49], v[204:207], v[66:69]\n"
        "ds_read_b64_tr_b16 v[44:45], v1 offset:33360\n"
        "v_mfma_f32_16x16x32_bf16 v[70:73], v[50:53], v[200:203], v[70:73]\n"
        "ds_read_b64_tr_b16 v[46:47], v1 offset:30840\n"
        "v_mfma_f32_16x16x32_bf16 v[74:77], v[50:53], v[204:207], v[74:77]\n"
        "ds_read_b64_tr_b16 v[48:49], v1 offset:33400\n"
        "s_waitcnt lgkmcnt(8)\n"
        "v_mfma_f32_16x16x32_bf16 v[78:81], v[26:29], v[200:203], v[78:81]\n"
        "ds_read_b64_tr_b16 v[50:51], v2 offset:30720\n"
        "v_mfma_f32_16x16x32_bf16 v[82:85], v[26:29], v[204:207], v[82:85]\n"
        "ds_read_b64_tr_b16 v[52:53], v2 offset:33280\n"
        "v_mfma_f32_16x16x32_bf16 v[86:89], v[30:33], v[200:203], v[86:89]\n"
        "ds_read_b64_tr_b16 v[26:27], v1 offset:35840\n"
        "v_mfma_f32_16x16x32_bf16 v[90:93], v[30:33], v[204:207], v[90:93]\n"
        "ds_read_b64_tr_b16 v[28:29], v1 offset:38400\n"
        "s_waitcnt lgkmcnt(8)\n"
        "v_mfma_f32_16x16x32_bf16 v[54:57], v[34:37], v[208:211], v[54:57]\n"
        "ds_read_b64_tr_b16 v[30:31], v1 offset:35880\n"
        "v_mfma_f32_16x16x32_bf16 v[58:61], v[34:37], v[212:215], v[58:61]\n"
        "ds_read_b64_tr_b16 v[32:33], v1 offset:38440\n"
        "v_mfma_f32_16x16x32_bf16 v[62:65], v[38:41], v[208:211], v[62:65]\n"
        "ds_read_b64_tr_b16 v[34:35], v1 offset:35920\n"
        "v_mfma_f32_16x16x32_bf16 v[66:69], v[38:41], v[212:215], v[66:69]\n"
        "ds_read_b64_tr_b16 v[36:37], v1 offset:38480\n"
        "s_waitcnt lgkmcnt(8)\n"
        "v_mfma_f32_16x16x32_bf16 v[70:73], v[42:45], v[208:211], v[70:73]\n"
        "ds_read_b64_tr_b16 v[38:39], v1 offset:35960\n"
        "v_mfma_f32_16x16x32_bf16 v[74:77], v[42:45], v[212:215], v[74:77]\n"
        "ds_read_b64_tr_b16 v[40:41], v1 offset:38520\n"
        "v_mfma_f32_16x16x32_bf16 v[78:81], v[46:49], v[208:211], v[78:81]\n"
        "ds_read_b64_tr_b16 v[42:43], v2 offset:35840\n"
        "v_mfma_f32_16x16x32_bf16 v[82:85], v[46:49], v[212:215], v[82:85]\n"
        "ds_read_b64_tr_b16 v[44:45], v2 offset:38400\n"
        "s_barrier\n"
        "s_waitcnt lgkmcnt(8)\n"
        "v_mfma_f32_16x16x32_bf16 v[86:89], v[50:53], v[208:211], v[86:89]\n"
        "ds_read_b64_tr_b16 v[46:47], v13 offset:0\n"
        "v_mfma_f32_16x16x32_bf16 v[90:93], v[50:53], v[212:215], v[90:93]\n"
        "ds_read_b64_tr_b16 v[48:49], v13 offset:2560\n"
        "v_mfma_f32_16x16x32_bf16 v[54:57], v[26:29], v[216:219], v[54:57]\n"
        "ds_read_b64_tr_b16 v[50:51], v13 offset:40\n"
        "v_mfma_f32_16x16x32_bf16 v[58:61], v[26:29], v[220:223], v[58:61]\n"
        "ds_read_b64_tr_b16 v[52:53], v13 offset:2600\n"
        "s_waitcnt lgkmcnt(8)\n"
        "v_mfma_f32_16x16x32_bf16 v[62:65], v[30:33], v[216:219], v[62:65]\n"
        "ds_read_b64_tr_b16 v[26:27], v13 offset:80\n"
        "v_mfma_f32_16x16x32_bf16 v[66:69], v[30:33], v[220:223], v[66:69]\n"
        "ds_read_b64_tr_b16 v[28:29], v13 offset:2640\n"
        "v_mfma_f32_16x16x32_bf16 v[70:73], v[34:37], v[216:219], v[70:73]\n"
        "ds_read_b64_tr_b16 v[30:31], v13 offset:120\n"
        "v_mfma_f32_16x16x32_bf16 v[74:77], v[34:37], v[220:223], v[74:77]\n"
        "ds_read_b64_tr_b16 v[32:33], v13 offset:2680\n"
        "s_waitcnt lgkmcnt(8)\n"
        "v_mfma_f32_16x16x32_bf16 v[78:81], v[38:41], v[216:219], v[78:81]\n"
        "ds_read_b64_tr_b16 v[34:35], v14 offset:0\n"
        "v_mfma_f32_16x16x32_bf16 v[82:85], v[38:41], v[220:223], v[82:85]\n"
        "ds_read_b64_tr_b16 v[36:37], v14 offset:2560\n"
        "v_mfma_f32_16x16x32_bf16 v[86:89], v[42:45], v[216:219], v[86:89]\n"
        "ds_read_b64_tr_b16 v[38:39], v13 offset:5120\n"
        "v_mfma_f32_16x16x32_bf16 v[90:93], v[42:45], v[220:223], v[90:93]\n"
        "ds_read_b64_tr_b16 v[40:41], v13 offset:7680\n"
        "s_waitcnt lgkmcnt(8)\n"
        "v_mfma_f32_16x16x32_bf16 v[54:57], v[46:49], v[224:227], v[54:57]\n"
        "ds_read_b64_tr_b16 v[42:43], v13 offset:5160\n"
        "v_mfma_f32_16x16x32_bf16 v[58:61], v[46:49], v[228:231], v[58:61]\n"
        "ds_read_b64_tr_b16 v[44:45], v13 offset:7720\n"
        "v_mfma_f32_16x16x32_bf16 v[62:65], v[50:53], v[224:227], v[62:65]\n"
        "ds_read_b64_tr_b16 v[46:47], v13 offset:5200\n"
        "v_mfma_f32_16x16x32_bf16 v[66:69], v[50:53], v[228:231], v[66:69]\n"
        "ds_read_b64_tr_b16 v[48:49], v13 offset:7760\n"
        "s_waitcnt lgkmcnt(8)\n"
        "v_mfma_f32_16x16x32_bf16 v[70:73], v[26:29], v[224:227], v[70:73]\n"
        "ds_read_b64_tr_b16 v[50:51], v13 offset:5240\n"
        "v_mfma_f32_16x16x32_bf16 v[74:77], v[26:29], v[228:231], v[74:77]\n"
        "ds_read_b64_tr_b16 v[52:53], v13 offset:7800\n"
        "v_mfma_f32_16x16x32_bf16 v[78:81], v[30:33], v[224:227], v[78:81]\n"
        "ds_read_b64_tr_b16 v[26:27], v14 offset:5120\n"
        "v_mfma_f32_16x16x32_bf16 v[82:85], v[30:33], v[228:231], v[82:85]\n"
        "ds_read_b64_tr_b16 v[28:29], v14 offset:7680\n"
        "s_waitcnt lgkmcnt(8)\n"
        "v_mfma_f32_16x16x32_bf16 v[86:89], v[34:37], v[224:227], v[86:89]\n"
        "ds_read_b64_tr_b16 v[30:31], v13 offset:10240\n"
        "v_mfma_f32_16x16x32_bf16 v[90:93], v[34:37], v[228:231], v[90:93]\n"
        "ds_read_b64_tr_b16 v[32:33], v13 offset:12800\n"
        "v_mfma_f32_16x16x32_bf16 v[54:57], v[38:41], v[232:235], v[54:57]\n"
        "ds_read_b64_tr_b16 v[34:35], v13 offset:10280\n"
        "v_mfma_f32_16x16x32_bf16 v[58:61], v[38:41], v[236:239], v[58:61]\n"
        "ds_read_b64_tr_b16 v[36:37], v13 offset:12840\n"
        "s_waitcnt lgkmcnt(8)\n"
        "v_mfma_f32_16x16x32_bf16 v[62:65], v[42:45], v[232:235], v[62:65]\n"
        "ds_read_b64_tr_b16 v[38:39], v13 offset:10320\n"
        "v_mfma_f32_16x16x32_bf16 v[66:69], v[42:45], v[236:239], v[66:69]\n"
        "ds_read_b64_tr_b16 v[40:41], v13 offset:12880\n"
        "v_mfma_f32_16x16x32_bf16 v[70:73], v[46:49], v[232:235], v[70:73]\n"
        "ds_read_b64_tr_b16 v[42:43], v13 offset:10360\n"
        "v_mfma_f32_16x16x32_bf16 v[74:77], v[46:49], v[236:239], v[74:77]\n"
        "ds_read_b64_tr_b16 v[44:45], v13 offset:12920\n"
        "s_waitcnt lgkmcnt(8)\n"
        "v_mfma_f32_16x16x32_bf16 v[78:81], v[50:53], v[232:235], v[78:81]\n"
        "ds_read_b64_tr_b16 v[46:47], v14 offset:10240\n"
        "v_mfma_f32_16x16x32_bf16 v[82:85], v[50:53], v[236:239], v[82:85]\n"
        "ds_read_b64_tr_b16 v[48:49], v14 offset:12800\n"
        "v_mfma_f32_16x16x32_bf16 v[86:89], v[26:29], v[232:235], v[86:89]\n"
        "ds_read_b64_tr_b16 v[50:51], v13 offset:15360\n"
        "v_mfma_f32_16x16x32_bf16 v[90:93], v[26:29], v[236:239], v[90:93]\n"
        "ds_read_b64_tr_b16 v[52:53], v13 offset:17920\n"
        "s_waitcnt lgkmcnt(8)\n"
        "v_mfma_f32_16x16x32_bf16 v[54:57], v[30:33], v[240:243], v[54:57]\n"
        "ds_read_b64_tr_b16 v[26:27], v13 offset:15400\n"
        "v_mfma_f32_16x16x32_bf16 v[58:61], v[30:33], v[244:247], v[58:61]\n"
        "ds_read_b64_tr_b16 v[28:29], v13 offset:17960\n"
        "v_mfma_f32_16x16x32_bf16 v[62:65], v[34:37], v[240:243], v[62:65]\n"
        "ds_read_b64_tr_b16 v[30:31], v13 offset:15440\n"
        "v_mfma_f32_16x16x32_bf16 v[66:69], v[34:37], v[244:247], v[66:69]\n"
        "ds_read_b64_tr_b16 v[32:33], v13 offset:18000\n"
        "s_waitcnt lgkmcnt(8)\n"
        "v_mfma_f32_16x16x32_bf16 v[70:73], v[38:41], v[240:243], v[70:73]\n"
        "ds_read_b64_tr_b16 v[34:35], v13 offset:15480\n"
        "v_mfma_f32_16x16x32_bf16 v[74:77], v[38:41], v[244:247], v[74:77]\n"
        "ds_read_b64_tr_b16 v[36:37], v13 offset:18040\n"
        "v_mfma_f32_16x16x32_bf16 v[78:81], v[42:45], v[240:243], v[78:81]\n"
        "ds_read_b64_tr_b16 v[38:39], v14 offset:15360\n"
        "v_mfma_f32_16x16x32_bf16 v[82:85], v[42:45], v[244:247], v[82:85]\n"
        "ds_read_b64_tr_b16 v[40:41], v14 offset:17920\n"
        "s_barrier\n"
        "s_waitcnt lgkmcnt(8)\n"
        "v_mfma_f32_16x16x32_bf16 v[86:89], v[46:49], v[240:243], v[86:89]\n"
        "ds_read_b64_tr_b16 v[42:43], v13 offset:20480\n"
        "v_mfma_f32_16x16x32_bf16 v[90:93], v[46:49], v[244:247], v[90:93]\n"
        "ds_read_b64_tr_b16 v[44:45], v13 offset:23040\n"
        "v_mfma_f32_16x16x32_bf16 v[54:57], v[50:53], v[248:251], v[54:57]\n"
        "ds_read_b64_tr_b16 v[46:47], v13 offset:20520\n"
        "v_mfma_f32_16x16x32_bf16 v[58:61], v[50:53], v[252:255], v[58:61]\n"
        "ds_read_b64_tr_b16 v[48:49], v13 offset:23080\n"
        "s_waitcnt lgkmcnt(8)\n"
        "v_mfma_f32_16x16x32_bf16 v[62:65], v[26:29], v[248:251], v[62:65]\n"
        "ds_read_b64_tr_b16 v[50:51], v13 offset:20560\n"
        "v_mfma_f32_16x16x32_bf16 v[66:69], v[26:29], v[252:255], v[66:69]\n"
        "ds_read_b64_tr_b16 v[52:53], v13 offset:23120\n"
        "v_mfma_f32_16x16x32_bf16 v[70:73], v[30:33], v[248:251], v[70:73]\n"
        "ds_read_b64_tr_b16 v[26:27], v13 offset:20600\n"
        "v_cvt_pk_bf16_f32 v18, v54, v55\n"
        "v_cvt_pk_bf16_f32 v19, v56, v57\n"
        "v_mfma_f32_16x16x32_bf16 v[74:77], v[30:33], v[252:255], v[74:77]\n"
        "ds_read_b64_tr_b16 v[28:29], v13 offset:23160\n"
        "ds_write_b64 v16, v[18:19] offset:0\n"
        "v_cvt_pk_bf16_f32 v20, v58, v59\n"
        "v_cvt_pk_bf16_f32 v21, v60, v61\n"
        "s_waitcnt lgkmcnt(9)\n"
        "v_mfma_f32_16x16x32_bf16 v[78:81], v[34:37], v[248:251], v[78:81]\n"
        "ds_read_b64_tr_b16 v[30:31], v14 offset:20480\n"
        "ds_write_b64 v16, v[20:21] offset:2816\n"
        "v_cvt_pk_bf16_f32 v22, v62, v63\n"
        "v_cvt_pk_bf16_f32 v23, v64, v65\n"
        "v_mfma_f32_16x16x32_bf16 v[82:85], v[34:37], v[252:255], v[82:85]\n"
        "ds_read_b64_tr_b16 v[32:33], v14 offset:23040\n"
        "ds_write_b64 v16, v[22:23] offset:40\n"
        "v_cvt_pk_bf16_f32 v24, v66, v67\n"
        "v_cvt_pk_bf16_f32 v25, v68, v69\n"
        "v_mfma_f32_16x16x32_bf16 v[86:89], v[38:41], v[248:251], v[86:89]\n"
        "ds_read_b64_tr_b16 v[34:35], v13 offset:25600\n"
        "ds_write_b64 v16, v[24:25] offset:2856\n"
        "v_cvt_pk_bf16_f32 v18, v70, v71\n"
        "v_cvt_pk_bf16_f32 v19, v72, v73\n"
        "v_mfma_f32_16x16x32_bf16 v[90:93], v[38:41], v[252:255], v[90:93]\n"
        "ds_read_b64_tr_b16 v[36:37], v13 offset:28160\n"
        "ds_write_b64 v16, v[18:19] offset:80\n"
        "v_cvt_pk_bf16_f32 v20, v74, v75\n"
        "v_cvt_pk_bf16_f32 v21, v76, v77\n"
        "s_waitcnt lgkmcnt(13)\n"
        "s_nop 0\n"
        "v_mfma_f32_16x16x32_bf16 v[54:57], v[42:45], v[96:99], 0\n"
        "ds_read_b64_tr_b16 v[38:39], v13 offset:25640\n"
        "ds_write_b64 v16, v[20:21] offset:2896\n"
        "v_cvt_pk_bf16_f32 v22, v78, v79\n"
        "v_cvt_pk_bf16_f32 v23, v80, v81\n"
        "v_mfma_f32_16x16x32_bf16 v[58:61], v[42:45], v[100:103], 0\n"
        "ds_read_b64_tr_b16 v[40:41], v13 offset:28200\n"
        "ds_write_b64 v16, v[22:23] offset:120\n"
        "v_cvt_pk_bf16_f32 v24, v82, v83\n"
        "v_cvt_pk_bf16_f32 v25, v84, v85\n"
        "v_mfma_f32_16x16x32_bf16 v[62:65], v[46:49], v[96:99], 0\n"
        "ds_read_b64_tr_b16 v[42:43], v13 offset:25680\n"
        "ds_write_b64 v16, v[24:25] offset:2936\n"
        "v_cvt_pk_bf16_f32 v18, v86, v87\n"
        "v_cvt_pk_bf16_f32 v19, v88, v89\n"
        "v_mfma_f32_16x16x32_bf16 v[66:69], v[46:49], v[100:103], 0\n"
        "ds_read_b64_tr_b16 v[44:45], v13 offset:28240\n"
        "ds_write_b64 v17, v[18:19] offset:0\n"
        "v_cvt_pk_bf16_f32 v20, v90, v91\n"
        "v_cvt_pk_bf16_f32 v21, v92, v93\n"
        "s_waitcnt lgkmcnt(15)\n"
        "v_mfma_f32_16x16x32_bf16 v[70:73], v[50:53], v[96:99], 0\n"
        "ds_read_b64_tr_b16 v[46:47], v13 offset:25720\n"
        "ds_write_b64 v17, v[20:21] offset:2816\n"
        "v_mfma_f32_16x16x32_bf16 v[74:77], v[50:53], v[100:103], 0\n"
        "ds_read_b64_tr_b16 v[48:49], v13 offset:28280\n"
        "v_mfma_f32_16x16x32_bf16 v[78:81], v[26:29], v[96:99], 0\n"
        "ds_read_b64_tr_b16 v[50:51], v14 offset:25600\n"
        "v_mfma_f32_16x16x32_bf16 v[82:85], v[26:29], v[100:103], 0\n"
        "ds_read_b64_tr_b16 v[52:53], v14 offset:28160\n"
        "s_waitcnt lgkmcnt(3)\n"
        "s_barrier\n"
        "v_mfma_f32_16x16x32_bf16 v[86:89], v[30:33], v[96:99], 0\n"
        "ds_read_b64_tr_b16 v[26:27], v1 offset:0\n"
        "v_mfma_f32_16x16x32_bf16 v[90:93], v[30:33], v[100:103], 0\n"
        "ds_read_b64_tr_b16 v[28:29], v1 offset:2560\n"
        "v_mfma_f32_16x16x32_bf16 v[54:57], v[34:37], v[104:107], v[54:57]\n"
        "ds_read_b64_tr_b16 v[30:31], v1 offset:40\n"
        "v_mfma_f32_16x16x32_bf16 v[58:61], v[34:37], v[108:111], v[58:61]\n"
        "ds_read_b64_tr_b16 v[32:33], v1 offset:2600\n"
        "v_mfma_f32_16x16x32_bf16 v[62:65], v[38:41], v[104:107], v[62:65]\n"
        "ds_read_b64_tr_b16 v[34:35], v1 offset:80\n"
        "v_mfma_f32_16x16x32_bf16 v[66:69], v[38:41], v[108:111], v[66:69]\n"
        "ds_read_b64_tr_b16 v[36:37], v1 offset:2640\n"
        "v_mfma_f32_16x16x32_bf16 v[70:73], v[42:45], v[104:107], v[70:73]\n"
        "ds_read_b64_tr_b16 v[38:39], v1 offset:120\n"
        "v_mfma_f32_16x16x32_bf16 v[74:77], v[42:45], v[108:111], v[74:77]\n"
        "ds_read_b64_tr_b16 v[40:41], v1 offset:2680\n"
        "s_waitcnt lgkmcnt(8)\n"
        "v_mfma_f32_16x16x32_bf16 v[78:81], v[46:49], v[104:107], v[78:81]\n"
        "ds_read_b64_tr_b16 v[42:43], v2 offset:0\n"
        "v_mfma_f32_16x16x32_bf16 v[82:85], v[46:49], v[108:111], v[82:85]\n"
        "ds_read_b64_tr_b16 v[44:45], v2 offset:2560\n"
        "v_mfma_f32_16x16x32_bf16 v[86:89], v[50:53], v[104:107], v[86:89]\n"
        "ds_read_b64_tr_b16 v[46:47], v1 offset:5120\n"
        "v_mfma_f32_16x16x32_bf16 v[90:93], v[50:53], v[108:111], v[90:93]\n"
        "ds_read_b64_tr_b16 v[48:49], v1 offset:7680\n"
        "s_waitcnt lgkmcnt(8)\n"
        "v_mfma_f32_16x16x32_bf16 v[54:57], v[26:29], v[112:115], v[54:57]\n"
        "ds_read_b64_tr_b16 v[50:51], v1 offset:5160\n"
        "v_mfma_f32_16x16x32_bf16 v[58:61], v[26:29], v[116:119], v[58:61]\n"
        "ds_read_b64_tr_b16 v[52:53], v1 offset:7720\n"
        "v_mfma_f32_16x16x32_bf16 v[62:65], v[30:33], v[112:115], v[62:65]\n"
        "ds_read_b64_tr_b16 v[26:27], v1 offset:5200\n"
        "v_mfma_f32_16x16x32_bf16 v[66:69], v[30:33], v[116:119], v[66:69]\n"
        "ds_read_b64_tr_b16 v[28:29], v1 offset:7760\n"
        "s_waitcnt lgkmcnt(8)\n"
        "v_mfma_f32_16x16x32_bf16 v[70:73], v[34:37], v[112:115], v[70:73]\n"
        "ds_read_b64_tr_b16 v[30:31], v1 offset:5240\n"
        "v_mfma_f32_16x16x32_bf16 v[74:77], v[34:37], v[116:119], v[74:77]\n"
        "ds_read_b64_tr_b16 v[32:33], v1 offset:7800\n"
        "v_mfma_f32_16x16x32_bf16 v[78:81], v[38:41], v[112:115], v[78:81]\n"
        "ds_read_b64_tr_b16 v[34:35], v2 offset:5120\n"
        "v_mfma_f32_16x16x32_bf16 v[82:85], v[38:41], v[116:119], v[82:85]\n"
        "ds_read_b64_tr_b16 v[36:37], v2 offset:7680\n"
        "s_waitcnt lgkmcnt(8)\n"
        "v_mfma_f32_16x16x32_bf16 v[86:89], v[42:45], v[112:115], v[86:89]\n"
        "ds_read_b64_tr_b16 v[38:39], v1 offset:10240\n"
        "v_mfma_f32_16x16x32_bf16 v[90:93], v[42:45], v[116:119], v[90:93]\n"
        "ds_read_b64_tr_b16 v[40:41], v1 offset:12800\n"
        "v_mfma_f32_16x16x32_bf16 v[54:57], v[46:49], v[120:123], v[54:57]\n"
        "ds_read_b64_tr_b16 v[42:43], v1 offset:10280\n"
        "v_mfma_f32_16x16x32_bf16 v[58:61], v[46:49], v[124:127], v[58:61]\n"
        "ds_read_b64_tr_b16 v[44:45], v1 offset:12840\n"
        "s_waitcnt lgkmcnt(8)\n"
        "v_mfma_f32_16x16x32_bf16 v[62:65], v[50:53], v[120:123], v[62:65]\n"
        "ds_read_b64_tr_b16 v[46:47], v1 offset:10320\n"
        "v_mfma_f32_16x16x32_bf16 v[66:69], v[50:53], v[124:127], v[66:69]\n"
        "ds_read_b64_tr_b16 v[48:49], v1 offset:12880\n"
        "v_mfma_f32_16x16x32_bf16 v[70:73], v[26:29], v[120:123], v[70:73]\n"
        "ds_read_b64_tr_b16 v[50:51], v1 offset:10360\n"
        "v_mfma_f32_16x16x32_bf16 v[74:77], v[26:29], v[124:127], v[74:77]\n"
        "ds_read_b64_tr_b16 v[52:53], v1 offset:12920\n"
        "s_waitcnt lgkmcnt(8)\n"
        "v_mfma_f32_16x16x32_bf16 v[78:81], v[30:33], v[120:123], v[78:81]\n"
        "ds_read_b64_tr_b16 v[26:27], v2 offset:10240\n"
        "v_mfma_f32_16x16x32_bf16 v[82:85], v[30:33], v[124:127], v[82:85]\n"
        "ds_read_b64_tr_b16 v[28:29], v2 offset:12800\n"
        "v_mfma_f32_16x16x32_bf16 v[86:89], v[34:37], v[120:123], v[86:89]\n"
        "ds_read_b64_tr_b16 v[30:31], v1 offset:15360\n"
        "v_mfma_f32_16x16x32_bf16 v[90:93], v[34:37], v[124:127], v[90:93]\n"
        "ds_read_b64_tr_b16 v[32:33], v1 offset:17920\n"
        "s_waitcnt lgkmcnt(8)\n"
        "v_mfma_f32_16x16x32_bf16 v[54:57], v[38:41], v[128:131], v[54:57]\n"
        "ds_read_b64_tr_b16 v[34:35], v1 offset:15400\n"
        "v_mfma_f32_16x16x32_bf16 v[58:61], v[38:41], v[132:135], v[58:61]\n"
        "ds_read_b64_tr_b16 v[36:37], v1 offset:17960\n"
        "v_mfma_f32_16x16x32_bf16 v[62:65], v[42:45], v[128:131], v[62:65]\n"
        "ds_read_b64_tr_b16 v[38:39], v1 offset:15440\n"
        "v_mfma_f32_16x16x32_bf16 v[66:69], v[42:45], v[132:135], v[66:69]\n"
        "ds_read_b64_tr_b16 v[40:41], v1 offset:18000\n"
        "s_waitcnt lgkmcnt(8)\n"
        "v_mfma_f32_16x16x32_bf16 v[70:73], v[46:49], v[128:131], v[70:73]\n"
        "ds_read_b64_tr_b16 v[42:43], v1 offset:15480\n"
        "v_mfma_f32_16x16x32_bf16 v[74:77], v[46:49], v[132:135], v[74:77]\n"
        "ds_read_b64_tr_b16 v[44:45], v1 offset:18040\n"
        "v_mfma_f32_16x16x32_bf16 v[78:81], v[50:53], v[128:131], v[78:81]\n"
        "ds_read_b64_tr_b16 v[46:47], v2 offset:15360\n"
        "v_mfma_f32_16x16x32_bf16 v[82:85], v[50:53], v[132:135], v[82:85]\n"
        "ds_read_b64_tr_b16 v[48:49], v2 offset:17920\n"
        "s_barrier\n"
        "s_waitcnt lgkmcnt(8)\n"
        "v_mfma_f32_16x16x32_bf16 v[86:89], v[26:29], v[128:131], v[86:89]\n"
        "ds_read_b64_tr_b16 v[50:51], v1 offset:20480\n"
        "v_mfma_f32_16x16x32_bf16 v[90:93], v[26:29], v[132:135], v[90:93]\n"
        "ds_read_b64_tr_b16 v[52:53], v1 offset:23040\n"
        "v_mfma_f32_16x16x32_bf16 v[54:57], v[30:33], v[136:139], v[54:57]\n"
        "ds_read_b64_tr_b16 v[26:27], v1 offset:20520\n"
        "v_mfma_f32_16x16x32_bf16 v[58:61], v[30:33], v[140:143], v[58:61]\n"
        "ds_read_b64_tr_b16 v[28:29], v1 offset:23080\n"
        "s_waitcnt lgkmcnt(8)\n"
        "v_mfma_f32_16x16x32_bf16 v[62:65], v[34:37], v[136:139], v[62:65]\n"
        "ds_read_b64_tr_b16 v[30:31], v1 offset:20560\n"
        "v_mfma_f32_16x16x32_bf16 v[66:69], v[34:37], v[140:143], v[66:69]\n"
        "ds_read_b64_tr_b16 v[32:33], v1 offset:23120\n"
        "v_mfma_f32_16x16x32_bf16 v[70:73], v[38:41], v[136:139], v[70:73]\n"
        "ds_read_b64_tr_b16 v[34:35], v1 offset:20600\n"
        "v_mfma_f32_16x16x32_bf16 v[74:77], v[38:41], v[140:143], v[74:77]\n"
        "ds_read_b64_tr_b16 v[36:37], v1 offset:23160\n"
        "s_waitcnt lgkmcnt(8)\n"
        "v_mfma_f32_16x16x32_bf16 v[78:81], v[42:45], v[136:139], v[78:81]\n"
        "ds_read_b64_tr_b16 v[38:39], v2 offset:20480\n"
        "v_mfma_f32_16x16x32_bf16 v[82:85], v[42:45], v[140:143], v[82:85]\n"
        "ds_read_b64_tr_b16 v[40:41], v2 offset:23040\n"
        "v_mfma_f32_16x16x32_bf16 v[86:89], v[46:49], v[136:139], v[86:89]\n"
        "ds_read_b64_tr_b16 v[42:43], v1 offset:25600\n"
        "v_mfma_f32_16x16x32_bf16 v[90:93], v[46:49], v[140:143], v[90:93]\n"
        "ds_read_b64_tr_b16 v[44:45], v1 offset:28160\n"
        "s_waitcnt lgkmcnt(8)\n"
        "v_mfma_f32_16x16x32_bf16 v[54:57], v[50:53], v[144:147], v[54:57]\n"
        "ds_read_b64_tr_b16 v[46:47], v1 offset:25640\n"
        "v_mfma_f32_16x16x32_bf16 v[58:61], v[50:53], v[148:151], v[58:61]\n"
        "ds_read_b64_tr_b16 v[48:49], v1 offset:28200\n"
        "v_mfma_f32_16x16x32_bf16 v[62:65], v[26:29], v[144:147], v[62:65]\n"
        "ds_read_b64_tr_b16 v[50:51], v1 offset:25680\n"
        "v_mfma_f32_16x16x32_bf16 v[66:69], v[26:29], v[148:151], v[66:69]\n"
        "ds_read_b64_tr_b16 v[52:53], v1 offset:28240\n"
        "s_waitcnt lgkmcnt(8)\n"
        "v_mfma_f32_16x16x32_bf16 v[70:73], v[30:33], v[144:147], v[70:73]\n"
        "ds_read_b64_tr_b16 v[26:27], v1 offset:25720\n"
        "v_mfma_f32_16x16x32_bf16 v[74:77], v[30:33], v[148:151], v[74:77]\n"
        "ds_read_b64_tr_b16 v[28:29], v1 offset:28280\n"
        "v_mfma_f32_16x16x32_bf16 v[78:81], v[34:37], v[144:147], v[78:81]\n"
        "ds_read_b64_tr_b16 v[30:31], v2 offset:25600\n"
        "v_mfma_f32_16x16x32_bf16 v[82:85], v[34:37], v[148:151], v[82:85]\n"
        "ds_read_b64_tr_b16 v[32:33], v2 offset:28160\n"
        "s_waitcnt lgkmcnt(8)\n"
        "v_mfma_f32_16x16x32_bf16 v[86:89], v[38:41], v[144:147], v[86:89]\n"
        "ds_read_b64_tr_b16 v[34:35], v1 offset:30720\n"
        "v_mfma_f32_16x16x32_bf16 v[90:93], v[38:41], v[148:151], v[90:93]\n"
        "ds_read_b64_tr_b16 v[36:37], v1 offset:33280\n"
        "v_mfma_f32_16x16x32_bf16 v[54:57], v[42:45], v[152:155], v[54:57]\n"
        "ds_read_b64_tr_b16 v[38:39], v1 offset:30760\n"
        "v_mfma_f32_16x16x32_bf16 v[58:61], v[42:45], v[156:159], v[58:61]\n"
        "ds_read_b64_tr_b16 v[40:41], v1 offset:33320\n"
        "s_waitcnt lgkmcnt(8)\n"
        "v_mfma_f32_16x16x32_bf16 v[62:65], v[46:49], v[152:155], v[62:65]\n"
        "ds_read_b64_tr_b16 v[42:43], v1 offset:30800\n"
        "v_mfma_f32_16x16x32_bf16 v[66:69], v[46:49], v[156:159], v[66:69]\n"
        "ds_read_b64_tr_b16 v[44:45], v1 offset:33360\n"
        "v_mfma_f32_16x16x32_bf16 v[70:73], v[50:53], v[152:155], v[70:73]\n"
        "ds_read_b64_tr_b16 v[46:47], v1 offset:30840\n"
        "v_mfma_f32_16x16x32_bf16 v[74:77], v[50:53], v[156:159], v[74:77]\n"
        "ds_read_b64_tr_b16 v[48:49], v1 offset:33400\n"
        "s_waitcnt lgkmcnt(8)\n"
        "v_mfma_f32_16x16x32_bf16 v[78:81], v[26:29], v[152:155], v[78:81]\n"
        "ds_read_b64_tr_b16 v[50:51], v2 offset:30720\n"
        "v_mfma_f32_16x16x32_bf16 v[82:85], v[26:29], v[156:159], v[82:85]\n"
        "ds_read_b64_tr_b16 v[52:53], v2 offset:33280\n"
        "v_mfma_f32_16x16x32_bf16 v[86:89], v[30:33], v[152:155], v[86:89]\n"
        "ds_read_b64_tr_b16 v[26:27], v1 offset:35840\n"
        "v_mfma_f32_16x16x32_bf16 v[90:93], v[30:33], v[156:159], v[90:93]\n"
        "ds_read_b64_tr_b16 v[28:29], v1 offset:38400\n"
        "s_waitcnt lgkmcnt(8)\n"
        "v_mfma_f32_16x16x32_bf16 v[54:57], v[34:37], v[160:163], v[54:57]\n"
        "ds_read_b64_tr_b16 v[30:31], v1 offset:35880\n"
        "v_mfma_f32_16x16x32_bf16 v[58:61], v[34:37], v[164:167], v[58:61]\n"
        "ds_read_b64_tr_b16 v[32:33], v1 offset:38440\n"
        "v_mfma_f32_16x16x32_bf16 v[62:65], v[38:41], v[160:163], v[62:65]\n"
        "ds_read_b64_tr_b16 v[34:35], v1 offset:35920\n"
        "v_mfma_f32_16x16x32_bf16 v[66:69], v[38:41], v[164:167], v[66:69]\n"
        "ds_read_b64_tr_b16 v[36:37], v1 offset:38480\n"
        "s_waitcnt lgkmcnt(8)\n"
        "v_mfma_f32_16x16x32_bf16 v[70:73], v[42:45], v[160:163], v[70:73]\n"
        "ds_read_b64_tr_b16 v[38:39], v1 offset:35960\n"
        "v_mfma_f32_16x16x32_bf16 v[74:77], v[42:45], v[164:167], v[74:77]\n"
        "ds_read_b64_tr_b16 v[40:41], v1 offset:38520\n"
        "v_mfma_f32_16x16x32_bf16 v[78:81], v[46:49], v[160:163], v[78:81]\n"
        "ds_read_b64_tr_b16 v[42:43], v2 offset:35840\n"
        "v_mfma_f32_16x16x32_bf16 v[82:85], v[46:49], v[164:167], v[82:85]\n"
        "ds_read_b64_tr_b16 v[44:45], v2 offset:38400\n"
        "s_waitcnt lgkmcnt(8)\n"
        "v_mfma_f32_16x16x32_bf16 v[86:89], v[50:53], v[160:163], v[86:89]\n"
        "ds_read_b64_tr_b16 v[46:47], v13 offset:0\n"
        "v_mfma_f32_16x16x32_bf16 v[90:93], v[50:53], v[164:167], v[90:93]\n"
        "ds_read_b64_tr_b16 v[48:49], v13 offset:2560\n"
        "v_mfma_f32_16x16x32_bf16 v[54:57], v[26:29], v[168:171], v[54:57]\n"
        "ds_read_b64_tr_b16 v[50:51], v13 offset:40\n"
        "v_mfma_f32_16x16x32_bf16 v[58:61], v[26:29], v[172:175], v[58:61]\n"
        "ds_read_b64_tr_b16 v[52:53], v13 offset:2600\n"
        "s_waitcnt lgkmcnt(8)\n"
        "v_mfma_f32_16x16x32_bf16 v[62:65], v[30:33], v[168:171], v[62:65]\n"
        "ds_read_b64_tr_b16 v[26:27], v13 offset:80\n"
        "v_mfma_f32_16x16x32_bf16 v[66:69], v[30:33], v[172:175], v[66:69]\n"
        "ds_read_b64_tr_b16 v[28:29], v13 offset:2640\n"
        "v_mfma_f32_16x16x32_bf16 v[70:73], v[34:37], v[168:171], v[70:73]\n"
        "ds_read_b64_tr_b16 v[30:31], v13 offset:120\n"
        "v_mfma_f32_16x16x32_bf16 v[74:77], v[34:37], v[172:175], v[74:77]\n"
        "ds_read_b64_tr_b16 v[32:33], v13 offset:2680\n"
        "s_waitcnt lgkmcnt(8)\n"
        "v_mfma_f32_16x16x32_bf16 v[78:81], v[38:41], v[168:171], v[78:81]\n"
        "ds_read_b64_tr_b16 v[34:35], v14 offset:0\n"
        "v_mfma_f32_16x16x32_bf16 v[82:85], v[38:41], v[172:175], v[82:85]\n"
        "ds_read_b64_tr_b16 v[36:37], v14 offset:2560\n"
        "v_mfma_f32_16x16x32_bf16 v[86:89], v[42:45], v[168:171], v[86:89]\n"
        "ds_read_b64_tr_b16 v[38:39], v13 offset:5120\n"
        "v_mfma_f32_16x16x32_bf16 v[90:93], v[42:45], v[172:175], v[90:93]\n"
        "ds_read_b64_tr_b16 v[40:41], v13 offset:7680\n"
        "s_waitcnt lgkmcnt(8)\n"
        "v_mfma_f32_16x16x32_bf16 v[54:57], v[46:49], v[176:179], v[54:57]\n"
        "ds_read_b64_tr_b16 v[42:43], v13 offset:5160\n"
        "v_mfma_f32_16x16x32_bf16 v[58:61], v[46:49], v[180:183], v[58:61]\n"
        "ds_read_b64_tr_b16 v[44:45], v13 offset:7720\n"
        "v_mfma_f32_16x16x32_bf16 v[62:65], v[50:53], v[176:179], v[62:65]\n"
        "ds_read_b64_tr_b16 v[46:47], v13 offset:5200\n"
        "v_mfma_f32_16x16x32_bf16 v[66:69], v[50:53], v[180:183], v[66:69]\n"
        "ds_read_b64_tr_b16 v[48:49], v13 offset:7760\n"
        "s_waitcnt lgkmcnt(8)\n"
        "v_mfma_f32_16x16x32_bf16 v[70:73], v[26:29], v[176:179], v[70:73]\n"
        "ds_read_b64_tr_b16 v[50:51], v13 offset:5240\n"
        "v_mfma_f32_16x16x32_bf16 v[74:77], v[26:29], v[180:183], v[74:77]\n"
        "ds_read_b64_tr_b16 v[52:53], v13 offset:7800\n"
        "v_mfma_f32_16x16x32_bf16 v[78:81], v[30:33], v[176:179], v[78:81]\n"
        "ds_read_b64_tr_b16 v[26:27], v14 offset:5120\n"
        "v_mfma_f32_16x16x32_bf16 v[82:85], v[30:33], v[180:183], v[82:85]\n"
        "ds_read_b64_tr_b16 v[28:29], v14 offset:7680\n"
        "s_waitcnt lgkmcnt(8)\n"
        "v_mfma_f32_16x16x32_bf16 v[86:89], v[34:37], v[176:179], v[86:89]\n"
        "ds_read_b64_tr_b16 v[30:31], v13 offset:10240\n"
        "v_mfma_f32_16x16x32_bf16 v[90:93], v[34:37], v[180:183], v[90:93]\n"
        "ds_read_b64_tr_b16 v[32:33], v13 offset:12800\n"
        "v_mfma_f32_16x16x32_bf16 v[54:57], v[38:41], v[184:187], v[54:57]\n"
        "ds_read_b64_tr_b16 v[34:35], v13 offset:10280\n"
        "v_mfma_f32_16x16x32_bf16 v[58:61], v[38:41], v[188:191], v[58:61]\n"
        "ds_read_b64_tr_b16 v[36:37], v13 offset:12840\n"
        "s_waitcnt lgkmcnt(8)\n"
        "v_mfma_f32_16x16x32_bf16 v[62:65], v[42:45], v[184:187], v[62:65]\n"
        "ds_read_b64_tr_b16 v[38:39], v13 offset:10320\n"
        "v_mfma_f32_16x16x32_bf16 v[66:69], v[42:45], v[188:191], v[66:69]\n"
        "ds_read_b64_tr_b16 v[40:41], v13 offset:12880\n"
        "v_mfma_f32_16x16x32_bf16 v[70:73], v[46:49], v[184:187], v[70:73]\n"
        "ds_read_b64_tr_b16 v[42:43], v13 offset:10360\n"
        "v_mfma_f32_16x16x32_bf16 v[74:77], v[46:49], v[188:191], v[74:77]\n"
        "ds_read_b64_tr_b16 v[44:45], v13 offset:12920\n"
        "s_waitcnt lgkmcnt(8)\n"
        "v_mfma_f32_16x16x32_bf16 v[78:81], v[50:53], v[184:187], v[78:81]\n"
        "ds_read_b64_tr_b16 v[46:47], v14 offset:10240\n"
        "v_mfma_f32_16x16x32_bf16 v[82:85], v[50:53], v[188:191], v[82:85]\n"
        "ds_read_b64_tr_b16 v[48:49], v14 offset:12800\n"
        "v_mfma_f32_16x16x32_bf16 v[86:89], v[26:29], v[184:187], v[86:89]\n"
        "ds_read_b64_tr_b16 v[50:51], v13 offset:15360\n"
        "v_mfma_f32_16x16x32_bf16 v[90:93], v[26:29], v[188:191], v[90:93]\n"
        "ds_read_b64_tr_b16 v[52:53], v13 offset:17920\n"
        "s_waitcnt lgkmcnt(8)\n"
        "v_mfma_f32_16x16x32_bf16 v[54:57], v[30:33], v[192:195], v[54:57]\n"
        "ds_read_b64_tr_b16 v[26:27], v13 offset:15400\n"
        "v_mfma_f32_16x16x32_bf16 v[58:61], v[30:33], v[196:199], v[58:61]\n"
        "ds_read_b64_tr_b16 v[28:29], v13 offset:17960\n"
        "v_mfma_f32_16x16x32_bf16 v[62:65], v[34:37], v[192:195], v[62:65]\n"
        "ds_read_b64_tr_b16 v[30:31], v13 offset:15440\n"
        "v_mfma_f32_16x16x32_bf16 v[66:69], v[34:37], v[196:199], v[66:69]\n"
        "ds_read_b64_tr_b16 v[32:33], v13 offset:18000\n"
        "s_waitcnt lgkmcnt(8)\n"
        "v_mfma_f32_16x16x32_bf16 v[70:73], v[38:41], v[192:195], v[70:73]\n"
        "ds_read_b64_tr_b16 v[34:35], v13 offset:15480\n"
        "v_mfma_f32_16x16x32_bf16 v[74:77], v[38:41], v[196:199], v[74:77]\n"
        "ds_read_b64_tr_b16 v[36:37], v13 offset:18040\n"
        "v_mfma_f32_16x16x32_bf16 v[78:81], v[42:45], v[192:195], v[78:81]\n"
        "ds_read_b64_tr_b16 v[38:39], v14 offset:15360\n"
        "v_mfma_f32_16x16x32_bf16 v[82:85], v[42:45], v[196:199], v[82:85]\n"
        "ds_read_b64_tr_b16 v[40:41], v14 offset:17920\n"
        "s_barrier\n"
        "s_waitcnt lgkmcnt(8)\n"
        "v_mfma_f32_16x16x32_bf16 v[86:89], v[46:49], v[192:195], v[86:89]\n"
        "ds_read_b64_tr_b16 v[42:43], v13 offset:20480\n"
        "v_mfma_f32_16x16x32_bf16 v[90:93], v[46:49], v[196:199], v[90:93]\n"
        "ds_read_b64_tr_b16 v[44:45], v13 offset:23040\n"
        "v_mfma_f32_16x16x32_bf16 v[54:57], v[50:53], v[200:203], v[54:57]\n"
        "ds_read_b64_tr_b16 v[46:47], v13 offset:20520\n"
        "v_mfma_f32_16x16x32_bf16 v[58:61], v[50:53], v[204:207], v[58:61]\n"
        "ds_read_b64_tr_b16 v[48:49], v13 offset:23080\n"
        "s_waitcnt lgkmcnt(8)\n"
        "v_mfma_f32_16x16x32_bf16 v[62:65], v[26:29], v[200:203], v[62:65]\n"
        "ds_read_b64_tr_b16 v[50:51], v13 offset:20560\n"
        "v_mfma_f32_16x16x32_bf16 v[66:69], v[26:29], v[204:207], v[66:69]\n"
        "ds_read_b64_tr_b16 v[52:53], v13 offset:23120\n"
        "v_mfma_f32_16x16x32_bf16 v[70:73], v[30:33], v[200:203], v[70:73]\n"
        "ds_read_b64_tr_b16 v[26:27], v13 offset:20600\n"
        "v_mfma_f32_16x16x32_bf16 v[74:77], v[30:33], v[204:207], v[74:77]\n"
        "ds_read_b64_tr_b16 v[28:29], v13 offset:23160\n"
        "s_waitcnt lgkmcnt(8)\n"
        "v_mfma_f32_16x16x32_bf16 v[78:81], v[34:37], v[200:203], v[78:81]\n"
        "ds_read_b64_tr_b16 v[30:31], v14 offset:20480\n"
        "v_mfma_f32_16x16x32_bf16 v[82:85], v[34:37], v[204:207], v[82:85]\n"
        "ds_read_b64_tr_b16 v[32:33], v14 offset:23040\n"
        "v_mfma_f32_16x16x32_bf16 v[86:89], v[38:41], v[200:203], v[86:89]\n"
        "ds_read_b64_tr_b16 v[34:35], v13 offset:25600\n"
        "v_mfma_f32_16x16x32_bf16 v[90:93], v[38:41], v[204:207], v[90:93]\n"
        "ds_read_b64_tr_b16 v[36:37], v13 offset:28160\n"
        "s_waitcnt lgkmcnt(8)\n"
        "v_mfma_f32_16x16x32_bf16 v[54:57], v[42:45], v[208:211], v[54:57]\n"
        "ds_read_b64_tr_b16 v[38:39], v13 offset:25640\n"
        "v_mfma_f32_16x16x32_bf16 v[58:61], v[42:45], v[212:215], v[58:61]\n"
        "ds_read_b64_tr_b16 v[40:41], v13 offset:28200\n"
        "v_mfma_f32_16x16x32_bf16 v[62:65], v[46:49], v[208:211], v[62:65]\n"
        "ds_read_b64_tr_b16 v[42:43], v13 offset:25680\n"
        "v_mfma_f32_16x16x32_bf16 v[66:69], v[46:49], v[212:215], v[66:69]\n"
        "ds_read_b64_tr_b16 v[44:45], v13 offset:28240\n"
        "s_waitcnt lgkmcnt(8)\n"
        "v_mfma_f32_16x16x32_bf16 v[70:73], v[50:53], v[208:211], v[70:73]\n"
        "ds_read_b64_tr_b16 v[46:47], v13 offset:25720\n"
        "v_mfma_f32_16x16x32_bf16 v[74:77], v[50:53], v[212:215], v[74:77]\n"
        "ds_read_b64_tr_b16 v[48:49], v13 offset:28280\n"
        "v_mfma_f32_16x16x32_bf16 v[78:81], v[26:29], v[208:211], v[78:81]\n"
        "ds_read_b64_tr_b16 v[50:51], v14 offset:25600\n"
        "v_mfma_f32_16x16x32_bf16 v[82:85], v[26:29], v[212:215], v[82:85]\n"
        "ds_read_b64_tr_b16 v[52:53], v14 offset:28160\n"
        "s_waitcnt lgkmcnt(8)\n"
        "v_mfma_f32_16x16x32_bf16 v[86:89], v[30:33], v[208:211], v[86:89]\n"
        "ds_read_b64_tr_b16 v[26:27], v1 offset:0\n"
        "v_mfma_f32_16x16x32_bf16 v[90:93], v[30:33], v[212:215], v[90:93]\n"
        "ds_read_b64_tr_b16 v[28:29], v1 offset:2560\n"
        "v_mfma_f32_16x16x32_bf16 v[54:57], v[34:37], v[216:219], v[54:57]\n"
        "ds_read_b64_tr_b16 v[30:31], v1 offset:40\n"
        "v_mfma_f32_16x16x32_bf16 v[58:61], v[34:37], v[220:223], v[58:61]\n"
        "ds_read_b64_tr_b16 v[32:33], v1 offset:2600\n"
        "s_waitcnt lgkmcnt(8)\n"
        "v_mfma_f32_16x16x32_bf16 v[62:65], v[38:41], v[216:219], v[62:65]\n"
        "ds_read_b64_tr_b16 v[34:35], v1 offset:80\n"
        "v_mfma_f32_16x16x32_bf16 v[66:69], v[38:41], v[220:223], v[66:69]\n"
        "ds_read_b64_tr_b16 v[36:37], v1 offset:2640\n"
        "v_mfma_f32_16x16x32_bf16 v[70:73], v[42:45], v[216:219], v[70:73]\n"
        "ds_read_b64_tr_b16 v[38:39], v1 offset:120\n"
        "v_mfma_f32_16x16x32_bf16 v[74:77], v[42:45], v[220:223], v[74:77]\n"
        "ds_read_b64_tr_b16 v[40:41], v1 offset:2680\n"
        "s_waitcnt lgkmcnt(8)\n"
        "v_mfma_f32_16x16x32_bf16 v[78:81], v[46:49], v[216:219], v[78:81]\n"
        "ds_read_b64_tr_b16 v[42:43], v2 offset:0\n"
        "v_mfma_f32_16x16x32_bf16 v[82:85], v[46:49], v[220:223], v[82:85]\n"
        "ds_read_b64_tr_b16 v[44:45], v2 offset:2560\n"
        "v_mfma_f32_16x16x32_bf16 v[86:89], v[50:53], v[216:219], v[86:89]\n"
        "ds_read_b64_tr_b16 v[46:47], v1 offset:5120\n"
        "v_mfma_f32_16x16x32_bf16 v[90:93], v[50:53], v[220:223], v[90:93]\n"
        "ds_read_b64_tr_b16 v[48:49], v1 offset:7680\n"
        "s_waitcnt lgkmcnt(8)\n"
        "v_mfma_f32_16x16x32_bf16 v[54:57], v[26:29], v[224:227], v[54:57]\n"
        "ds_read_b64_tr_b16 v[50:51], v1 offset:5160\n"
        "v_mfma_f32_16x16x32_bf16 v[58:61], v[26:29], v[228:231], v[58:61]\n"
        "ds_read_b64_tr_b16 v[52:53], v1 offset:7720\n"
        "v_mfma_f32_16x16x32_bf16 v[62:65], v[30:33], v[224:227], v[62:65]\n"
        "ds_read_b64_tr_b16 v[26:27], v1 offset:5200\n"
        "v_mfma_f32_16x16x32_bf16 v[66:69], v[30:33], v[228:231], v[66:69]\n"
        "ds_read_b64_tr_b16 v[28:29], v1 offset:7760\n"
        "s_waitcnt lgkmcnt(8)\n"
        "v_mfma_f32_16x16x32_bf16 v[70:73], v[34:37], v[224:227], v[70:73]\n"
        "ds_read_b64_tr_b16 v[30:31], v1 offset:5240\n"
        "v_mfma_f32_16x16x32_bf16 v[74:77], v[34:37], v[228:231], v[74:77]\n"
        "ds_read_b64_tr_b16 v[32:33], v1 offset:7800\n"
        "v_mfma_f32_16x16x32_bf16 v[78:81], v[38:41], v[224:227], v[78:81]\n"
        "ds_read_b64_tr_b16 v[34:35], v2 offset:5120\n"
        "v_mfma_f32_16x16x32_bf16 v[82:85], v[38:41], v[228:231], v[82:85]\n"
        "ds_read_b64_tr_b16 v[36:37], v2 offset:7680\n"
        "s_waitcnt lgkmcnt(8)\n"
        "v_mfma_f32_16x16x32_bf16 v[86:89], v[42:45], v[224:227], v[86:89]\n"
        "ds_read_b64_tr_b16 v[38:39], v13 offset:30720\n"
        "v_mfma_f32_16x16x32_bf16 v[90:93], v[42:45], v[228:231], v[90:93]\n"
        "ds_read_b64_tr_b16 v[40:41], v13 offset:33280\n"
        "v_mfma_f32_16x16x32_bf16 v[54:57], v[46:49], v[232:235], v[54:57]\n"
        "ds_read_b64_tr_b16 v[42:43], v13 offset:30760\n"
        "v_mfma_f32_16x16x32_bf16 v[58:61], v[46:49], v[236:239], v[58:61]\n"
        "ds_read_b64_tr_b16 v[44:45], v13 offset:33320\n"
        "s_waitcnt lgkmcnt(8)\n"
        "v_mfma_f32_16x16x32_bf16 v[62:65], v[50:53], v[232:235], v[62:65]\n"
        "ds_read_b64_tr_b16 v[46:47], v13 offset:30800\n"
        "v_mfma_f32_16x16x32_bf16 v[66:69], v[50:53], v[236:239], v[66:69]\n"
        "ds_read_b64_tr_b16 v[48:49], v13 offset:33360\n"
        "v_mfma_f32_16x16x32_bf16 v[70:73], v[26:29], v[232:235], v[70:73]\n"
        "ds_read_b64_tr_b16 v[50:51], v13 offset:30840\n"
        "v_mfma_f32_16x16x32_bf16 v[74:77], v[26:29], v[236:239], v[74:77]\n"
        "ds_read_b64_tr_b16 v[52:53], v13 offset:33400\n"
        "s_waitcnt lgkmcnt(8)\n"
        "v_mfma_f32_16x16x32_bf16 v[78:81], v[30:33], v[232:235], v[78:81]\n"
        "ds_read_b64_tr_b16 v[26:27], v14 offset:30720\n"
        "v_mfma_f32_16x16x32_bf16 v[82:85], v[30:33], v[236:239], v[82:85]\n"
        "ds_read_b64_tr_b16 v[28:29], v14 offset:33280\n"
        "v_mfma_f32_16x16x32_bf16 v[86:89], v[34:37], v[232:235], v[86:89]\n"
        "ds_read_b64_tr_b16 v[30:31], v13 offset:35840\n"
        "v_mfma_f32_16x16x32_bf16 v[90:93], v[34:37], v[236:239], v[90:93]\n"
        "ds_read_b64_tr_b16 v[32:33], v13 offset:38400\n"
        "s_waitcnt lgkmcnt(8)\n"
        "v_mfma_f32_16x16x32_bf16 v[54:57], v[38:41], v[240:243], v[54:57]\n"
        "ds_read_b64_tr_b16 v[34:35], v13 offset:35880\n"
        "v_mfma_f32_16x16x32_bf16 v[58:61], v[38:41], v[244:247], v[58:61]\n"
        "ds_read_b64_tr_b16 v[36:37], v13 offset:38440\n"
        "v_mfma_f32_16x16x32_bf16 v[62:65], v[42:45], v[240:243], v[62:65]\n"
        "ds_read_b64_tr_b16 v[38:39], v13 offset:35920\n"
        "v_mfma_f32_16x16x32_bf16 v[66:69], v[42:45], v[244:247], v[66:69]\n"
        "ds_read_b64_tr_b16 v[40:41], v13 offset:38480\n"
        "s_waitcnt lgkmcnt(8)\n"
        "v_mfma_f32_16x16x32_bf16 v[70:73], v[46:49], v[240:243], v[70:73]\n"
        "ds_read_b64_tr_b16 v[42:43], v13 offset:35960\n"
        "v_mfma_f32_16x16x32_bf16 v[74:77], v[46:49], v[244:247], v[74:77]\n"
        "ds_read_b64_tr_b16 v[44:45], v13 offset:38520\n"
        "v_mfma_f32_16x16x32_bf16 v[78:81], v[50:53], v[240:243], v[78:81]\n"
        "ds_read_b64_tr_b16 v[46:47], v14 offset:35840\n"
        "v_mfma_f32_16x16x32_bf16 v[82:85], v[50:53], v[244:247], v[82:85]\n"
        "ds_read_b64_tr_b16 v[48:49], v14 offset:38400\n"
        "s_waitcnt lgkmcnt(8)\n"
        "v_mfma_f32_16x16x32_bf16 v[86:89], v[26:29], v[240:243], v[86:89]\n"
        "ds_read_b128 v[160:163], v5 offset:0\n"
        "v_mfma_f32_16x16x32_bf16 v[90:93], v[26:29], v[244:247], v[90:93]\n"
        "ds_read_b128 v[164:167], v5 offset:1024\n"
        "v_mfma_f32_16x16x32_bf16 v[54:57], v[30:33], v[248:251], v[54:57]\n"
        "ds_read_b128 v[168:171], v5 offset:2048\n"
        "v_mfma_f32_16x16x32_bf16 v[58:61], v[30:33], v[252:255], v[58:61]\n"
        "ds_read_b128 v[172:175], v5 offset:3072\n"
        "s_waitcnt lgkmcnt(8)\n"
        "v_mfma_f32_16x16x32_bf16 v[62:65], v[34:37], v[248:251], v[62:65]\n"
        "ds_read_b128 v[176:179], v5 offset:4096\n"
        "v_mfma_f32_16x16x32_bf16 v[66:69], v[34:37], v[252:255], v[66:69]\n"
        "ds_read_b128 v[180:183], v5 offset:5120\n"
        "v_mfma_f32_16x16x32_bf16 v[70:73], v[38:41], v[248:251], v[70:73]\n"
        "ds_read_b128 v[184:187], v5 offset:6144\n"
        "v_cvt_pk_bf16_f32 v192, v54, v55\n"
        "v_cvt_pk_bf16_f32 v193, v56, v57\n"
        "v_mfma_f32_16x16x32_bf16 v[74:77], v[38:41], v[252:255], v[74:77]\n"
        "ds_read_b128 v[188:191], v5 offset:7168\n"
        "v_cvt_pk_bf16_f32 v204, v58, v59\n"
        "v_cvt_pk_bf16_f32 v205, v60, v61\n"
        "s_waitcnt lgkmcnt(8)\n"
        "s_nop 0\n"
        "v_mfma_f32_16x16x32_bf16 v[78:81], v[42:45], v[248:251], v[78:81]\n"
        "v_cvt_pk_bf16_f32 v196, v62, v63\n"
        "v_cvt_pk_bf16_f32 v197, v64, v65\n"
        "ds_read_b64 v[10:11], v10\n"
        "v_mfma_f32_16x16x32_bf16 v[82:85], v[42:45], v[252:255], v[82:85]\n"
        "v_cvt_pk_bf16_f32 v208, v66, v67\n"
        "v_cvt_pk_bf16_f32 v209, v68, v69\n"
        "v_mfma_f32_16x16x32_bf16 v[86:89], v[46:49], v[248:251], v[86:89]\n"
        "v_cvt_pk_bf16_f32 v198, v70, v71\n"
        "v_cvt_pk_bf16_f32 v199, v72, v73\n"
        "v_mfma_f32_16x16x32_bf16 v[90:93], v[46:49], v[252:255], v[90:93]\n"
        "v_cvt_pk_bf16_f32 v210, v74, v75\n"
        "v_cvt_pk_bf16_f32 v211, v76, v77\n"
        ".Lx0:\n"
        "v_cvt_pk_bf16_f32 v202, v78, v79\n"
        "v_cvt_pk_bf16_f32 v203, v80, v81\n"
        "v_cvt_pk_bf16_f32 v214, v82, v83\n"
        "v_cvt_pk_bf16_f32 v215, v84, v85\n"
        "v_cvt_pk_bf16_f32 v194, v86, v87\n"
        "v_cvt_pk_bf16_f32 v195, v88, v89\n"
        "v_cvt_pk_bf16_f32 v200, v86, v87\n"
        "v_cvt_pk_bf16_f32 v201, v88, v89\n"
        "v_cvt_pk_bf16_f32 v206, v90, v91\n"
        "v_cvt_pk_bf16_f32 v207, v92, v93\n"
        "v_cvt_pk_bf16_f32 v212, v90, v91\n"
        "v_cvt_pk_bf16_f32 v213, v92, v93\n"
        "s_waitcnt lgkmcnt(8)\n"
        "v_mfma_f32_16x16x32_bf16 v[54:57], v[160:163], v[192:195], 0\n"
        "v_mfma_f32_16x16x32_bf16 v[58:61], v[160:163], v[204:207], 0\n"
        "s_waitcnt lgkmcnt(6)\n"
        "v_mfma_f32_16x16x32_bf16 v[62:65], v[168:171], v[194:197], 0\n"
        "v_mfma_f32_16x16x32_bf16 v[66:69], v[168:171], v[206:209], 0\n"
        "s_waitcnt lgkmcnt(4)\n"
        "v_mfma_f32_16x16x32_bf16 v[70:73], v[176:179], v[198:201], 0\n"
        "s_nop 1\n"
        "v_exp_f32_e64 v144, v54\n"
        "v_mfma_f32_16x16x32_bf16 v[74:77], v[176:179], v[210:213], 0\n"
        "v_exp_f32 v145, v55\n"
        "s_waitcnt lgkmcnt(2)\n"
        "v_mfma_f32_16x16x32_bf16 v[78:81], v[184:187], v[200:203], 0\n"
        "v_exp_f32_e64 v146, v56\n"
        "v_mfma_f32_16x16x32_bf16 v[82:85], v[184:187], v[212:215], 0\n"
        "v_exp_f32_e64 v147, v57\n"
        "v_mfma_f32_16x16x32_bf16 v[86:89], v[164:167], v[192:195], 0\n"
        "v_cvt_pk_bf16_f32 v96, v144, v145\n"
        "v_cvt_pk_bf16_f32 v97, v146, v147\n"
        "v_mfma_f32_16x16x32_bf16 v[90:93], v[164:167], v[204:207], 0\n"
        "v_exp_f32 v148, -v54\n"
        "v_mfma_f32_16x16x32_bf16 v[86:89], v[172:175], v[194:197], v[86:89]\n"
        "v_exp_f32 v149, -v55\n"
        "v_mfma_f32_16x16x32_bf16 v[90:93], v[172:175], v[206:209], v[90:93]\n"
        "v_exp_f32 v150, -v56\n"
        "v_mfma_f32_16x16x32_bf16 v[86:89], v[180:183], v[198:201], v[86:89]\n"
        "v_exp_f32 v151, -v57\n"
        "v_mfma_f32_16x16x32_bf16 v[90:93], v[180:183], v[210:213], v[90:93]\n"
        "v_cvt_pk_bf16_f32 v108, v148, v149\n"
        "v_cvt_pk_bf16_f32 v109, v150, v151\n"
        "s_waitcnt lgkmcnt(1)\n"
        "v_mfma_f32_16x16x32_bf16 v[86:89], v[188:191], v[200:203], v[86:89]\n"
        "v_exp_f32 v152, v58\n"
        "v_mfma_f32_16x16x32_bf16 v[90:93], v[188:191], v[212:215], v[90:93]\n"
        "v_exp_f32 v153, v59\n"
        "v_exp_f32 v154, v60\n"
        "v_exp_f32_e64 v155, v61\n"
        "v_cvt_pk_bf16_f32 v120, v152, v153\n"
        "v_cvt_pk_bf16_f32 v121, v154, v155\n"
        "v_exp_f32 v156, -v58\n"
        "v_exp_f32 v157, -v59\n"
        "v_exp_f32 v158, -v60\n"
        "v_exp_f32 v159, -v61\n"
        "v_cvt_pk_bf16_f32 v132, v156, v157\n"
        "v_cvt_pk_bf16_f32 v133, v158, v159\n"
        "v_exp_f32 v144, v62\n"
        "v_exp_f32 v145, v63\n"
        "v_exp_f32 v146, v64\n"
        "v_exp_f32 v147, v65\n"
        "v_cvt_pk_bf16_f32 v100, v144, v145\n"
        "v_cvt_pk_bf16_f32 v101, v146, v147\n"
        "v_exp_f32 v148, -v62\n"
        "v_exp_f32 v149, -v63\n"
        "v_exp_f32 v150, -v64\n"
        "v_exp_f32 v151, -v65\n"
        "v_cvt_pk_bf16_f32 v112, v148, v149\n"
        "v_cvt_pk_bf16_f32 v113, v150, v151\n"
        "v_exp_f32 v152, v66\n"
        "v_exp_f32 v153, v67\n"
        "v_exp_f32 v154, v68\n"
        "v_exp_f32 v155, v69\n"
        "v_cvt_pk_bf16_f32 v124, v152, v153\n"
        "v_max3_f32 v18, |v54|, |v55|, |v56|\n"
        "v_cvt_pk_bf16_f32 v125, v154, v155\n"
        "v_exp_f32 v156, -v66\n"
        "v_exp_f32 v157, -v67\n"
        "v_exp_f32 v158, -v68\n"
        "v_max3_f32 v19, |v57|, |v58|, |v59|\n"
        "v_exp_f32 v159, -v69\n"
        "v_cvt_pk_bf16_f32 v136, v156, v157\n"
        "v_cvt_pk_bf16_f32 v137, v158, v159\n"
        "v_exp_f32_e64 v144, v70\n"
        "v_max3_f32 v19, v19, |v60|, |v61|\n"
        "v_exp_f32 v145, v71\n"
        "v_exp_f32 v146, v72\n"
        "v_exp_f32_e64 v147, v73\n"
        "v_cvt_pk_bf16_f32 v102, v144, v145\n"
        "v_max3_f32 v18, v18, |v62|, |v63|\n"
        "v_cvt_pk_bf16_f32 v103, v146, v147\n"
        "v_exp_f32 v148, -v70\n"
        "v_exp_f32 v149, -v71\n"
        "v_exp_f32 v150, -v72\n"
        "v_max3_f32 v19, v19, |v64|, |v65|\n"
        "v_exp_f32 v151, -v73\n"
        "v_cvt_pk_bf16_f32 v114, v148, v149\n"
        "v_cvt_pk_bf16_f32 v115, v150, v151\n"
        "v_exp_f32_e64 v152, v74\n"
        "v_max3_f32 v18, v18, |v66|, |v67|\n"
        "v_exp_f32 v153, v75\n"
        "v_exp_f32 v154, v76\n"
        "v_exp_f32_e64 v155, v77\n"
        "v_cvt_pk_bf16_f32 v126, v152, v153\n"
        "v_max3_f32 v19, v19, |v68|, |v69|\n"
        "v_cvt_pk_bf16_f32 v127, v154, v155\n"
        "v_exp_f32 v156, -v74\n"
        "v_exp_f32 v157, -v75\n"
        "v_exp_f32 v158, -v76\n"
        "v_max3_f32 v18, v18, |v70|, |v71|\n"
        "v_exp_f32 v159, -v77\n"
        "v_cvt_pk_bf16_f32 v138, v156, v157\n"
        "v_cvt_pk_bf16_f32 v139, v158, v159\n"
        "v_exp_f32_e64 v144, v78\n"
        "v_max3_f32 v19, v19, |v72|, |v73|\n"
        "v_exp_f32 v145, v79\n"
        "v_exp_f32 v146, v80\n"
        "v_exp_f32_e64 v147, v81\n"
        "v_cvt_pk_bf16_f32 v106, v144, v145\n"
        "v_max3_f32 v18, v18, |v74|, |v75|\n"
        "v_cvt_pk_bf16_f32 v107, v146, v147\n"
        "v_exp_f32 v148, -v78\n"
        "v_exp_f32 v149, -v79\n"
        "v_exp_f32 v150, -v80\n"
        "v_max3_f32 v19, v19, |v76|, |v77|\n"
        "v_exp_f32 v151, -v81\n"
        "v_cvt_pk_bf16_f32 v118, v148, v149\n"
        "v_cvt_pk_bf16_f32 v119, v150, v151\n"
        "v_exp_f32_e64 v152, v82\n"
        "v_max3_f32 v18, v18, |v78|, |v79|\n"
        "v_exp_f32 v153, v83\n"
        "v_exp_f32 v154, v84\n"
        "v_exp_f32_e64 v155, v85\n"
        "v_cvt_pk_bf16_f32 v130, v152, v153\n"
        "v_max3_f32 v19, v19, |v80|, |v81|\n"
        "v_cvt_pk_bf16_f32 v131, v154, v155\n"
        "v_exp_f32 v156, -v82\n"
        "v_exp_f32 v157, -v83\n"
        "v_exp_f32 v158, -v84\n"
        "v_max3_f32 v18, v18, |v82|, |v83|\n"
        "v_exp_f32 v159, -v85\n"
        "v_cvt_pk_bf16_f32 v142, v156, v157\n"
        "v_cvt_pk_bf16_f32 v143, v158, v159\n"
        "v_exp_f32_e64 v144, v86\n"
        "v_max3_f32 v19, v19, |v84|, |v85|\n"
        "v_exp_f32 v145, v87\n"
        "v_exp_f32 v146, v88\n"
        "v_exp_f32_e64 v147, v89\n"
        "v_cvt_pk_bf16_f32 v98, v144, v145\n"
        "v_max3_f32 v18, v18, |v86|, |v87|\n"
        "v_cvt_pk_bf16_f32 v99, v146, v147\n"
        "v_cvt_pk_bf16_f32 v104, v144, v145\n"
        "v_cvt_pk_bf16_f32 v105, v146, v147\n"
        "v_exp_f32 v148, -v86\n"
        "v_max3_f32 v19, v19, |v88|, |v89|\n"
        "v_exp_f32 v149, -v87\n"
        "v_exp_f32 v150, -v88\n"
        "v_exp_f32 v151, -v89\n"
        "v_cvt_pk_bf16_f32 v110, v148, v149\n"
        "v_max3_f32 v18, v18, |v90|, |v91|\n"
        "v_cvt_pk_bf16_f32 v111, v150, v151\n"
        "v_cvt_pk_bf16_f32 v116, v148, v149\n"
        "v_cvt_pk_bf16_f32 v117, v150, v151\n"
        "v_exp_f32_e64 v152, v90\n"
        "v_max3_f32 v19, v19, |v92|, |v93|\n"
        "v_exp_f32 v153, v91\n"
        "v_exp_f32 v154, v92\n"
        "v_exp_f32_e64 v155, v93\n"
        "v_cvt_pk_bf16_f32 v122, v152, v153\n"
        "v_max_f32_e64 v18, v18, v19\n"
        "v_cvt_pk_bf16_f32 v123, v154, v155\n"
        "v_cvt_pk_bf16_f32 v128, v152, v153\n"
        "v_cvt_pk_bf16_f32 v129, v154, v155\n"
        "v_exp_f32 v156, -v90\n"
        "v_cmp_lt_f32_e64 vcc, s67, v18\n"
        "v_exp_f32 v157, -v91\n"
        "v_exp_f32 v158, -v92\n"
        "v_exp_f32 v159, -v93\n"
        "v_cvt_pk_bf16_f32 v134, v156, v157\n"
        "v_cvt_pk_bf16_f32 v135, v158, v159\n"
        "v_cvt_pk_bf16_f32 v140, v156, v157\n"
        "v_cvt_pk_bf16_f32 v141, v158, v159\n"
        "s_cbranch_vccnz .Lqs321\n"
        ".Lx1:\n"
        ".Lqj322:\n"
        ".Lw323:\n"
        "ds_read_b32 v15, v12 offset:32\n"
        "s_waitcnt lgkmcnt(0)\n"
        "v_cmp_gt_u32_e64 vcc, 5, v15\n"
        "s_nop 0\n"
        "s_cbranch_vccz .Ld324\n"
        "s_sub_i32 s68, s68, 1\n"
        "s_cmp_gt_i32 s68, 0\n"
        "s_cbranch_scc1 .Lw323\n"
        ".Ld324:\n"
        "s_setprio 2\n"
        "ds_write_b32 v94, v95 offset:16\n"
        "ds_read_b128 v[28:31], v6 offset:0\n"
        "ds_read_b128 v[32:35], v7 offset:0\n"
        "ds_read_b128 v[36:39], v6 offset:1408\n"
        "ds_read_b128 v[40:43], v7 offset:1408\n"
        "ds_read_b128 v[44:47], v6 offset:2816\n"
        "ds_read_b128 v[48:51], v7 offset:2816\n"
        "ds_read_b128 v[52:55], v6 offset:4224\n"
        "ds_read_b128 v[56:59], v7 offset:4224\n"
        "ds_read_b128 v[60:63], v6 offset:5632\n"
        "ds_read_b128 v[64:67], v7 offset:5632\n"
        "ds_read_b128 v[68:71], v6 offset:7040\n"
        "ds_read_b128 v[72:75], v7 offset:7040\n"
        "ds_read_b128 v[76:79], v6 offset:8448\n"
        "ds_read_b128 v[80:83], v7 offset:8448\n"
        "ds_read_b128 v[84:87], v6 offset:9856\n"
        "ds_read_b128 v[88:91], v7 offset:9856\n"
        "s_mov_b64 exec, 0xffff\n"
        "buffer_store_dwordx2 v[10:11], v9, s[36:39], s64 offen sc1\n"
        "s_mov_b64 exec, -1\n"
        "s_waitcnt lgkmcnt(14)\n"
        "v_mfma_f32_16x16x32_bf16 v[16:19], v[32:35], v[96:99], 0\n"
        "v_mfma_f32_16x16x32_bf16 v[12:15], v[28:31], v[96:99], 0\n"
        "s_waitcnt lgkmcnt(12)\n"
        "v_mfma_f32_16x16x32_bf16 v[24:27], v[40:43], v[108:111], 0\n"
        "v_mfma_f32_16x16x32_bf16 v[20:23], v[36:39], v[108:111], 0\n"
        "s_waitcnt lgkmcnt(10)\n"
        "v_mfma_f32_16x16x32_bf16 v[148:151], v[48:51], v[98:101], 0\n"
        "v_mfma_f32_16x16x32_bf16 v[144:147], v[44:47], v[98:101], 0\n"
        "s_waitcnt lgkmcnt(8)\n"
        "v_mfma_f32_16x16x32_bf16 v[156:159], v[56:59], v[110:113], 0\n"
        "v_mfma_f32_16x16x32_bf16 v[152:155], v[52:55], v[110:113], 0\n"
        "v_mfma_f32_16x16x32_bf16 v[164:167], v[32:35], v[120:123], 0\n"
        "v_mfma_f32_16x16x32_bf16 v[160:163], v[28:31], v[120:123], 0\n"
        "v_rcp_f32 v27, v26\n"
        "s_nop 3\n"
        "v_rcp_f32 v159, v158\n"
        "v_fmac_f32 v17, s16, v18\n"
        "v_fmac_f32 v149, s16, v150\n"
        "v_mul_f32 v27, v18, v27\n"
        "v_mul_f32 v159, v150, v159\n"
        "v_mfma_f32_16x16x32_bf16 v[172:175], v[40:43], v[132:135], 0\n"
        "v_fmac_f32 v17, v27, v25\n"
        "v_fmac_f32 v149, v159, v157\n"
        "v_rcp_f32 v19, v17\n"
        "v_rcp_f32 v151, v149\n"
        "v_fmac_f32_e64 v12, v27, v20\n"
        "v_mfma_f32_16x16x32_bf16 v[168:171], v[36:39], v[132:135], 0\n"
        "v_fmac_f32 v144, v159, v152\n"
        "v_fmac_f32 v13, v27, v21\n"
        "v_fmac_f32 v145, v159, v153\n"
        "v_fmac_f32 v14, v27, v22\n"
        "v_fmac_f32_e64 v146, v159, v154\n"
        "v_mfma_f32_16x16x32_bf16 v[180:183], v[48:51], v[122:125], 0\n"
        "v_fmac_f32 v15, v27, v23\n"
        "v_fmac_f32 v147, v159, v155\n"
        "v_fmac_f32 v16, v27, v24\n"
        "v_fmac_f32 v148, v159, v156\n"
        "v_mul_f32_e64 v12, v19, v12\n"
        "v_mfma_f32_16x16x32_bf16 v[176:179], v[44:47], v[122:125], 0\n"
        "v_mul_f32 v144, v151, v144\n"
        "v_mul_f32 v13, v19, v13\n"
        "v_mul_f32 v145, v151, v145\n"
        "v_mul_f32 v14, v19, v14\n"
        "v_mul_f32_e64 v146, v151, v146\n"
        "v_mfma_f32_16x16x32_bf16 v[188:191], v[56:59], v[134:137], 0\n"
        "v_mul_f32 v15, v19, v15\n"
        "v_mul_f32 v147, v151, v147\n"
        "v_mul_f32 v16, v19, v16\n"
        "v_mul_f32 v148, v151, v148\n"
        "v_cvt_pk_bf16_f32 v224, v12, v13\n"
        "v_mfma_f32_16x16x32_bf16 v[184:187], v[52:55], v[134:137], 0\n"
        "v_cvt_pk_bf16_f32 v226, v144, v145\n"
        "v_cvt_pk_bf16_f32 v225, v14, v15\n"
        "v_cvt_pk_bf16_f32 v227, v146, v147\n"
        "v_cvt_pk_bf16_f32 v240, v16, v148\n"
        "buffer_store_dwordx4 v[224:227], v0, s[36:39], s65 offen offset:0 sc1\n"
        "s_waitcnt lgkmcnt(6)\n"
        "v_mfma_f32_16x16x32_bf16 v[196:199], v[64:67], v[102:105], 0\n"
        "v_rcp_f32 v175, v174\n"
        "v_rcp_f32 v191, v190\n"
        "v_fmac_f32 v165, s16, v166\n"
        "v_fmac_f32 v181, s16, v182\n"
        "v_mul_f32 v175, v166, v175\n"
        "v_mfma_f32_16x16x32_bf16 v[192:195], v[60:63], v[102:105], 0\n"
        "v_mul_f32 v191, v182, v191\n"
        "v_fmac_f32 v165, v175, v173\n"
        "v_fmac_f32_e64 v181, v191, v189\n"
        "v_rcp_f32 v167, v165\n"
        "s_waitcnt lgkmcnt(4)\n"
        "v_mfma_f32_16x16x32_bf16 v[204:207], v[72:75], v[114:117], 0\n"
        "v_rcp_f32 v183, v181\n"
        "v_fmac_f32 v160, v175, v168\n"
        "v_fmac_f32 v176, v191, v184\n"
        "v_fmac_f32 v161, v175, v169\n"
        "v_fmac_f32_e64 v177, v191, v185\n"
        "v_mfma_f32_16x16x32_bf16 v[200:203], v[68:71], v[114:117], 0\n"
        "v_fmac_f32 v162, v175, v170\n"
        "v_fmac_f32 v178, v191, v186\n"
        "v_fmac_f32_e64 v163, v175, v171\n"
        "v_fmac_f32 v179, v191, v187\n"
        "s_waitcnt lgkmcnt(2)\n"
        "v_mfma_f32_16x16x32_bf16 v[212:215], v[80:83], v[104:107], 0\n"
        "v_fmac_f32 v164, v175, v172\n"
        "v_fmac_f32 v180, v191, v188\n"
        "v_mul_f32 v160, v167, v160\n"
        "v_mul_f32 v176, v183, v176\n"
        "v_mul_f32_e64 v161, v167, v161\n"
        "v_mfma_f32_16x16x32_bf16 v[208:211], v[76:79], v[104:107], 0\n"
        "v_mul_f32 v177, v183, v177\n"
        "v_mul_f32 v162, v167, v162\n"
        "v_mul_f32_e64 v178, v183, v178\n"
        "v_mul_f32 v163, v167, v163\n"
        "s_waitcnt lgkmcnt(0)\n"
        "v_mfma_f32_16x16x32_bf16 v[220:223], v[88:91], v[116:119], 0\n"
        "v_mul_f32 v179, v183, v179\n"
        "v_mul_f32 v164, v167, v164\n"
        "v_mul_f32_e64 v180, v183, v180\n"
        "v_cvt_pk_bf16_f32 v232, v160, v161\n"
        "v_cvt_pk_bf16_f32 v234, v176, v177\n"
        "v_mfma_f32_16x16x32_bf16 v[216:219], v[84:87], v[116:119], 0\n"
        "v_cvt_pk_bf16_f32 v233, v162, v163\n"
        "v_cvt_pk_bf16_f32 v235, v178, v179\n"
        "v_cvt_pk_bf16_f32 v242, v164, v180\n"
        "buffer_store_dwordx4 v[232:235], v0, s[36:39], s65 offen offset:256 sc1\n"
        "v_mfma_f32_16x16x32_bf16 v[16:19], v[64:67], v[126:129], 0\n"
        "v_rcp_f32 v207, v206\n"
        "v_rcp_f32 v223, v222\n"
        "v_fmac_f32 v197, s16, v198\n"
        "v_fmac_f32 v213, s16, v214\n"
        "v_mul_f32_e64 v207, v198, v207\n"
        "v_mfma_f32_16x16x32_bf16 v[12:15], v[60:63], v[126:129], 0\n"
        "v_mul_f32 v223, v214, v223\n"
        "v_fmac_f32 v197, v207, v205\n"
        "v_fmac_f32 v213, v223, v221\n"
        "v_rcp_f32 v199, v197\n"
        "v_mfma_f32_16x16x32_bf16 v[24:27], v[72:75], v[138:141], 0\n"
        "v_rcp_f32 v215, v213\n"
        "v_fmac_f32 v192, v207, v200\n"
        "v_fmac_f32 v208, v223, v216\n"
        "v_fmac_f32 v193, v207, v201\n"
        "v_fmac_f32_e64 v209, v223, v217\n"
        "v_mfma_f32_16x16x32_bf16 v[20:23], v[68:71], v[138:141], 0\n"
        "v_fmac_f32 v194, v207, v202\n"
        "v_fmac_f32 v210, v223, v218\n"
        "v_fmac_f32 v195, v207, v203\n"
        "v_fmac_f32 v211, v223, v219\n"
        "v_mfma_f32_16x16x32_bf16 v[148:151], v[80:83], v[128:131], 0\n"
        "v_fmac_f32 v196, v207, v204\n"
        "v_fmac_f32 v212, v223, v220\n"
        "v_mul_f32 v192, v199, v192\n"
        "v_mul_f32 v208, v215, v208\n"
        "v_mul_f32_e64 v193, v199, v193\n"
        "v_mfma_f32_16x16x32_bf16 v[144:147], v[76:79], v[128:131], 0\n"
        "v_mul_f32 v209, v215, v209\n"
        "v_mul_f32 v194, v199, v194\n"
        "v_mul_f32 v210, v215, v210\n"
        "v_mul_f32 v195, v199, v195\n"
        "v_mfma_f32_16x16x32_bf16 v[156:159], v[88:91], v[140:143], 0\n"
        "v_mul_f32 v211, v215, v211\n"
        "v_mul_f32 v196, v199, v196\n"
        "v_mul_f32_e64 v212, v215, v212\n"
        "v_cvt_pk_bf16_f32 v228, v192, v193\n"
        "v_cvt_pk_bf16_f32 v230, v208, v209\n"
        "v_mfma_f32_16x16x32_bf16 v[152:155], v[84:87], v[140:143], 0\n"
        "v_cvt_pk_bf16_f32 v229, v194, v195\n"
        "v_cvt_pk_bf16_f32 v231, v210, v211\n"
        "v_cvt_pk_bf16_f32 v241, v196, v212\n"
        "buffer_store_dwordx4 v[228:231], v0, s[36:39], s65 offen offset:2048 sc1\n"
        "v_rcp_f32 v27, v26\n"
        "v_rcp_f32 v159, v158\n"
        "v_fmac_f32 v17, s16, v18\n"
        "v_fmac_f32 v149, s16, v150\n"
        "v_mul_f32 v27, v18, v27\n"
        "v_mul_f32 v159, v150, v159\n"
        "v_fmac_f32 v17, v27, v25\n"
        "v_fmac_f32 v149, v159, v157\n"
        "v_rcp_f32 v19, v17\n"
        "v_rcp_f32 v151, v149\n"
        "v_fmac_f32 v12, v27, v20\n"
        "v_fmac_f32 v144, v159, v152\n"
        "v_fmac_f32 v13, v27, v21\n"
        "v_fmac_f32 v145, v159, v153\n"
        "v_fmac_f32 v14, v27, v22\n"
        "v_fmac_f32 v146, v159, v154\n"
        "v_fmac_f32 v15, v27, v23\n"
        "v_fmac_f32 v147, v159, v155\n"
        "v_fmac_f32 v16, v27, v24\n"
        "v_fmac_f32 v148, v159, v156\n"
        "v_mul_f32 v12, v19, v12\n"
        "v_mul_f32 v144, v151, v144\n"
        "v_mul_f32 v13, v19, v13\n"
        "v_mul_f32 v145, v151, v145\n"
        "v_mul_f32 v14, v19, v14\n"
        "v_mul_f32 v146, v151, v146\n"
        "v_mul_f32 v15, v19, v15\n"
        "v_mul_f32 v147, v151, v147\n"
        "v_mul_f32 v16, v19, v16\n"
        "v_mul_f32 v148, v151, v148\n"
        "v_cvt_pk_bf16_f32 v236, v12, v13\n"
        "v_cvt_pk_bf16_f32 v238, v144, v145\n"
        "v_cvt_pk_bf16_f32 v237, v14, v15\n"
        "v_cvt_pk_bf16_f32 v239, v146, v147\n"
        "v_cvt_pk_bf16_f32 v243, v16, v148\n"
        "v_permlane16_swap_b32 v240, v242\n"
        "buffer_store_dwordx4 v[236:239], v0, s[36:39], s65 offen offset:2304 sc1\n"
        "v_permlane16_swap_b32 v241, v243\n"
        "buffer_store_dwordx4 v[240:243], v8, s[36:39], s66 offen sc1\n"
        "s_endpgm\n"
        ".LnotA:\n"
        "s_mul_i32 s3, s29, 10\n"
        "s_add_u32 s3, s3, s30\n"
        "s_sub_u32 s52, s3, 16\n"
        "s_cmp_lt_u32 s30, 26\n"
        "s_cbranch_scc0 .Lspare\n"
        "v_and_b32 v0, 63, v0\n"
        "s_and_b32 s55, s52, 7\n"
        "s_lshr_b32 s33, s52, 3\n"
        "v_lshrrev_b32 v16, 3, v0\n"
        "v_and_b32 v17, 2, v16\n"
        "v_lshlrev_b32 v17, 1, v17\n"
        "v_and_b32 v18, 7, v0\n"
        "v_xor_b32 v17, v17, v18\n"
        "v_lshlrev_b32_e64 v17, 4, v17\n"
        "v_mul_u32_u24 v1, 1280, v16\n"
        "v_add_u32 v1, v1, v17\n"
        "s_lshl3_add_u32 s34, s55, s28\n"
        "s_mul_i32 s35, s34, 40960\n"
        "s_lshl_b32 s3, s33, 7\n"
        "s_add_u32 s35, s35, s3\n"
        "s_mul_i32 s53, s28, 9216\n"
        "s_add_u32 s53, s53, 67328\n"
        "v_lshlrev_b32 v4, 4, v0\n"
        "v_lshlrev_b32 v14, 3, v0\n"
        "v_mov_b32 v110, 0\n"
        "v_mov_b32 v111, 0\n"
        "s_waitcnt lgkmcnt(0)\n"
        "s_mov_b32 s64, s14\n"
        "s_and_b32 s65, s15, 0xffff\n"
        "s_mov_b32 s66, 2621440\n"
        "s_mov_b32 s67, 0x20000\n"
        "s_mov_b32 s8, s16\n"
        "s_and_b32 s9, s17, 0xffff\n"
        "s_mov_b32 s10, 2621440\n"
        "s_mov_b32 s11, 0x20000\n"
        "s_mov_b32 s44, s18\n"
        "s_and_b32 s45, s19, 0xffff\n"
        "s_mov_b32 s46, 5120\n"
        "s_mov_b32 s47, 0x20000\n"
        "s_mov_b32 s4, s26\n"
        "s_and_b32 s5, s27, 0xffff\n"
        "s_mov_b32 s6, 8388608\n"
        "s_mov_b32 s7, 0x20000\n"
        "s_barrier\n"
        "s_lshl_b32 s3, s33, 9\n"
        "buffer_load_dwordx2 v[12:13], v14, s[44:47], s3 offen\n"
        "buffer_load_dwordx2 v[38:39], v110, s[4:7], 0 offen sc1\n"
        "s_sub_u32 s3, s35, 10240\n"
        "s_cmp_eq_u32 s34, 0\n"
        "s_cselect_b32 s3, 2621440, s3\n"
        "s_mov_b32 m0, s53\n"
        "s_nop 0\n"
        "buffer_load_dwordx4 v1, s[64:67], s3 offen lds\n"
        "s_add_u32 s3, s35, 0\n"
        "s_add_u32 m0, s53, 1024\n"
        "s_nop 0\n"
        "buffer_load_dwordx4 v1, s[64:67], s3 offen lds\n"
        "s_add_u32 m0, s53, 5120\n"
        "s_nop 0\n"
        "buffer_load_dwordx4 v1, s[8:11], s3 offen lds\n"
        "s_add_u32 s3, s35, 10240\n"
        "s_add_u32 m0, s53, 2048\n"
        "s_nop 0\n"
        "buffer_load_dwordx4 v1, s[64:67], s3 offen lds\n"
        "s_add_u32 m0, s53, 6144\n"
        "s_nop 0\n"
        "buffer_load_dwordx4 v1, s[8:11], s3 offen lds\n"
        "s_barrier\n"
        "s_add_u32 s3, s35, 20480\n"
        "s_add_u32 m0, s53, 3072\n"
        "s_nop 0\n"
        "buffer_load_dwordx4 v1, s[64:67], s3 offen lds\n"
        "s_add_u32 m0, s53, 7168\n"
        "s_nop 0\n"
        "buffer_load_dwordx4 v1, s[8:11], s3 offen lds\n"
        "s_add_u32 s3, s35, 30720\n"
        "s_add_u32 m0, s53, 4096\n"
        "s_nop 0\n"
        "buffer_load_dwordx4 v1, s[64:67], s3 offen lds\n"
        "s_add_u32 m0, s53, 8192\n"
        "s_nop 0\n"
        "buffer_load_dwordx4 v1, s[8:11], s3 offen lds\n"
        "s_mov_b32 s40, s20\n"
        "s_and_b32 s41, s21, 0xffff\n"
        "s_mov_b32 s42, 2621440\n"
        "s_mov_b32 s43, 0x20000\n"
        "s_mov_b32 s36, s12\n"
        "s_and_b32 s37, s13, 0xffff\n"
        "s_mov_b32 s38, 2621440\n"
        "s_mov_b32 s39, 0x20000\n"
        "s_mov_b32 s48, s24\n"
        "s_and_b32 s49, s25, 0xffff\n"
        "s_mov_b32 s50, 5242880\n"
        "s_mov_b32 s51, 0x20000\n"
        "s_mul_i32 s57, s28, 4288\n"
        "s_add_u32 s57, s57, 33024\n"
        "s_lshl3_add_u32 s62, s52, s28\n"
        "s_lshl_b32 s3, s62, 1\n"
        "s_and_b32 s3, s3, -4\n"
        "s_load_dword s58, s[22:23], s3\n"
        "v_add_u32_e64 v6, s57, v14\n"
        "s_mov_b64 exec, 0xfff\n"
        "ds_write_b64 v6, v[110:111]\n"
        "s_mov_b32 exec_lo, -1\n"
        "ds_write_b64 v14, v[110:111]\n"
        "s_mov_b64 exec, -1\n"
        "v_lshrrev_b32_e64 v18, 4, v0\n"
        "v_bfe_u32 v19, v0, 2, 2\n"
        "v_and_b32_e64 v20, 3, v0\n"
        "v_lshl_add_u32 v21, v18, 2, v20\n"
        "v_and_b32 v16, 2, v19\n"
        "v_lshlrev_b32 v16, 2, v16\n"
        "v_xor_b32 v16, v16, v21\n"
        "v_lshlrev_b32 v16, 3, v16\n"
        "v_lshl_add_u32 v16, v19, 7, v16\n"
        "v_add_u32 v3, s53, v16\n"
        "v_and_b32 v21, 7, v21\n"
        "v_mul_u32_u24 v7, 4288, v19\n"
        "s_lshl_b32 s3, s28, 6\n"
        "s_add_u32 s3, s3, 33024\n"
        "v_lshl_add_u32 v21, v21, 3, s3\n"
        "v_add_u32 v7, v7, v21\n"
        "v_lshl_add_u32 v21, v18, 2, v19\n"
        "v_add3_u32 v16, v21, v20, 1\n"
        "v_lshrrev_b32 v17, 1, v16\n"
        "v_and_b32 v16, 1, v16\n"
        "v_lshlrev_b32_e64 v16, 3, v16\n"
        "v_lshl_add_u32 v16, v17, 7, v16\n"
        "s_lshl_b32 s3, s28, 4\n"
        "v_add_u32 v5, s3, v16\n"
        "v_lshlrev_b32 v16, 2, v20\n"
        "v_sub_u32 v16, v16, v21\n"
        "s_add_u32 s3, s57, 120\n"
        "v_lshl_add_u32 v15, v16, 3, s3\n"
        "s_lshl_b32 s3, s28, 1\n"
        "s_add_u32 s3, s3, 67328\n"
        "v_add_u32 v8, s3, v4\n"
        "s_add_u32 s59, s28, s55\n"
        "s_and_b32 s59, s59, 7\n"
        "s_lshl_b32 s54, s59, 9\n"
        "s_add_u32 s54, s54, 67328\n"
        "s_lshl_b32 s63, s52, 4\n"
        "v_lshl_add_u32 v16, s59, 5, v0\n"
        "v_mul_u32_u24 v2, 1280, v16\n"
        "v_add_u32 v2, s63, v2\n"
        "s_lshl_b32 s3, s28, 3\n"
        "v_lshlrev_b32 v16, 6, v20\n"
        "v_lshrrev_b32 v17, 2, v0\n"
        "v_add3_u32 v16, v16, v17, s3\n"
        "v_mul_u32_u24 v9, 2560, v16\n"
        "s_add_u32 s3, s63, 1280\n"
        "v_add_u32 v9, s3, v9\n"
        "s_mov_b32 s60, 0\n"
        "s_brev_b32 s61, 1\n"
        "s_bitcmp1_b32 s28, 0\n"
        "s_cselect_b32 s3, 0, 16\n"
        "s_waitcnt lgkmcnt(0)\n"
        "s_lshl_b32 s58, s58, s3\n"
        "s_and_b32 s58, s58, s31\n"
        "v_mov_b32 v30, 0\n"
        "v_mov_b32 v31, 0\n"
        "v_mov_b32_e64 v32, 0\n"
        "v_mov_b32 v33, 0\n"
        "s_lshl_b32 s35, s62, 12\n"
        "s_add_u32 s35, s35, 5242880\n"
        "s_waitcnt vmcnt(10)\n"
        "v_lshlrev_b32 v10, 16, v12\n"
        "v_and_b32 v11, s31, v12\n"
        "v_lshlrev_b32 v12, 16, v13\n"
        "v_and_b32_e64 v13, s31, v13\n"
        "s_waitcnt vmcnt(9)\n"
        "v_add_co_u32 v38, vcc, 1, v38\n"
        "s_nop 1\n"
        "v_addc_co_u32 v39, vcc, 0, v39, vcc\n"
        "v_mov_b32 v40, s31\n"
        "s_waitcnt vmcnt(6)\n"
        "ds_read_b64_tr_b16 v[176:177], v3 offset:512\n"
        "ds_read_b64_tr_b16 v[178:179], v3 offset:1024\n"
        "ds_read_b64_tr_b16 v[196:197], v3 offset:5120\n"
        "ds_read_b64_tr_b16 v[180:181], v3 offset:1536\n"
        "ds_read_b64_tr_b16 v[198:199], v3 offset:5632\n"
        "s_waitcnt lgkmcnt(4)\n"
        "v_lshlrev_b32 v212, 16, v176\n"
        "v_and_b32 v213, v40, v176\n"
        "v_lshlrev_b32 v214, 16, v177\n"
        "v_and_b32_e64 v215, v40, v177\n"
        "s_add_u32 m0, s57, 96\n"
        "s_lshl_b32 s56, s62, 12\n"
        "buffer_load_dwordx4 v4, s[40:43], s56 offen lds\n"
        "buffer_load_dwordx4 v4, s[40:43], s56 offen offset:1024 lds\n"
        "buffer_load_dwordx4 v4, s[40:43], s56 offen offset:2048 lds\n"
        "buffer_load_dwordx4 v4, s[40:43], s56 offen offset:3072 lds\n"
        "s_waitcnt lgkmcnt(3)\n"
        "v_lshlrev_b32 v216, 16, v178\n"
        "v_and_b32 v217, v40, v178\n"
        "v_lshlrev_b32 v218, 16, v179\n"
        "v_and_b32 v219, v40, v179\n"
        "v_mul_f32 v228, v13, v213\n"
        "v_mul_f32 v229, v13, v214\n"
        "v_mul_f32 v230, v13, v215\n"
        "v_mul_f32 v231, v13, v216\n"
        "v_fmac_f32 v228, v12, v214\n"
        "v_fmac_f32 v229, v12, v215\n"
        "v_fmac_f32 v230, v12, v216\n"
        "v_fmac_f32 v231, v12, v217\n"
        "v_fmac_f32 v228, v11, v215\n"
        "v_fmac_f32 v229, v11, v216\n"
        "v_fmac_f32 v230, v11, v217\n"
        "v_fmac_f32 v231, v11, v218\n"
        "v_fmac_f32 v228, v10, v216\n"
        "v_fmac_f32 v229, v10, v217\n"
        "v_fmac_f32 v230, v10, v218\n"
        "v_fmac_f32 v231, v10, v219\n"
        "s_waitcnt lgkmcnt(2)\n"
        "v_lshlrev_b32 v224, 16, v196\n"
        "v_and_b32 v225, v40, v196\n"
        "v_lshlrev_b32 v226, 16, v197\n"
        "v_and_b32 v227, v40, v197\n"
        "v_mul_f32 v228, v228, v224\n"
        "v_mul_f32 v229, v229, v225\n"
        "v_mul_f32 v230, v230, v226\n"
        "v_mul_f32_e64 v231, v231, v227\n"
        "v_cvt_pk_bf16_f32 v232, v228, v229\n"
        "v_cvt_pk_bf16_f32 v233, v230, v231\n"
        "s_waitcnt vmcnt(8)\n"
        "ds_read_b64_tr_b16 v[182:183], v3 offset:2048\n"
        "ds_read_b64_tr_b16 v[200:201], v3 offset:6144\n"
        "ds_read_b64_tr_b16 v[184:185], v3 offset:2560\n"
        "ds_read_b64_tr_b16 v[202:203], v3 offset:6656\n"
        "s_waitcnt lgkmcnt(5)\n"
        "v_lshlrev_b32 v212, 16, v180\n"
        "v_and_b32 v213, v40, v180\n"
        "v_lshlrev_b32 v214, 16, v181\n"
        "v_and_b32 v215, v40, v181\n"
        "v_mul_f32 v228, v13, v217\n"
        "v_mul_f32 v229, v13, v218\n"
        "v_mul_f32 v230, v13, v219\n"
        "v_mul_f32 v231, v13, v212\n"
        "v_fmac_f32 v228, v12, v218\n"
        "v_fmac_f32 v229, v12, v219\n"
        "v_fmac_f32 v230, v12, v212\n"
        "v_fmac_f32 v231, v12, v213\n"
        "v_fmac_f32 v228, v11, v219\n"
        "v_fmac_f32 v229, v11, v212\n"
        "v_fmac_f32 v230, v11, v213\n"
        "v_fmac_f32 v231, v11, v214\n"
        "v_fmac_f32 v228, v10, v212\n"
        "v_fmac_f32 v229, v10, v213\n"
        "v_fmac_f32 v230, v10, v214\n"
        "v_fmac_f32 v231, v10, v215\n"
        "s_waitcnt lgkmcnt(4)\n"
        "v_lshlrev_b32 v224, 16, v198\n"
        "v_and_b32 v225, v40, v198\n"
        "v_lshlrev_b32 v226, 16, v199\n"
        "v_and_b32 v227, v40, v199\n"
        "v_mul_f32 v228, v228, v224\n"
        "v_mul_f32 v229, v229, v225\n"
        "v_mul_f32 v230, v230, v226\n"
        "v_mul_f32_e64 v231, v231, v227\n"
        "v_cvt_pk_bf16_f32 v234, v228, v229\n"
        "v_cvt_pk_bf16_f32 v235, v230, v231\n"
        "buffer_store_dwordx4 v[232:235], v4, s[4:7], s35 offen offset:0 sc1\n"
        "s_waitcnt lgkmcnt(3)\n"
        "v_lshlrev_b32 v216, 16, v182\n"
        "v_and_b32 v217, v40, v182\n"
        "v_lshlrev_b32 v218, 16, v183\n"
        "v_and_b32 v219, v40, v183\n"
        "v_mul_f32 v228, v13, v213\n"
        "v_mul_f32 v229, v13, v214\n"
        "v_mul_f32 v230, v13, v215\n"
        "v_mul_f32 v231, v13, v216\n"
        "v_fmac_f32 v228, v12, v214\n"
        "v_fmac_f32 v229, v12, v215\n"
        "v_fmac_f32 v230, v12, v216\n"
        "v_fmac_f32 v231, v12, v217\n"
        "v_fmac_f32 v228, v11, v215\n"
        "v_fmac_f32 v229, v11, v216\n"
        "v_fmac_f32 v230, v11, v217\n"
        "v_fmac_f32 v231, v11, v218\n"
        "v_fmac_f32 v228, v10, v216\n"
        "v_fmac_f32 v229, v10, v217\n"
        "v_fmac_f32 v230, v10, v218\n"
        "v_fmac_f32 v231, v10, v219\n"
        "s_waitcnt lgkmcnt(2)\n"
        "v_lshlrev_b32 v224, 16, v200\n"
        "v_and_b32 v225, v40, v200\n"
        "v_lshlrev_b32 v226, 16, v201\n"
        "v_and_b32 v227, v40, v201\n"
        "v_mul_f32 v228, v228, v224\n"
        "v_mul_f32 v229, v229, v225\n"
        "v_mul_f32 v230, v230, v226\n"
        "v_mul_f32 v231, v231, v227\n"
        "v_cvt_pk_bf16_f32 v236, v228, v229\n"
        "v_cvt_pk_bf16_f32 v237, v230, v231\n"
        "s_waitcnt vmcnt(7)\n"
        "ds_read_b64_tr_b16 v[186:187], v3 offset:3072\n"
        "ds_read_b64_tr_b16 v[204:205], v3 offset:7168\n"
        "ds_read_b64_tr_b16 v[188:189], v3 offset:3584\n"
        "ds_read_b64_tr_b16 v[206:207], v3 offset:7680\n"
        "s_waitcnt lgkmcnt(5)\n"
        "v_lshlrev_b32 v212, 16, v184\n"
        "v_and_b32 v213, v40, v184\n"
        "v_lshlrev_b32 v214, 16, v185\n"
        "v_and_b32 v215, v40, v185\n"
        "v_mul_f32 v228, v13, v217\n"
        "v_mul_f32 v229, v13, v218\n"
        "v_mul_f32 v230, v13, v219\n"
        "v_mul_f32 v231, v13, v212\n"
        "v_fmac_f32 v228, v12, v218\n"
        "v_fmac_f32 v229, v12, v219\n"
        "v_fmac_f32 v230, v12, v212\n"
        "v_fmac_f32 v231, v12, v213\n"
        "v_fmac_f32 v228, v11, v219\n"
        "v_fmac_f32 v229, v11, v212\n"
        "v_fmac_f32 v230, v11, v213\n"
        "v_fmac_f32 v231, v11, v214\n"
        "v_fmac_f32 v228, v10, v212\n"
        "v_fmac_f32 v229, v10, v213\n"
        "v_fmac_f32 v230, v10, v214\n"
        "v_fmac_f32 v231, v10, v215\n"
        "s_waitcnt lgkmcnt(4)\n"
        "v_lshlrev_b32 v224, 16, v202\n"
        "v_and_b32 v225, v40, v202\n"
        "v_lshlrev_b32 v226, 16, v203\n"
        "v_and_b32 v227, v40, v203\n"
        "v_mul_f32 v228, v228, v224\n"
        "v_mul_f32 v229, v229, v225\n"
        "v_mul_f32 v230, v230, v226\n"
        "v_mul_f32_e64 v231, v231, v227\n"
        "v_cvt_pk_bf16_f32 v238, v228, v229\n"
        "v_cvt_pk_bf16_f32 v239, v230, v231\n"
        "buffer_store_dwordx4 v[236:239], v4, s[4:7], s35 offen offset:1024 sc1\n"
        "s_waitcnt lgkmcnt(3)\n"
        "v_lshlrev_b32 v216, 16, v186\n"
        "v_and_b32 v217, v40, v186\n"
        "v_lshlrev_b32 v218, 16, v187\n"
        "v_and_b32 v219, v40, v187\n"
        "v_mul_f32 v228, v13, v213\n"
        "v_mul_f32 v229, v13, v214\n"
        "v_mul_f32 v230, v13, v215\n"
        "v_mul_f32 v231, v13, v216\n"
        "v_fmac_f32 v228, v12, v214\n"
        "v_fmac_f32 v229, v12, v215\n"
        "v_fmac_f32 v230, v12, v216\n"
        "v_fmac_f32 v231, v12, v217\n"
        "v_fmac_f32 v228, v11, v215\n"
        "v_fmac_f32 v229, v11, v216\n"
        "v_fmac_f32 v230, v11, v217\n"
        "v_fmac_f32 v231, v11, v218\n"
        "v_fmac_f32 v228, v10, v216\n"
        "v_fmac_f32 v229, v10, v217\n"
        "v_fmac_f32 v230, v10, v218\n"
        "v_fmac_f32 v231, v10, v219\n"
        "s_waitcnt lgkmcnt(2)\n"
        "v_lshlrev_b32 v224, 16, v204\n"
        "v_and_b32 v225, v40, v204\n"
        "v_lshlrev_b32 v226, 16, v205\n"
        "v_and_b32 v227, v40, v205\n"
        "v_mul_f32 v228, v228, v224\n"
        "v_mul_f32 v229, v229, v225\n"
        "v_mul_f32 v230, v230, v226\n"
        "v_mul_f32 v231, v231, v227\n"
        "v_cvt_pk_bf16_f32 v240, v228, v229\n"
        "v_cvt_pk_bf16_f32 v241, v230, v231\n"
        "s_waitcnt vmcnt(6)\n"
        "ds_read_b64_tr_b16 v[190:191], v3 offset:4096\n"
        "ds_read_b64_tr_b16 v[208:209], v3 offset:8192\n"
        "ds_read_b64_tr_b16 v[192:193], v3 offset:4608\n"
        "ds_read_b64_tr_b16 v[210:211], v3 offset:8704\n"
        "s_waitcnt lgkmcnt(5)\n"
        "v_lshlrev_b32 v212, 16, v188\n"
        "v_and_b32 v213, v40, v188\n"
        "v_lshlrev_b32 v214, 16, v189\n"
        "v_and_b32 v215, v40, v189\n"
        "v_mul_f32 v228, v13, v217\n"
        "v_mul_f32 v229, v13, v218\n"
        "v_mul_f32 v230, v13, v219\n"
        "v_mul_f32 v231, v13, v212\n"
        "v_fmac_f32 v228, v12, v218\n"
        "v_fmac_f32 v229, v12, v219\n"
        "v_fmac_f32 v230, v12, v212\n"
        "v_fmac_f32 v231, v12, v213\n"
        "v_fmac_f32 v228, v11, v219\n"
        "v_fmac_f32 v229, v11, v212\n"
        "v_fmac_f32 v230, v11, v213\n"
        "v_fmac_f32 v231, v11, v214\n"
        "v_fmac_f32 v228, v10, v212\n"
        "v_fmac_f32 v229, v10, v213\n"
        "v_fmac_f32 v230, v10, v214\n"
        "v_fmac_f32 v231, v10, v215\n"
        "s_waitcnt lgkmcnt(4)\n"
        "v_lshlrev_b32 v224, 16, v206\n"
        "v_and_b32 v225, v40, v206\n"
        "v_lshlrev_b32 v226, 16, v207\n"
        "v_and_b32 v227, v40, v207\n"
        "v_mul_f32 v228, v228, v224\n"
        "v_mul_f32 v229, v229, v225\n"
        "v_mul_f32 v230, v230, v226\n"
        "v_mul_f32_e64 v231, v231, v227\n"
        "v_cvt_pk_bf16_f32 v242, v228, v229\n"
        "v_cvt_pk_bf16_f32 v243, v230, v231\n"
        "buffer_store_dwordx4 v[240:243], v4, s[4:7], s35 offen offset:2048 sc1\n"
        "s_waitcnt lgkmcnt(3)\n"
        "v_lshlrev_b32 v216, 16, v190\n"
        "v_and_b32 v217, v40, v190\n"
        "v_lshlrev_b32 v218, 16, v191\n"
        "v_and_b32 v219, v40, v191\n"
        "v_mul_f32 v228, v13, v213\n"
        "v_mul_f32 v229, v13, v214\n"
        "v_mul_f32 v230, v13, v215\n"
        "v_mul_f32 v231, v13, v216\n"
        "v_fmac_f32 v228, v12, v214\n"
        "v_fmac_f32 v229, v12, v215\n"
        "v_fmac_f32 v230, v12, v216\n"
        "v_fmac_f32 v231, v12, v217\n"
        "v_fmac_f32 v228, v11, v215\n"
        "v_fmac_f32 v229, v11, v216\n"
        "v_fmac_f32 v230, v11, v217\n"
        "v_fmac_f32 v231, v11, v218\n"
        "v_fmac_f32 v228, v10, v216\n"
        "v_fmac_f32 v229, v10, v217\n"
        "v_fmac_f32 v230, v10, v218\n"
        "v_fmac_f32 v231, v10, v219\n"
        "s_waitcnt lgkmcnt(2)\n"
        "v_lshlrev_b32 v224, 16, v208\n"
        "v_and_b32 v225, v40, v208\n"
        "v_lshlrev_b32 v226, 16, v209\n"
        "v_and_b32 v227, v40, v209\n"
        "v_mul_f32 v228, v228, v224\n"
        "v_mul_f32 v229, v229, v225\n"
        "v_mul_f32 v230, v230, v226\n"
        "v_mul_f32 v231, v231, v227\n"
        "v_cvt_pk_bf16_f32 v244, v228, v229\n"
        "v_cvt_pk_bf16_f32 v245, v230, v231\n"
        "s_waitcnt lgkmcnt(1)\n"
        "v_lshlrev_b32 v212, 16, v192\n"
        "v_and_b32 v213, v40, v192\n"
        "v_lshlrev_b32 v214, 16, v193\n"
        "v_and_b32 v215, v40, v193\n"
        "v_mul_f32 v228, v13, v217\n"
        "v_mul_f32 v229, v13, v218\n"
        "v_mul_f32 v230, v13, v219\n"
        "v_mul_f32 v231, v13, v212\n"
        "v_fmac_f32 v228, v12, v218\n"
        "v_fmac_f32 v229, v12, v219\n"
        "v_fmac_f32 v230, v12, v212\n"
        "v_fmac_f32 v231, v12, v213\n"
        "v_fmac_f32 v228, v11, v219\n"
        "v_fmac_f32 v229, v11, v212\n"
        "v_fmac_f32 v230, v11, v213\n"
        "v_fmac_f32 v231, v11, v214\n"
        "v_fmac_f32 v228, v10, v212\n"
        "v_fmac_f32 v229, v10, v213\n"
        "v_fmac_f32 v230, v10, v214\n"
        "v_fmac_f32 v231, v10, v215\n"
        "s_waitcnt lgkmcnt(0)\n"
        "v_lshlrev_b32 v224, 16, v210\n"
        "v_and_b32 v225, v40, v210\n"
        "v_lshlrev_b32 v226, 16, v211\n"
        "v_and_b32 v227, v40, v211\n"
        "v_mul_f32 v228, v228, v224\n"
        "v_mul_f32 v229, v229, v225\n"
        "v_mul_f32 v230, v230, v226\n"
        "v_mul_f32 v231, v231, v227\n"
        "v_cvt_pk_bf16_f32 v246, v228, v229\n"
        "v_cvt_pk_bf16_f32 v247, v230, v231\n"
        "buffer_store_dwordx4 v[244:247], v4, s[4:7], s35 offen offset:3072 sc1\n"
        "s_lshl_b32 s3, s52, 7\n"
        "s_add_u32 s3, s3, 1835008\n"
        "s_lshl_b32 s12, s2, 7\n"
        "s_add_u32 s12, s12, 4096\n"
        "s_cmp_eq_u32 s28, 0\n"
        "s_cselect_b32 s12, s3, s12\n"
        "s_cmp_lt_u32 s28, 2\n"
        "s_cselect_b32 s12, s12, 8388608\n"
        "v_lshrrev_b32 v17, 3, v0\n"
        "v_and_b32_e64 v20, 7, v0\n"
        "s_lshl3_add_u32 s13, s33, s28\n"
        "s_lshl_b32 s3, s13, 7\n"
        "s_add_u32 s3, s3, 1835008\n"
        "v_mov_b32 v16, s3\n"
        "s_lshl_b32 s13, s13, 15\n"
        "s_lshl_b32 s3, s55, 7\n"
        "s_add_u32 s13, s13, s3\n"
        "s_add_u32 s13, s13, 5242880\n"
        "v_lshlrev_b32_e64 v20, 4, v20\n"
        "v_lshl_add_u32 v17, v17, 10, v20\n"
        "s_lshl_b32 s14, s28, 12\n"
        "s_add_u32 s14, s14, 256\n"
        "s_sub_u32 s3, s30, 16\n"
        "s_lshl3_add_u32 s3, s3, s28\n"
        "s_mul_i32 s3, s3, 13\n"
        "v_mul_u32_u24 v22, 79, v0\n"
        "v_lshrrev_b32 v22, 10, v22\n"
        "v_mul_u32_u24 v23, 13, v22\n"
        "v_sub_u32 v23, v0, v23\n"
        "v_add_u32 v23, s3, v23\n"
        "v_min_u32 v23, 1023, v23\n"
        "v_lshl_add_u32 v22, v22, 10, v23\n"
        "v_and_b32 v23, 1, v22\n"
        "v_lshrrev_b32 v22, 1, v22\n"
        "v_mul_u32_u24 v22, 1280, v22\n"
        "v_lshl_add_u32 v22, v23, 7, v22\n"
        "s_mul_i32 s15, s29, 160\n"
        "s_and_b32 s15, s15, -128\n"
        "s_waitcnt vmcnt(0)\n"
        "s_barrier\n"
        "s_mov_b64 exec, 0xffff\n"
        "buffer_store_dwordx2 v[38:39], v14, s[4:7], s12 offen sc1\n"
        "s_mov_b64 exec, -1\n"
        "s_add_u32 m0, s54, 0\n"
        "s_mov_b32 s56, 0\n"
        "s_mov_b32 exec_hi, 0\n"
        "buffer_load_dwordx4 v2, s[36:39], s56 offen lds\n"
        "s_mov_b32 exec_hi, -1\n"
        "s_sleep 4\n"
        "s_mov_b32 s3, 0x40000\n"
        ".Lp325:\n"
        "buffer_load_dwordx2 v[18:19], v16, s[4:7], 0 offen sc1\n"
        "s_waitcnt vmcnt(0)\n"
        "v_cmp_ne_u64 vcc, v[18:19], v[38:39]\n"
        "s_nop 0\n"
        "s_cbranch_vccz .Ld326\n"
        "s_sleep 2\n"
        "s_sub_u32 s3, s3, 1\n"
        "s_cmp_eq_u32 s3, 0\n"
        "s_cbranch_scc0 .Lp325\n"
        ".Ld326:\n"
        "s_add_u32 m0, s14, 0\n"
        "s_add_u32 s3, s13, 0\n"
        "buffer_load_dwordx4 v17, s[4:7], s3 offen sc1 lds\n"
        "s_add_u32 m0, s14, 1024\n"
        "s_add_u32 s3, s13, 8192\n"
        "buffer_load_dwordx4 v17, s[4:7], s3 offen sc1 lds\n"
        "s_add_u32 m0, s14, 2048\n"
        "s_add_u32 s3, s13, 16384\n"
        "buffer_load_dwordx4 v17, s[4:7], s3 offen sc1 lds\n"
        "s_add_u32 m0, s14, 3072\n"
        "s_add_u32 s3, s13, 24576\n"
        "buffer_load_dwordx4 v17, s[4:7], s3 offen sc1 lds\n"
        "ds_read_b64_tr_b16 v[46:47], v15 offset:128\n"
        "ds_read_b64_tr_b16 v[48:49], v15 offset:0\n"
        "ds_read_b64_tr_b16 v[50:51], v15 offset:384\n"
        "ds_read_b64_tr_b16 v[52:53], v15 offset:256\n"
        "ds_read_b64_tr_b16 v[54:55], v15 offset:640\n"
        "ds_read_b64_tr_b16 v[56:57], v15 offset:512\n"
        "ds_read_b64_tr_b16 v[58:59], v15 offset:896\n"
        "ds_read_b64_tr_b16 v[60:61], v15 offset:768\n"
        "ds_read_b64_tr_b16 v[62:63], v15 offset:1152\n"
        "ds_read_b64_tr_b16 v[64:65], v15 offset:1024\n"
        "ds_read_b64_tr_b16 v[66:67], v15 offset:1408\n"
        "ds_read_b64_tr_b16 v[68:69], v15 offset:1280\n"
        "ds_read_b64_tr_b16 v[70:71], v15 offset:1664\n"
        "ds_read_b64_tr_b16 v[72:73], v15 offset:1536\n"
        "ds_read_b64_tr_b16 v[74:75], v15 offset:1920\n"
        "ds_read_b64_tr_b16 v[76:77], v15 offset:1792\n"
        "ds_read_b64_tr_b16 v[78:79], v15 offset:2176\n"
        "ds_read_b64_tr_b16 v[80:81], v15 offset:2048\n"
        "ds_read_b64_tr_b16 v[82:83], v15 offset:2432\n"
        "ds_read_b64_tr_b16 v[84:85], v15 offset:2304\n"
        "ds_read_b64_tr_b16 v[86:87], v15 offset:2688\n"
        "ds_read_b64_tr_b16 v[88:89], v15 offset:2560\n"
        "ds_read_b64_tr_b16 v[90:91], v15 offset:2944\n"
        "ds_read_b64_tr_b16 v[92:93], v15 offset:2816\n"
        "ds_read_b64_tr_b16 v[94:95], v15 offset:3200\n"
        "ds_read_b64_tr_b16 v[96:97], v15 offset:3072\n"
        "ds_read_b64_tr_b16 v[98:99], v15 offset:3456\n"
        "ds_read_b64_tr_b16 v[100:101], v15 offset:3328\n"
        "ds_read_b64_tr_b16 v[102:103], v15 offset:3712\n"
        "ds_read_b64_tr_b16 v[104:105], v15 offset:3584\n"
        "ds_read_b64_tr_b16 v[106:107], v15 offset:3968\n"
        "ds_read_b64_tr_b16 v[108:109], v15 offset:3840\n"
        "s_add_u32 m0, s54, 4096\n"
        "s_mov_b32 s56, 327680\n"
        "s_mov_b32 exec_hi, 0\n"
        "buffer_load_dwordx4 v2, s[36:39], s56 offen lds\n"
        "s_mov_b32 exec_hi, -1\n"
        "s_waitcnt vmcnt(1)\n"
        "s_waitcnt lgkmcnt(0)\n"
        "s_barrier\n"
        "ds_read_b64_tr_b16 v[112:113], v5 offset:0\n"
        "ds_read_b64_tr_b16 v[114:115], v5 offset:1024\n"
        "ds_read_b64_tr_b16 v[116:117], v5 offset:2048\n"
        "ds_read_b64_tr_b16 v[118:119], v5 offset:3072\n"
        "ds_read_b64_tr_b16 v[120:121], v5 offset:4096\n"
        "ds_read_b64_tr_b16 v[122:123], v5 offset:5120\n"
        "s_waitcnt lgkmcnt(5)\n"
        "v_mfma_f32_16x16x32_bf16 v[176:179], v[46:49], v[110:113], 0\n"
        "ds_read_b64_tr_b16 v[124:125], v5 offset:6144\n"
        "s_waitcnt lgkmcnt(5)\n"
        "v_mfma_f32_16x16x32_bf16 v[180:183], v[46:49], v[112:115], 0\n"
        "ds_read_b64_tr_b16 v[126:127], v5 offset:7168\n"
        "v_mfma_f32_16x16x32_bf16 v[184:187], v[50:53], v[110:113], 0\n"
        "ds_read_b64_tr_b16 v[128:129], v5 offset:8192\n"
        "s_waitcnt lgkmcnt(6)\n"
        "v_mfma_f32_16x16x32_bf16 v[184:187], v[46:49], v[114:117], v[184:187]\n"
        "ds_read_b64_tr_b16 v[130:131], v5 offset:9216\n"
        "v_mfma_f32_16x16x32_bf16 v[188:191], v[50:53], v[112:115], 0\n"
        "ds_read_b64_tr_b16 v[132:133], v5 offset:10240\n"
        "s_waitcnt lgkmcnt(7)\n"
        "v_mfma_f32_16x16x32_bf16 v[188:191], v[46:49], v[116:119], v[188:191]\n"
        "ds_read_u16_d16_hi v30, v8 offset:0\n"
        "v_mfma_f32_16x16x32_bf16 v[192:195], v[54:57], v[110:113], 0\n"
        "ds_read_u16_d16_hi v31, v8 offset:1024\n"
        "v_mfma_f32_16x16x32_bf16 v[192:195], v[50:53], v[114:117], v[192:195]\n"
        "ds_read_u16_d16_hi v32, v8 offset:2048\n"
        "s_waitcnt lgkmcnt(9)\n"
        "v_mfma_f32_16x16x32_bf16 v[192:195], v[46:49], v[118:121], v[192:195]\n"
        "ds_read_u16_d16_hi v33, v8 offset:3072\n"
        "v_and_b32_e64 v40, s31, v113\n"
        "v_mfma_f32_16x16x32_bf16 v[196:199], v[54:57], v[112:115], 0\n"
        "ds_read_b64_tr_b16 v[134:135], v5 offset:11264\n"
        "v_and_b32_e64 v41, s31, v115\n"
        "v_mfma_f32_16x16x32_bf16 v[196:199], v[50:53], v[116:119], v[196:199]\n"
        "ds_read_b64_tr_b16 v[136:137], v5 offset:12288\n"
        "v_and_b32 v42, s31, v117\n"
        "s_waitcnt lgkmcnt(11)\n"
        "v_mfma_f32_16x16x32_bf16 v[196:199], v[46:49], v[120:123], v[196:199]\n"
        "ds_read_b64_tr_b16 v[138:139], v5 offset:13312\n"
        "v_and_b32_e64 v43, s31, v119\n"
        "v_mfma_f32_16x16x32_bf16 v[200:203], v[58:61], v[110:113], 0\n"
        "ds_read_b64_tr_b16 v[140:141], v5 offset:14336\n"
        "v_fmac_f32_e64 v176, s58, v40\n"
        "v_mfma_f32_16x16x32_bf16 v[200:203], v[54:57], v[114:117], v[200:203]\n"
        "ds_read_b64_tr_b16 v[142:143], v5 offset:15360\n"
        "v_fmac_f32_e64 v180, s58, v41\n"
        "v_mfma_f32_16x16x32_bf16 v[200:203], v[50:53], v[118:121], v[200:203]\n"
        "s_add_u32 m0, s54, 8192\n"
        "s_mov_b32 s56, 655360\n"
        "s_mov_b32 exec_hi, 0\n"
        "buffer_load_dwordx4 v2, s[36:39], s56 offen lds\n"
        "s_mov_b32 exec_hi, -1\n"
        "v_fmac_f32 v184, s58, v42\n"
        "s_waitcnt lgkmcnt(13)\n"
        "v_mfma_f32_16x16x32_bf16 v[200:203], v[46:49], v[122:125], v[200:203]\n"
        "v_fmac_f32_e64 v188, s58, v43\n"
        "v_cndmask_b32 v41, v183, v179, s[60:61]\n"
        "v_mfma_f32_16x16x32_bf16 v[204:207], v[58:61], v[112:115], 0\n"
        "v_cndmask_b32 v42, v187, v183, s[60:61]\n"
        "v_cndmask_b32 v43, v191, v187, s[60:61]\n"
        "v_mfma_f32_16x16x32_bf16 v[204:207], v[54:57], v[116:119], v[204:207]\n"
        "v_add_f32_dpp v178, v179, v178 wave_shr:1 row_mask:0xf bank_mask:0xf bound_ctrl:1\n"
        "v_add_f32_dpp v182, v41, v182 wave_ror:1 row_mask:0xf bank_mask:0xf bound_ctrl:1\n"
        "v_mfma_f32_16x16x32_bf16 v[204:207], v[50:53], v[120:123], v[204:207]\n"
        "v_add_f32_dpp v186, v42, v186 wave_ror:1 row_mask:0xf bank_mask:0xf bound_ctrl:1\n"
        "v_add_f32_dpp v190, v43, v190 wave_ror:1 row_mask:0xf bank_mask:0xf bound_ctrl:1\n"
        "s_waitcnt lgkmcnt(12)\n"
        "s_nop 0\n"
        "v_mfma_f32_16x16x32_bf16 v[204:207], v[46:49], v[124:127], v[204:207]\n"
        "v_cndmask_b32 v41, v182, v178, s[60:61]\n"
        "v_cndmask_b32 v42, v186, v182, s[60:61]\n"
        "v_mfma_f32_16x16x32_bf16 v[208:211], v[62:65], v[110:113], 0\n"
        "v_cndmask_b32 v43, v190, v186, s[60:61]\n"
        "v_add_f32_dpp v177, v178, v177 wave_shr:1 row_mask:0xf bank_mask:0xf bound_ctrl:1\n"
        "v_mfma_f32_16x16x32_bf16 v[208:211], v[58:61], v[114:117], v[208:211]\n"
        "v_add_f32_dpp v181, v41, v181 wave_ror:1 row_mask:0xf bank_mask:0xf bound_ctrl:1\n"
        "v_add_f32_dpp v185, v42, v185 wave_ror:1 row_mask:0xf bank_mask:0xf bound_ctrl:1\n"
        "v_mfma_f32_16x16x32_bf16 v[208:211], v[54:57], v[118:121], v[208:211]\n"
        "v_add_f32_dpp v189, v43, v189 wave_ror:1 row_mask:0xf bank_mask:0xf bound_ctrl:1\n"
        "v_cndmask_b32 v41, v181, v177, s[60:61]\n"
        "v_mfma_f32_16x16x32_bf16 v[208:211], v[50:53], v[122:125], v[208:211]\n"
        "v_cndmask_b32 v42, v185, v181, s[60:61]\n"
        "v_cndmask_b32 v43, v189, v185, s[60:61]\n"
        "s_waitcnt lgkmcnt(11)\n"
        "v_mfma_f32_16x16x32_bf16 v[208:211], v[46:49], v[126:129], v[208:211]\n"
        "v_add_f32_dpp v176, v177, v176 wave_shr:1 row_mask:0xf bank_mask:0xf bound_ctrl:1\n"
        "v_add_f32_dpp v180, v41, v180 wave_ror:1 row_mask:0xf bank_mask:0xf bound_ctrl:1\n"
        "v_mfma_f32_16x16x32_bf16 v[212:215], v[62:65], v[112:115], 0\n"
        "v_add_f32_dpp v184, v42, v184 wave_ror:1 row_mask:0xf bank_mask:0xf bound_ctrl:1\n"
        "v_add_f32_dpp v188, v43, v188 wave_ror:1 row_mask:0xf bank_mask:0xf bound_ctrl:1\n"
        "v_mfma_f32_16x16x32_bf16 v[212:215], v[58:61], v[116:119], v[212:215]\n"
        "s_waitcnt lgkmcnt(8)\n"
        "v_mul_f32 v176, v30, v176\n"
        "s_waitcnt lgkmcnt(7)\n"
        "v_mul_f32_e64 v180, v31, v180\n"
        "v_mfma_f32_16x16x32_bf16 v[212:215], v[54:57], v[120:123], v[212:215]\n"
        "s_waitcnt lgkmcnt(6)\n"
        "v_mul_f32 v184, v32, v184\n"
        "s_waitcnt lgkmcnt(5)\n"
        "v_mul_f32 v188, v33, v188\n"
        "v_mfma_f32_16x16x32_bf16 v[212:215], v[50:53], v[124:127], v[212:215]\n"
        "v_cvt_pk_bf16_f32 v44, v176, v180\n"
        "v_cvt_pk_bf16_f32 v45, v184, v188\n"
        "v_mfma_f32_16x16x32_bf16 v[212:215], v[46:49], v[128:131], v[212:215]\n"
        "ds_write_b64 v6, v[44:45] offset:0\n"
        "v_mfma_f32_16x16x32_bf16 v[216:219], v[66:69], v[110:113], 0\n"
        "v_mfma_f32_16x16x32_bf16 v[216:219], v[62:65], v[114:117], v[216:219]\n"
        "v_mfma_f32_16x16x32_bf16 v[216:219], v[58:61], v[118:121], v[216:219]\n"
        "v_mfma_f32_16x16x32_bf16 v[216:219], v[54:57], v[122:125], v[216:219]\n"
        "v_mfma_f32_16x16x32_bf16 v[216:219], v[50:53], v[126:129], v[216:219]\n"
        "v_mfma_f32_16x16x32_bf16 v[216:219], v[46:49], v[130:133], v[216:219]\n"
        "s_waitcnt vmcnt(1) lgkmcnt(0)\n"
        "s_barrier\n"
        "v_mfma_f32_16x16x32_bf16 v[220:223], v[66:69], v[112:115], 0\n"
        "s_add_u32 m0, s54, 12288\n"
        "s_mov_b32 s56, 983040\n"
        "s_mov_b32 exec_hi, 0\n"
        "buffer_load_dwordx4 v2, s[36:39], s56 offen lds\n"
        "s_mov_b32 exec_hi, -1\n"
        "v_mfma_f32_16x16x32_bf16 v[220:223], v[62:65], v[116:119], v[220:223]\n"
        "ds_read_u16_d16_hi v30, v8 offset:4096\n"
        "v_mfma_f32_16x16x32_bf16 v[220:223], v[58:61], v[120:123], v[220:223]\n"
        "ds_read_u16_d16_hi v31, v8 offset:5120\n"
        "v_mfma_f32_16x16x32_bf16 v[220:223], v[54:57], v[124:127], v[220:223]\n"
        "ds_read_u16_d16_hi v32, v8 offset:6144\n"
        "v_mfma_f32_16x16x32_bf16 v[220:223], v[50:53], v[128:131], v[220:223]\n"
        "ds_read_u16_d16_hi v33, v8 offset:7168\n"
        "v_mfma_f32_16x16x32_bf16 v[220:223], v[46:49], v[132:135], v[220:223]\n"
        "ds_read_b64_tr_b16 v[34:35], v7 offset:0\n"
        "v_mfma_f32_16x16x32_bf16 v[224:227], v[70:73], v[110:113], 0\n"
        "ds_read_b64_tr_b16 v[36:37], v7 offset:17152\n"
        "v_mfma_f32_16x16x32_bf16 v[224:227], v[66:69], v[114:117], v[224:227]\n"
        "ds_read_b64_tr_b16 v[144:145], v5 offset:16384\n"
        "v_mfma_f32_16x16x32_bf16 v[224:227], v[62:65], v[118:121], v[224:227]\n"
        "ds_read_b64_tr_b16 v[146:147], v5 offset:17408\n"
        "v_and_b32_e64 v40, s31, v121\n"
        "v_mfma_f32_16x16x32_bf16 v[224:227], v[58:61], v[122:125], v[224:227]\n"
        "ds_read_b64_tr_b16 v[148:149], v5 offset:18432\n"
        "v_and_b32_e64 v41, s31, v123\n"
        "v_mfma_f32_16x16x32_bf16 v[224:227], v[54:57], v[126:129], v[224:227]\n"
        "ds_read_b64_tr_b16 v[150:151], v5 offset:19456\n"
        "v_and_b32_e64 v42, s31, v125\n"
        "v_mfma_f32_16x16x32_bf16 v[224:227], v[50:53], v[130:133], v[224:227]\n"
        "v_and_b32 v43, s31, v127\n"
        "v_fmac_f32 v192, s58, v40\n"
        "v_mfma_f32_16x16x32_bf16 v[224:227], v[46:49], v[134:137], v[224:227]\n"
        "v_fmac_f32 v196, s58, v41\n"
        "v_fmac_f32 v200, s58, v42\n"
        "v_mfma_f32_16x16x32_bf16 v[228:231], v[70:73], v[112:115], 0\n"
        "v_fmac_f32_e64 v204, s58, v43\n"
        "v_cndmask_b32 v40, v195, v191, s[60:61]\n"
        "v_mfma_f32_16x16x32_bf16 v[228:231], v[66:69], v[116:119], v[228:231]\n"
        "v_cndmask_b32 v41, v199, v195, s[60:61]\n"
        "v_cndmask_b32 v42, v203, v199, s[60:61]\n"
        "v_mfma_f32_16x16x32_bf16 v[228:231], v[62:65], v[120:123], v[228:231]\n"
        "v_cndmask_b32 v43, v207, v203, s[60:61]\n"
        "v_add_f32_dpp v194, v40, v194 wave_ror:1 row_mask:0xf bank_mask:0xf bound_ctrl:1\n"
        "v_mfma_f32_16x16x32_bf16 v[228:231], v[58:61], v[124:127], v[228:231]\n"
        "v_add_f32_dpp v198, v41, v198 wave_ror:1 row_mask:0xf bank_mask:0xf bound_ctrl:1\n"
        "v_add_f32_dpp v202, v42, v202 wave_ror:1 row_mask:0xf bank_mask:0xf bound_ctrl:1\n"
        "v_mfma_f32_16x16x32_bf16 v[228:231], v[54:57], v[128:131], v[228:231]\n"
        "v_add_f32_dpp v206, v43, v206 wave_ror:1 row_mask:0xf bank_mask:0xf bound_ctrl:1\n"
        "v_cndmask_b32 v40, v194, v190, s[60:61]\n"
        "v_mfma_f32_16x16x32_bf16 v[228:231], v[50:53], v[132:135], v[228:231]\n"
        "v_cndmask_b32 v41, v198, v194, s[60:61]\n"
        "v_cndmask_b32 v42, v202, v198, s[60:61]\n"
        "v_mfma_f32_16x16x32_bf16 v[228:231], v[46:49], v[136:139], v[228:231]\n"
        "v_cndmask_b32 v43, v206, v202, s[60:61]\n"
        "v_add_f32_dpp v193, v40, v193 wave_ror:1 row_mask:0xf bank_mask:0xf bound_ctrl:1\n"
        "v_mfma_f32_16x16x32_bf16 v[232:235], v[74:77], v[110:113], 0\n"
        "v_add_f32_dpp v197, v41, v197 wave_ror:1 row_mask:0xf bank_mask:0xf bound_ctrl:1\n"
        "v_add_f32_dpp v201, v42, v201 wave_ror:1 row_mask:0xf bank_mask:0xf bound_ctrl:1\n"
        "v_mfma_f32_16x16x32_bf16 v[232:235], v[70:73], v[114:117], v[232:235]\n"
        "v_add_f32_dpp v205, v43, v205 wave_ror:1 row_mask:0xf bank_mask:0xf bound_ctrl:1\n"
        "v_cndmask_b32 v40, v193, v189, s[60:61]\n"
        "v_mfma_f32_16x16x32_bf16 v[232:235], v[66:69], v[118:121], v[232:235]\n"
        "v_cndmask_b32 v41, v197, v193, s[60:61]\n"
        "v_cndmask_b32 v42, v201, v197, s[60:61]\n"
        "v_mfma_f32_16x16x32_bf16 v[232:235], v[62:65], v[122:125], v[232:235]\n"
        "v_cndmask_b32 v43, v205, v201, s[60:61]\n"
        "v_add_f32_dpp v192, v40, v192 wave_ror:1 row_mask:0xf bank_mask:0xf bound_ctrl:1\n"
        "v_mfma_f32_16x16x32_bf16 v[232:235], v[58:61], v[126:129], v[232:235]\n"
        "v_add_f32_dpp v196, v41, v196 wave_ror:1 row_mask:0xf bank_mask:0xf bound_ctrl:1\n"
        "v_add_f32_dpp v200, v42, v200 wave_ror:1 row_mask:0xf bank_mask:0xf bound_ctrl:1\n"
        "v_mfma_f32_16x16x32_bf16 v[232:235], v[54:57], v[130:133], v[232:235]\n"
        "v_add_f32_dpp v204, v43, v204 wave_ror:1 row_mask:0xf bank_mask:0xf bound_ctrl:1\n"
        "s_waitcnt lgkmcnt(9)\n"
        "v_mul_f32 v192, v30, v192\n"
        "v_mfma_f32_16x16x32_bf16 v[232:235], v[50:53], v[134:137], v[232:235]\n"
        "s_waitcnt lgkmcnt(8)\n"
        "v_mul_f32 v196, v31, v196\n"
        "s_waitcnt lgkmcnt(7)\n"
        "v_mul_f32 v200, v32, v200\n"
        "v_mfma_f32_16x16x32_bf16 v[232:235], v[46:49], v[138:141], v[232:235]\n"
        "s_waitcnt lgkmcnt(6)\n"
        "v_mul_f32 v204, v33, v204\n"
        "v_cvt_pk_bf16_f32 v44, v192, v196\n"
        "v_mfma_f32_16x16x32_bf16 v[236:239], v[74:77], v[112:115], 0\n"
        "v_cvt_pk_bf16_f32 v45, v200, v204\n"
        "ds_write_b64 v6, v[44:45] offset:512\n"
        "v_mfma_f32_16x16x32_bf16 v[236:239], v[70:73], v[116:119], v[236:239]\n"
        "v_mfma_f32_16x16x32_bf16 v[236:239], v[66:69], v[120:123], v[236:239]\n"
        "v_mfma_f32_16x16x32_bf16 v[236:239], v[62:65], v[124:127], v[236:239]\n"
        "v_mfma_f32_16x16x32_bf16 v[236:239], v[58:61], v[128:131], v[236:239]\n"
        "v_mfma_f32_16x16x32_bf16 v[236:239], v[54:57], v[132:135], v[236:239]\n"
        "v_mfma_f32_16x16x32_bf16 v[236:239], v[50:53], v[136:139], v[236:239]\n"
        "v_mfma_f32_16x16x32_bf16 v[236:239], v[46:49], v[140:143], v[236:239]\n"
        "s_mov_b32 s57, 0\n"
        "s_waitcnt lgkmcnt(5)\n"
        "s_mov_b32 exec_hi, 0\n"
        "buffer_store_dwordx4 v[34:37], v9, s[48:51], s57 offen\n"
        "s_mov_b32 exec_hi, -1\n"
        "s_waitcnt vmcnt(2) lgkmcnt(0)\n"
        "s_barrier\n"
        "v_mfma_f32_16x16x32_bf16 v[240:243], v[78:81], v[110:113], 0\n"
        "s_add_u32 m0, s54, 16384\n"
        "s_mov_b32 s56, 1310720\n"
        "s_mov_b32 exec_hi, 0\n"
        "buffer_load_dwordx4 v2, s[36:39], s56 offen lds\n"
        "s_mov_b32 exec_hi, -1\n"
        "v_mfma_f32_16x16x32_bf16 v[240:243], v[74:77], v[114:117], v[240:243]\n"
        "ds_read_u16_d16_hi v30, v8 offset:8192\n"
        "v_mfma_f32_16x16x32_bf16 v[240:243], v[70:73], v[118:121], v[240:243]\n"
        "ds_read_u16_d16_hi v31, v8 offset:9216\n"
        "v_mfma_f32_16x16x32_bf16 v[240:243], v[66:69], v[122:125], v[240:243]\n"
        "ds_read_u16_d16_hi v32, v8 offset:10240\n"
        "v_mfma_f32_16x16x32_bf16 v[240:243], v[62:65], v[126:129], v[240:243]\n"
        "ds_read_u16_d16_hi v33, v8 offset:11264\n"
        "v_mfma_f32_16x16x32_bf16 v[240:243], v[58:61], v[130:133], v[240:243]\n"
        "ds_read_b64_tr_b16 v[34:35], v7 offset:512\n"
        "v_mfma_f32_16x16x32_bf16 v[240:243], v[54:57], v[134:137], v[240:243]\n"
        "ds_read_b64_tr_b16 v[36:37], v7 offset:17664\n"
        "v_mfma_f32_16x16x32_bf16 v[240:243], v[50:53], v[138:141], v[240:243]\n"
        "ds_read_b64_tr_b16 v[152:153], v5 offset:20480\n"
        "v_mfma_f32_16x16x32_bf16 v[240:243], v[46:49], v[142:145], v[240:243]\n"
        "ds_read_b64_tr_b16 v[154:155], v5 offset:21504\n"
        "v_and_b32_e64 v40, s31, v129\n"
        "v_mfma_f32_16x16x32_bf16 v[244:247], v[78:81], v[112:115], 0\n"
        "ds_read_b64_tr_b16 v[156:157], v5 offset:22528\n"
        "v_and_b32_e64 v41, s31, v131\n"
        "v_mfma_f32_16x16x32_bf16 v[244:247], v[74:77], v[116:119], v[244:247]\n"
        "v_and_b32 v42, s31, v133\n"
        "v_and_b32 v43, s31, v135\n"
        "v_mfma_f32_16x16x32_bf16 v[244:247], v[70:73], v[120:123], v[244:247]\n"
        "v_fmac_f32 v208, s58, v40\n"
        "v_fmac_f32 v212, s58, v41\n"
        "v_mfma_f32_16x16x32_bf16 v[244:247], v[66:69], v[124:127], v[244:247]\n"
        "v_fmac_f32 v216, s58, v42\n"
        "v_fmac_f32 v220, s58, v43\n"
        "v_mfma_f32_16x16x32_bf16 v[244:247], v[62:65], v[128:131], v[244:247]\n"
        "v_cndmask_b32 v40, v211, v207, s[60:61]\n"
        "v_cndmask_b32 v41, v215, v211, s[60:61]\n"
        "v_mfma_f32_16x16x32_bf16 v[244:247], v[58:61], v[132:135], v[244:247]\n"
        "v_cndmask_b32 v42, v219, v215, s[60:61]\n"
        "v_cndmask_b32 v43, v223, v219, s[60:61]\n"
        "v_mfma_f32_16x16x32_bf16 v[244:247], v[54:57], v[136:139], v[244:247]\n"
        "v_add_f32_dpp v210, v40, v210 wave_ror:1 row_mask:0xf bank_mask:0xf bound_ctrl:1\n"
        "v_add_f32_dpp v214, v41, v214 wave_ror:1 row_mask:0xf bank_mask:0xf bound_ctrl:1\n"
        "v_mfma_f32_16x16x32_bf16 v[244:247], v[50:53], v[140:143], v[244:247]\n"
        "v_add_f32_dpp v218, v42, v218 wave_ror:1 row_mask:0xf bank_mask:0xf bound_ctrl:1\n"
        "v_add_f32_dpp v222, v43, v222 wave_ror:1 row_mask:0xf bank_mask:0xf bound_ctrl:1\n"
        "v_mfma_f32_16x16x32_bf16 v[244:247], v[46:49], v[144:147], v[244:247]\n"
        "v_cndmask_b32 v40, v210, v206, s[60:61]\n"
        "v_cndmask_b32 v41, v214, v210, s[60:61]\n"
        "v_mfma_f32_16x16x32_bf16 v[248:251], v[82:85], v[110:113], 0\n"
        "v_cndmask_b32 v42, v218, v214, s[60:61]\n"
        "v_cndmask_b32 v43, v222, v218, s[60:61]\n"
        "v_mfma_f32_16x16x32_bf16 v[248:251], v[78:81], v[114:117], v[248:251]\n"
        "v_add_f32_dpp v209, v40, v209 wave_ror:1 row_mask:0xf bank_mask:0xf bound_ctrl:1\n"
        "v_add_f32_dpp v213, v41, v213 wave_ror:1 row_mask:0xf bank_mask:0xf bound_ctrl:1\n"
        "v_mfma_f32_16x16x32_bf16 v[248:251], v[74:77], v[118:121], v[248:251]\n"
        "v_add_f32_dpp v217, v42, v217 wave_ror:1 row_mask:0xf bank_mask:0xf bound_ctrl:1\n"
        "v_add_f32_dpp v221, v43, v221 wave_ror:1 row_mask:0xf bank_mask:0xf bound_ctrl:1\n"
        "v_mfma_f32_16x16x32_bf16 v[248:251], v[70:73], v[122:125], v[248:251]\n"
        "v_cndmask_b32 v40, v209, v205, s[60:61]\n"
        "v_cndmask_b32 v41, v213, v209, s[60:61]\n"
        "v_mfma_f32_16x16x32_bf16 v[248:251], v[66:69], v[126:129], v[248:251]\n"
        "v_cndmask_b32 v42, v217, v213, s[60:61]\n"
        "v_cndmask_b32 v43, v221, v217, s[60:61]\n"
        "v_mfma_f32_16x16x32_bf16 v[248:251], v[62:65], v[130:133], v[248:251]\n"
        "v_add_f32_dpp v208, v40, v208 wave_ror:1 row_mask:0xf bank_mask:0xf bound_ctrl:1\n"
        "v_add_f32_dpp v212, v41, v212 wave_ror:1 row_mask:0xf bank_mask:0xf bound_ctrl:1\n"
        "v_mfma_f32_16x16x32_bf16 v[248:251], v[58:61], v[134:137], v[248:251]\n"
        "v_add_f32_dpp v216, v42, v216 wave_ror:1 row_mask:0xf bank_mask:0xf bound_ctrl:1\n"
        "v_add_f32_dpp v220, v43, v220 wave_ror:1 row_mask:0xf bank_mask:0xf bound_ctrl:1\n"
        "v_mfma_f32_16x16x32_bf16 v[248:251], v[54:57], v[138:141], v[248:251]\n"
        "s_waitcnt lgkmcnt(8)\n"
        "v_mul_f32 v208, v30, v208\n"
        "s_waitcnt lgkmcnt(7)\n"
        "v_mul_f32 v212, v31, v212\n"
        "v_mfma_f32_16x16x32_bf16 v[248:251], v[50:53], v[142:145], v[248:251]\n"
        "s_waitcnt lgkmcnt(6)\n"
        "v_mul_f32 v216, v32, v216\n"
        "s_waitcnt lgkmcnt(5)\n"
        "v_mul_f32 v220, v33, v220\n"
        "v_mfma_f32_16x16x32_bf16 v[248:251], v[46:49], v[146:149], v[248:251]\n"
        "v_cvt_pk_bf16_f32 v44, v208, v212\n"
        "v_cvt_pk_bf16_f32 v45, v216, v220\n"
        "v_mfma_f32_16x16x32_bf16 v[252:255], v[82:85], v[112:115], 0\n"
        "ds_write_b64 v6, v[44:45] offset:1024\n"
        "v_mfma_f32_16x16x32_bf16 v[252:255], v[78:81], v[116:119], v[252:255]\n"
        "v_mfma_f32_16x16x32_bf16 v[252:255], v[74:77], v[120:123], v[252:255]\n"
        "v_mfma_f32_16x16x32_bf16 v[252:255], v[70:73], v[124:127], v[252:255]\n"
        "v_mfma_f32_16x16x32_bf16 v[252:255], v[66:69], v[128:131], v[252:255]\n"
        "v_mfma_f32_16x16x32_bf16 v[252:255], v[62:65], v[132:135], v[252:255]\n"
        "v_mfma_f32_16x16x32_bf16 v[252:255], v[58:61], v[136:139], v[252:255]\n"
        "v_mfma_f32_16x16x32_bf16 v[252:255], v[54:57], v[140:143], v[252:255]\n"
        "s_mov_b32 s57, 655360\n"
        "s_waitcnt lgkmcnt(4)\n"
        "s_mov_b32 exec_hi, 0\n"
        "buffer_store_dwordx4 v[34:37], v9, s[48:51], s57 offen\n"
        "s_mov_b32 exec_hi, -1\n"
        "s_waitcnt vmcnt(3) lgkmcnt(0)\n"
        "s_barrier\n"
        "v_mfma_f32_16x16x32_bf16 v[252:255], v[50:53], v[144:147], v[252:255]\n"
        "s_add_u32 m0, s54, 20480\n"
        "s_mov_b32 s56, 1638400\n"
        "s_mov_b32 exec_hi, 0\n"
        "buffer_load_dwordx4 v2, s[36:39], s56 offen lds\n"
        "s_mov_b32 exec_hi, -1\n"
        "v_mfma_f32_16x16x32_bf16 v[252:255], v[46:49], v[148:151], v[252:255]\n"
        "ds_read_u16_d16_hi v30, v8 offset:12288\n"
        "v_mfma_f32_16x16x32_bf16 v[176:179], v[86:89], v[110:113], 0\n"
        "ds_read_u16_d16_hi v31, v8 offset:13312\n"
        "v_mfma_f32_16x16x32_bf16 v[176:179], v[82:85], v[114:117], v[176:179]\n"
        "ds_read_u16_d16_hi v32, v8 offset:14336\n"
        "v_mfma_f32_16x16x32_bf16 v[176:179], v[78:81], v[118:121], v[176:179]\n"
        "ds_read_u16_d16_hi v33, v8 offset:15360\n"
        "v_mfma_f32_16x16x32_bf16 v[176:179], v[74:77], v[122:125], v[176:179]\n"
        "ds_read_b64_tr_b16 v[34:35], v7 offset:1024\n"
        "v_mfma_f32_16x16x32_bf16 v[176:179], v[70:73], v[126:129], v[176:179]\n"
        "ds_read_b64_tr_b16 v[36:37], v7 offset:18176\n"
        "v_mfma_f32_16x16x32_bf16 v[176:179], v[66:69], v[130:133], v[176:179]\n"
        "ds_read_b64_tr_b16 v[158:159], v5 offset:23552\n"
        "v_mfma_f32_16x16x32_bf16 v[176:179], v[62:65], v[134:137], v[176:179]\n"
        "ds_read_b64_tr_b16 v[160:161], v5 offset:24576\n"
        "v_and_b32 v40, s31, v137\n"
        "v_mfma_f32_16x16x32_bf16 v[176:179], v[58:61], v[138:141], v[176:179]\n"
        "ds_read_b64_tr_b16 v[162:163], v5 offset:25600\n"
        "v_and_b32_e64 v41, s31, v139\n"
        "v_mfma_f32_16x16x32_bf16 v[176:179], v[54:57], v[142:145], v[176:179]\n"
        "v_and_b32 v42, s31, v141\n"
        "v_and_b32 v43, s31, v143\n"
        "v_mfma_f32_16x16x32_bf16 v[176:179], v[50:53], v[146:149], v[176:179]\n"
        "v_fmac_f32 v224, s58, v40\n"
        "v_fmac_f32 v228, s58, v41\n"
        "v_mfma_f32_16x16x32_bf16 v[176:179], v[46:49], v[150:153], v[176:179]\n"
        "v_fmac_f32 v232, s58, v42\n"
        "v_fmac_f32 v236, s58, v43\n"
        "v_mfma_f32_16x16x32_bf16 v[180:183], v[86:89], v[112:115], 0\n"
        "v_cndmask_b32 v40, v227, v223, s[60:61]\n"
        "v_cndmask_b32 v41, v231, v227, s[60:61]\n"
        "v_mfma_f32_16x16x32_bf16 v[180:183], v[82:85], v[116:119], v[180:183]\n"
        "v_cndmask_b32 v42, v235, v231, s[60:61]\n"
        "v_cndmask_b32 v43, v239, v235, s[60:61]\n"
        "v_mfma_f32_16x16x32_bf16 v[180:183], v[78:81], v[120:123], v[180:183]\n"
        "v_add_f32_dpp v226, v40, v226 wave_ror:1 row_mask:0xf bank_mask:0xf bound_ctrl:1\n"
        "v_add_f32_dpp v230, v41, v230 wave_ror:1 row_mask:0xf bank_mask:0xf bound_ctrl:1\n"
        "v_mfma_f32_16x16x32_bf16 v[180:183], v[74:77], v[124:127], v[180:183]\n"
        "v_add_f32_dpp v234, v42, v234 wave_ror:1 row_mask:0xf bank_mask:0xf bound_ctrl:1\n"
        "v_add_f32_dpp v238, v43, v238 wave_ror:1 row_mask:0xf bank_mask:0xf bound_ctrl:1\n"
        "v_mfma_f32_16x16x32_bf16 v[180:183], v[70:73], v[128:131], v[180:183]\n"
        "v_cndmask_b32 v40, v226, v222, s[60:61]\n"
        "v_cndmask_b32 v41, v230, v226, s[60:61]\n"
        "v_mfma_f32_16x16x32_bf16 v[180:183], v[66:69], v[132:135], v[180:183]\n"
        "v_cndmask_b32 v42, v234, v230, s[60:61]\n"
        "v_cndmask_b32 v43, v238, v234, s[60:61]\n"
        "v_mfma_f32_16x16x32_bf16 v[180:183], v[62:65], v[136:139], v[180:183]\n"
        "v_add_f32_dpp v225, v40, v225 wave_ror:1 row_mask:0xf bank_mask:0xf bound_ctrl:1\n"
        "v_add_f32_dpp v229, v41, v229 wave_ror:1 row_mask:0xf bank_mask:0xf bound_ctrl:1\n"
        "v_mfma_f32_16x16x32_bf16 v[180:183], v[58:61], v[140:143], v[180:183]\n"
        "v_add_f32_dpp v233, v42, v233 wave_ror:1 row_mask:0xf bank_mask:0xf bound_ctrl:1\n"
        "v_add_f32_dpp v237, v43, v237 wave_ror:1 row_mask:0xf bank_mask:0xf bound_ctrl:1\n"
        "v_mfma_f32_16x16x32_bf16 v[180:183], v[54:57], v[144:147], v[180:183]\n"
        "v_cndmask_b32 v40, v225, v221, s[60:61]\n"
        "v_cndmask_b32 v41, v229, v225, s[60:61]\n"
        "v_mfma_f32_16x16x32_bf16 v[180:183], v[50:53], v[148:151], v[180:183]\n"
        "v_cndmask_b32 v42, v233, v229, s[60:61]\n"
        "v_cndmask_b32 v43, v237, v233, s[60:61]\n"
        "v_mfma_f32_16x16x32_bf16 v[180:183], v[46:49], v[152:155], v[180:183]\n"
        "v_add_f32_dpp v224, v40, v224 wave_ror:1 row_mask:0xf bank_mask:0xf bound_ctrl:1\n"
        "v_add_f32_dpp v228, v41, v228 wave_ror:1 row_mask:0xf bank_mask:0xf bound_ctrl:1\n"
        "v_mfma_f32_16x16x32_bf16 v[184:187], v[90:93], v[110:113], 0\n"
        "v_add_f32_dpp v232, v42, v232 wave_ror:1 row_mask:0xf bank_mask:0xf bound_ctrl:1\n"
        "v_add_f32_dpp v236, v43, v236 wave_ror:1 row_mask:0xf bank_mask:0xf bound_ctrl:1\n"
        "v_mfma_f32_16x16x32_bf16 v[184:187], v[86:89], v[114:117], v[184:187]\n"
        "s_waitcnt lgkmcnt(8)\n"
        "v_mul_f32 v224, v30, v224\n"
        "s_waitcnt lgkmcnt(7)\n"
        "v_mul_f32 v228, v31, v228\n"
        "v_mfma_f32_16x16x32_bf16 v[184:187], v[82:85], v[118:121], v[184:187]\n"
        "s_waitcnt lgkmcnt(6)\n"
        "v_mul_f32 v232, v32, v232\n"
        "s_waitcnt lgkmcnt(5)\n"
        "v_mul_f32 v236, v33, v236\n"
        "v_mfma_f32_16x16x32_bf16 v[184:187], v[78:81], v[122:125], v[184:187]\n"
        "v_cvt_pk_bf16_f32 v44, v224, v228\n"
        "v_cvt_pk_bf16_f32 v45, v232, v236\n"
        "v_mfma_f32_16x16x32_bf16 v[184:187], v[74:77], v[126:129], v[184:187]\n"
        "ds_write_b64 v6, v[44:45] offset:1536\n"
        "v_mfma_f32_16x16x32_bf16 v[184:187], v[70:73], v[130:133], v[184:187]\n"
        "v_mfma_f32_16x16x32_bf16 v[184:187], v[66:69], v[134:137], v[184:187]\n"
        "v_mfma_f32_16x16x32_bf16 v[184:187], v[62:65], v[138:141], v[184:187]\n"
        "v_mfma_f32_16x16x32_bf16 v[184:187], v[58:61], v[142:145], v[184:187]\n"
        "v_mfma_f32_16x16x32_bf16 v[184:187], v[54:57], v[146:149], v[184:187]\n"
        "v_mfma_f32_16x16x32_bf16 v[184:187], v[50:53], v[150:153], v[184:187]\n"
        "v_mfma_f32_16x16x32_bf16 v[184:187], v[46:49], v[154:157], v[184:187]\n"
        "s_mov_b32 s57, 1310720\n"
        "s_waitcnt lgkmcnt(4)\n"
        "s_mov_b32 exec_hi, 0\n"
        "buffer_store_dwordx4 v[34:37], v9, s[48:51], s57 offen\n"
        "s_mov_b32 exec_hi, -1\n"
        "s_waitcnt vmcnt(3) lgkmcnt(0)\n"
        "s_barrier\n"
        "v_mfma_f32_16x16x32_bf16 v[188:191], v[90:93], v[112:115], 0\n"
        "s_add_u32 m0, s54, 24576\n"
        "s_mov_b32 s56, 1966080\n"
        "s_mov_b32 exec_hi, 0\n"
        "buffer_load_dwordx4 v2, s[36:39], s56 offen lds\n"
        "s_mov_b32 exec_hi, -1\n"
        "v_mfma_f32_16x16x32_bf16 v[188:191], v[86:89], v[116:119], v[188:191]\n"
        "ds_read_u16_d16_hi v30, v8 offset:16384\n"
        "v_mfma_f32_16x16x32_bf16 v[188:191], v[82:85], v[120:123], v[188:191]\n"
        "ds_read_u16_d16_hi v31, v8 offset:17408\n"
        "v_mfma_f32_16x16x32_bf16 v[188:191], v[78:81], v[124:127], v[188:191]\n"
        "ds_read_u16_d16_hi v32, v8 offset:18432\n"
        "v_mfma_f32_16x16x32_bf16 v[188:191], v[74:77], v[128:131], v[188:191]\n"
        "ds_read_u16_d16_hi v33, v8 offset:19456\n"
        "v_mfma_f32_16x16x32_bf16 v[188:191], v[70:73], v[132:135], v[188:191]\n"
        "ds_read_b64_tr_b16 v[34:35], v7 offset:1536\n"
        "v_mfma_f32_16x16x32_bf16 v[188:191], v[66:69], v[136:139], v[188:191]\n"
        "ds_read_b64_tr_b16 v[36:37], v7 offset:18688\n"
        "v_mfma_f32_16x16x32_bf16 v[188:191], v[62:65], v[140:143], v[188:191]\n"
        "ds_read_b64_tr_b16 v[164:165], v5 offset:26624\n"
        "v_mfma_f32_16x16x32_bf16 v[188:191], v[58:61], v[144:147], v[188:191]\n"
        "ds_read_b64_tr_b16 v[166:167], v5 offset:27648\n"
        "v_and_b32 v40, s31, v145\n"
        "v_mfma_f32_16x16x32_bf16 v[188:191], v[54:57], v[148:151], v[188:191]\n"
        "ds_read_b64_tr_b16 v[168:169], v5 offset:28672\n"
        "v_and_b32_e64 v41, s31, v147\n"
        "v_mfma_f32_16x16x32_bf16 v[188:191], v[50:53], v[152:155], v[188:191]\n"
        "v_and_b32 v42, s31, v149\n"
        "v_and_b32 v43, s31, v151\n"
        "v_mfma_f32_16x16x32_bf16 v[188:191], v[46:49], v[156:159], v[188:191]\n"
        "v_fmac_f32 v240, s58, v40\n"
        "v_fmac_f32 v244, s58, v41\n"
        "v_mfma_f32_16x16x32_bf16 v[192:195], v[94:97], v[110:113], 0\n"
        "v_fmac_f32 v248, s58, v42\n"
        "v_fmac_f32 v252, s58, v43\n"
        "v_mfma_f32_16x16x32_bf16 v[192:195], v[90:93], v[114:117], v[192:195]\n"
        "v_cndmask_b32 v40, v243, v239, s[60:61]\n"
        "v_cndmask_b32 v41, v247, v243, s[60:61]\n"
        "v_mfma_f32_16x16x32_bf16 v[192:195], v[86:89], v[118:121], v[192:195]\n"
        "v_cndmask_b32 v42, v251, v247, s[60:61]\n"
        "v_cndmask_b32 v43, v255, v251, s[60:61]\n"
        "v_mfma_f32_16x16x32_bf16 v[192:195], v[82:85], v[122:125], v[192:195]\n"
        "v_add_f32_dpp v242, v40, v242 wave_ror:1 row_mask:0xf bank_mask:0xf bound_ctrl:1\n"
        "v_add_f32_dpp v246, v41, v246 wave_ror:1 row_mask:0xf bank_mask:0xf bound_ctrl:1\n"
        "v_mfma_f32_16x16x32_bf16 v[192:195], v[78:81], v[126:129], v[192:195]\n"
        "v_add_f32_dpp v250, v42, v250 wave_ror:1 row_mask:0xf bank_mask:0xf bound_ctrl:1\n"
        "v_add_f32_dpp v254, v43, v254 wave_ror:1 row_mask:0xf bank_mask:0xf bound_ctrl:1\n"
        "v_mfma_f32_16x16x32_bf16 v[192:195], v[74:77], v[130:133], v[192:195]\n"
        "v_cndmask_b32 v40, v242, v238, s[60:61]\n"
        "v_cndmask_b32 v41, v246, v242, s[60:61]\n"
        "v_mfma_f32_16x16x32_bf16 v[192:195], v[70:73], v[134:137], v[192:195]\n"
        "v_cndmask_b32 v42, v250, v246, s[60:61]\n"
        "v_cndmask_b32 v43, v254, v250, s[60:61]\n"
        "v_mfma_f32_16x16x32_bf16 v[192:195], v[66:69], v[138:141], v[192:195]\n"
        "v_add_f32_dpp v241, v40, v241 wave_ror:1 row_mask:0xf bank_mask:0xf bound_ctrl:1\n"
        "v_add_f32_dpp v245, v41, v245 wave_ror:1 row_mask:0xf bank_mask:0xf bound_ctrl:1\n"
        "v_mfma_f32_16x16x32_bf16 v[192:195], v[62:65], v[142:145], v[192:195]\n"
        "v_add_f32_dpp v249, v42, v249 wave_ror:1 row_mask:0xf bank_mask:0xf bound_ctrl:1\n"
        "v_add_f32_dpp v253, v43, v253 wave_ror:1 row_mask:0xf bank_mask:0xf bound_ctrl:1\n"
        "v_mfma_f32_16x16x32_bf16 v[192:195], v[58:61], v[146:149], v[192:195]\n"
        "v_cndmask_b32 v40, v241, v237, s[60:61]\n"
        "v_cndmask_b32 v41, v245, v241, s[60:61]\n"
        "v_mfma_f32_16x16x32_bf16 v[192:195], v[54:57], v[150:153], v[192:195]\n"
        "v_cndmask_b32 v42, v249, v245, s[60:61]\n"
        "v_cndmask_b32 v43, v253, v249, s[60:61]\n"
        "v_mfma_f32_16x16x32_bf16 v[192:195], v[50:53], v[154:157], v[192:195]\n"
        "v_add_f32_dpp v240, v40, v240 wave_ror:1 row_mask:0xf bank_mask:0xf bound_ctrl:1\n"
        "v_add_f32_dpp v244, v41, v244 wave_ror:1 row_mask:0xf bank_mask:0xf bound_ctrl:1\n"
        "v_mfma_f32_16x16x32_bf16 v[192:195], v[46:49], v[158:161], v[192:195]\n"
        "v_add_f32_dpp v248, v42, v248 wave_ror:1 row_mask:0xf bank_mask:0xf bound_ctrl:1\n"
        "v_add_f32_dpp v252, v43, v252 wave_ror:1 row_mask:0xf bank_mask:0xf bound_ctrl:1\n"
        "v_mfma_f32_16x16x32_bf16 v[196:199], v[94:97], v[112:115], 0\n"
        "s_waitcnt lgkmcnt(8)\n"
        "v_mul_f32 v240, v30, v240\n"
        "s_waitcnt lgkmcnt(7)\n"
        "v_mul_f32 v244, v31, v244\n"
        "v_mfma_f32_16x16x32_bf16 v[196:199], v[90:93], v[116:119], v[196:199]\n"
        "s_waitcnt lgkmcnt(6)\n"
        "v_mul_f32 v248, v32, v248\n"
        "s_waitcnt lgkmcnt(5)\n"
        "v_mul_f32 v252, v33, v252\n"
        "v_mfma_f32_16x16x32_bf16 v[196:199], v[86:89], v[120:123], v[196:199]\n"
        "v_cvt_pk_bf16_f32 v44, v240, v244\n"
        "v_cvt_pk_bf16_f32 v45, v248, v252\n"
        "v_mfma_f32_16x16x32_bf16 v[196:199], v[82:85], v[124:127], v[196:199]\n"
        "ds_write_b64 v6, v[44:45] offset:2048\n"
        "v_mfma_f32_16x16x32_bf16 v[196:199], v[78:81], v[128:131], v[196:199]\n"
        "v_mfma_f32_16x16x32_bf16 v[196:199], v[74:77], v[132:135], v[196:199]\n"
        "v_mfma_f32_16x16x32_bf16 v[196:199], v[70:73], v[136:139], v[196:199]\n"
        "v_mfma_f32_16x16x32_bf16 v[196:199], v[66:69], v[140:143], v[196:199]\n"
        "v_mfma_f32_16x16x32_bf16 v[196:199], v[62:65], v[144:147], v[196:199]\n"
        "v_mfma_f32_16x16x32_bf16 v[196:199], v[58:61], v[148:151], v[196:199]\n"
        "v_mfma_f32_16x16x32_bf16 v[196:199], v[54:57], v[152:155], v[196:199]\n"
        "s_mov_b32 s57, 1966080\n"
        "s_waitcnt lgkmcnt(4)\n"
        "s_mov_b32 exec_hi, 0\n"
        "buffer_store_dwordx4 v[34:37], v9, s[48:51], s57 offen\n"
        "s_mov_b32 exec_hi, -1\n"
        "s_waitcnt vmcnt(3) lgkmcnt(0)\n"
        "s_barrier\n"
        "v_mfma_f32_16x16x32_bf16 v[196:199], v[50:53], v[156:159], v[196:199]\n"
        "s_add_u32 m0, s54, 28672\n"
        "s_mov_b32 s56, 2293760\n"
        "s_mov_b32 exec_hi, 0\n"
        "buffer_load_dwordx4 v2, s[36:39], s56 offen lds\n"
        "s_mov_b32 exec_hi, -1\n"
        "v_mfma_f32_16x16x32_bf16 v[196:199], v[46:49], v[160:163], v[196:199]\n"
        "ds_read_u16_d16_hi v30, v8 offset:20480\n"
        "v_mfma_f32_16x16x32_bf16 v[200:203], v[98:101], v[110:113], 0\n"
        "ds_read_u16_d16_hi v31, v8 offset:21504\n"
        "v_mfma_f32_16x16x32_bf16 v[200:203], v[94:97], v[114:117], v[200:203]\n"
        "ds_read_u16_d16_hi v32, v8 offset:22528\n"
        "v_mfma_f32_16x16x32_bf16 v[200:203], v[90:93], v[118:121], v[200:203]\n"
        "ds_read_u16_d16_hi v33, v8 offset:23552\n"
        "v_mfma_f32_16x16x32_bf16 v[200:203], v[86:89], v[122:125], v[200:203]\n"
        "ds_read_b64_tr_b16 v[34:35], v7 offset:2048\n"
        "v_mfma_f32_16x16x32_bf16 v[200:203], v[82:85], v[126:129], v[200:203]\n"
        "ds_read_b64_tr_b16 v[36:37], v7 offset:19200\n"
        "v_mfma_f32_16x16x32_bf16 v[200:203], v[78:81], v[130:133], v[200:203]\n"
        "ds_read_b64_tr_b16 v[170:171], v5 offset:29696\n"
        "v_mfma_f32_16x16x32_bf16 v[200:203], v[74:77], v[134:137], v[200:203]\n"
        "ds_read_b64_tr_b16 v[172:173], v5 offset:30720\n"
        "v_and_b32 v40, s31, v153\n"
        "v_mfma_f32_16x16x32_bf16 v[200:203], v[70:73], v[138:141], v[200:203]\n"
        "v_and_b32 v41, s31, v155\n"
        "v_and_b32 v42, s31, v157\n"
        "v_mfma_f32_16x16x32_bf16 v[200:203], v[66:69], v[142:145], v[200:203]\n"
        "v_and_b32 v43, s31, v159\n"
        "v_fmac_f32 v176, s58, v40\n"
        "v_mfma_f32_16x16x32_bf16 v[200:203], v[62:65], v[146:149], v[200:203]\n"
        "v_fmac_f32 v180, s58, v41\n"
        "v_fmac_f32 v184, s58, v42\n"
        "v_mfma_f32_16x16x32_bf16 v[200:203], v[58:61], v[150:153], v[200:203]\n"
        "v_fmac_f32_e64 v188, s58, v43\n"
        "v_cndmask_b32 v40, v179, v255, s[60:61]\n"
        "v_mfma_f32_16x16x32_bf16 v[200:203], v[54:57], v[154:157], v[200:203]\n"
        "v_cndmask_b32 v41, v183, v179, s[60:61]\n"
        "v_cndmask_b32 v42, v187, v183, s[60:61]\n"
        "v_mfma_f32_16x16x32_bf16 v[200:203], v[50:53], v[158:161], v[200:203]\n"
        "v_cndmask_b32 v43, v191, v187, s[60:61]\n"
        "v_add_f32_dpp v178, v40, v178 wave_ror:1 row_mask:0xf bank_mask:0xf bound_ctrl:1\n"
        "v_mfma_f32_16x16x32_bf16 v[200:203], v[46:49], v[162:165], v[200:203]\n"
        "v_add_f32_dpp v182, v41, v182 wave_ror:1 row_mask:0xf bank_mask:0xf bound_ctrl:1\n"
        "v_add_f32_dpp v186, v42, v186 wave_ror:1 row_mask:0xf bank_mask:0xf bound_ctrl:1\n"
        "v_mfma_f32_16x16x32_bf16 v[204:207], v[98:101], v[112:115], 0\n"
        "v_add_f32_dpp v190, v43, v190 wave_ror:1 row_mask:0xf bank_mask:0xf bound_ctrl:1\n"
        "v_cndmask_b32 v40, v178, v254, s[60:61]\n"
        "v_mfma_f32_16x16x32_bf16 v[204:207], v[94:97], v[116:119], v[204:207]\n"
        "v_cndmask_b32 v41, v182, v178, s[60:61]\n"
        "v_cndmask_b32 v42, v186, v182, s[60:61]\n"
        "v_mfma_f32_16x16x32_bf16 v[204:207], v[90:93], v[120:123], v[204:207]\n"
        "v_cndmask_b32 v43, v190, v186, s[60:61]\n"
        "v_add_f32_dpp v177, v40, v177 wave_ror:1 row_mask:0xf bank_mask:0xf bound_ctrl:1\n"
        "v_mfma_f32_16x16x32_bf16 v[204:207], v[86:89], v[124:127], v[204:207]\n"
        "v_add_f32_dpp v181, v41, v181 wave_ror:1 row_mask:0xf bank_mask:0xf bound_ctrl:1\n"
        "v_add_f32_dpp v185, v42, v185 wave_ror:1 row_mask:0xf bank_mask:0xf bound_ctrl:1\n"
        "v_mfma_f32_16x16x32_bf16 v[204:207], v[82:85], v[128:131], v[204:207]\n"
        "v_add_f32_dpp v189, v43, v189 wave_ror:1 row_mask:0xf bank_mask:0xf bound_ctrl:1\n"
        "v_cndmask_b32 v40, v177, v253, s[60:61]\n"
        "v_mfma_f32_16x16x32_bf16 v[204:207], v[78:81], v[132:135], v[204:207]\n"
        "v_cndmask_b32 v41, v181, v177, s[60:61]\n"
        "v_cndmask_b32 v42, v185, v181, s[60:61]\n"
        "v_mfma_f32_16x16x32_bf16 v[204:207], v[74:77], v[136:139], v[204:207]\n"
        "v_cndmask_b32 v43, v189, v185, s[60:61]\n"
        "v_add_f32_dpp v176, v40, v176 wave_ror:1 row_mask:0xf bank_mask:0xf bound_ctrl:1\n"
        "v_mfma_f32_16x16x32_bf16 v[204:207], v[70:73], v[140:143], v[204:207]\n"
        "v_add_f32_dpp v180, v41, v180 wave_ror:1 row_mask:0xf bank_mask:0xf bound_ctrl:1\n"
        "v_add_f32_dpp v184, v42, v184 wave_ror:1 row_mask:0xf bank_mask:0xf bound_ctrl:1\n"
        "v_mfma_f32_16x16x32_bf16 v[204:207], v[66:69], v[144:147], v[204:207]\n"
        "v_add_f32_dpp v188, v43, v188 wave_ror:1 row_mask:0xf bank_mask:0xf bound_ctrl:1\n"
        "s_waitcnt lgkmcnt(7)\n"
        "v_mul_f32 v176, v30, v176\n"
        "v_mfma_f32_16x16x32_bf16 v[204:207], v[62:65], v[148:151], v[204:207]\n"
        "s_waitcnt lgkmcnt(6)\n"
        "v_mul_f32 v180, v31, v180\n"
        "s_waitcnt lgkmcnt(5)\n"
        "v_mul_f32 v184, v32, v184\n"
        "v_mfma_f32_16x16x32_bf16 v[204:207], v[58:61], v[152:155], v[204:207]\n"
        "s_waitcnt lgkmcnt(4)\n"
        "v_mul_f32 v188, v33, v188\n"
        "v_cvt_pk_bf16_f32 v44, v176, v180\n"
        "v_mfma_f32_16x16x32_bf16 v[204:207], v[54:57], v[156:159], v[204:207]\n"
        "v_cvt_pk_bf16_f32 v45, v184, v188\n"
        "ds_write_b64 v6, v[44:45] offset:2560\n"
        "v_mfma_f32_16x16x32_bf16 v[204:207], v[50:53], v[160:163], v[204:207]\n"
        "v_mfma_f32_16x16x32_bf16 v[204:207], v[46:49], v[164:167], v[204:207]\n"
        "v_mfma_f32_16x16x32_bf16 v[208:211], v[102:105], v[110:113], 0\n"
        "v_mfma_f32_16x16x32_bf16 v[208:211], v[98:101], v[114:117], v[208:211]\n"
        "v_mfma_f32_16x16x32_bf16 v[208:211], v[94:97], v[118:121], v[208:211]\n"
        "v_mfma_f32_16x16x32_bf16 v[208:211], v[90:93], v[122:125], v[208:211]\n"
        "v_mfma_f32_16x16x32_bf16 v[208:211], v[86:89], v[126:129], v[208:211]\n"
        "v_mfma_f32_16x16x32_bf16 v[208:211], v[82:85], v[130:133], v[208:211]\n"
        "s_mov_b32 s57, 2621440\n"
        "s_waitcnt lgkmcnt(3)\n"
        "s_mov_b32 exec_hi, 0\n"
        "buffer_store_dwordx4 v[34:37], v9, s[48:51], s57 offen\n"
        "s_mov_b32 exec_hi, -1\n"
        "s_waitcnt vmcnt(3) lgkmcnt(0)\n"
        "s_barrier\n"
        "v_mfma_f32_16x16x32_bf16 v[208:211], v[78:81], v[134:137], v[208:211]\n"
        "ds_read_u16_d16_hi v30, v8 offset:24576\n"
        "v_mfma_f32_16x16x32_bf16 v[208:211], v[74:77], v[138:141], v[208:211]\n"
        "ds_read_u16_d16_hi v31, v8 offset:25600\n"
        "v_mfma_f32_16x16x32_bf16 v[208:211], v[70:73], v[142:145], v[208:211]\n"
        "ds_read_u16_d16_hi v32, v8 offset:26624\n"
        "v_mfma_f32_16x16x32_bf16 v[208:211], v[66:69], v[146:149], v[208:211]\n"
        "ds_read_u16_d16_hi v33, v8 offset:27648\n"
        "v_mfma_f32_16x16x32_bf16 v[208:211], v[62:65], v[150:153], v[208:211]\n"
        "ds_read_b64_tr_b16 v[34:35], v7 offset:2560\n"
        "v_mfma_f32_16x16x32_bf16 v[208:211], v[58:61], v[154:157], v[208:211]\n"
        "ds_read_b64_tr_b16 v[36:37], v7 offset:19712\n"
        "v_mfma_f32_16x16x32_bf16 v[208:211], v[54:57], v[158:161], v[208:211]\n"
        "ds_read_b64_tr_b16 v[174:175], v5 offset:31744\n"
        "v_mfma_f32_16x16x32_bf16 v[208:211], v[50:53], v[162:165], v[208:211]\n"
        "v_mfma_f32_16x16x32_bf16 v[208:211], v[46:49], v[166:169], v[208:211]\n"
        "v_and_b32 v40, s31, v161\n"
        "v_and_b32_e64 v41, s31, v163\n"
        "v_mfma_f32_16x16x32_bf16 v[212:215], v[102:105], v[112:115], 0\n"
        "v_and_b32 v42, s31, v165\n"
        "v_and_b32 v43, s31, v167\n"
        "v_mfma_f32_16x16x32_bf16 v[212:215], v[98:101], v[116:119], v[212:215]\n"
        "v_fmac_f32 v192, s58, v40\n"
        "v_fmac_f32 v196, s58, v41\n"
        "v_mfma_f32_16x16x32_bf16 v[212:215], v[94:97], v[120:123], v[212:215]\n"
        "v_fmac_f32 v200, s58, v42\n"
        "v_fmac_f32 v204, s58, v43\n"
        "v_mfma_f32_16x16x32_bf16 v[212:215], v[90:93], v[124:127], v[212:215]\n"
        "v_cndmask_b32 v40, v195, v191, s[60:61]\n"
        "v_cndmask_b32 v41, v199, v195, s[60:61]\n"
        "v_mfma_f32_16x16x32_bf16 v[212:215], v[86:89], v[128:131], v[212:215]\n"
        "v_cndmask_b32 v42, v203, v199, s[60:61]\n"
        "v_cndmask_b32 v43, v207, v203, s[60:61]\n"
        "v_mfma_f32_16x16x32_bf16 v[212:215], v[82:85], v[132:135], v[212:215]\n"
        "v_add_f32_dpp v194, v40, v194 wave_ror:1 row_mask:0xf bank_mask:0xf bound_ctrl:1\n"
        "v_add_f32_dpp v198, v41, v198 wave_ror:1 row_mask:0xf bank_mask:0xf bound_ctrl:1\n"
        "v_mfma_f32_16x16x32_bf16 v[212:215], v[78:81], v[136:139], v[212:215]\n"
        "v_add_f32_dpp v202, v42, v202 wave_ror:1 row_mask:0xf bank_mask:0xf bound_ctrl:1\n"
        "v_add_f32_dpp v206, v43, v206 wave_ror:1 row_mask:0xf bank_mask:0xf bound_ctrl:1\n"
        "v_mfma_f32_16x16x32_bf16 v[212:215], v[74:77], v[140:143], v[212:215]\n"
        "v_cndmask_b32 v40, v194, v190, s[60:61]\n"
        "v_cndmask_b32 v41, v198, v194, s[60:61]\n"
        "v_mfma_f32_16x16x32_bf16 v[212:215], v[70:73], v[144:147], v[212:215]\n"
        "v_cndmask_b32 v42, v202, v198, s[60:61]\n"
        "v_cndmask_b32 v43, v206, v202, s[60:61]\n"
        "v_mfma_f32_16x16x32_bf16 v[212:215], v[66:69], v[148:151], v[212:215]\n"
        "v_add_f32_dpp v193, v40, v193 wave_ror:1 row_mask:0xf bank_mask:0xf bound_ctrl:1\n"
        "v_add_f32_dpp v197, v41, v197 wave_ror:1 row_mask:0xf bank_mask:0xf bound_ctrl:1\n"
        "v_mfma_f32_16x16x32_bf16 v[212:215], v[62:65], v[152:155], v[212:215]\n"
        "v_add_f32_dpp v201, v42, v201 wave_ror:1 row_mask:0xf bank_mask:0xf bound_ctrl:1\n"
        "v_add_f32_dpp v205, v43, v205 wave_ror:1 row_mask:0xf bank_mask:0xf bound_ctrl:1\n"
        "v_mfma_f32_16x16x32_bf16 v[212:215], v[58:61], v[156:159], v[212:215]\n"
        "v_cndmask_b32 v40, v193, v189, s[60:61]\n"
        "v_cndmask_b32 v41, v197, v193, s[60:61]\n"
        "v_mfma_f32_16x16x32_bf16 v[212:215], v[54:57], v[160:163], v[212:215]\n"
        "v_cndmask_b32 v42, v201, v197, s[60:61]\n"
        "v_cndmask_b32 v43, v205, v201, s[60:61]\n"
        "v_mfma_f32_16x16x32_bf16 v[212:215], v[50:53], v[164:167], v[212:215]\n"
        "v_add_f32_dpp v192, v40, v192 wave_ror:1 row_mask:0xf bank_mask:0xf bound_ctrl:1\n"
        "v_add_f32_dpp v196, v41, v196 wave_ror:1 row_mask:0xf bank_mask:0xf bound_ctrl:1\n"
        "v_mfma_f32_16x16x32_bf16 v[212:215], v[46:49], v[168:171], v[212:215]\n"
        "v_add_f32_dpp v200, v42, v200 wave_ror:1 row_mask:0xf bank_mask:0xf bound_ctrl:1\n"
        "v_add_f32_dpp v204, v43, v204 wave_ror:1 row_mask:0xf bank_mask:0xf bound_ctrl:1\n"
        "v_mfma_f32_16x16x32_bf16 v[216:219], v[106:109], v[110:113], 0\n"
        "s_waitcnt lgkmcnt(6)\n"
        "v_mul_f32 v192, v30, v192\n"
        "s_waitcnt lgkmcnt(5)\n"
        "v_mul_f32 v196, v31, v196\n"
        "v_mfma_f32_16x16x32_bf16 v[216:219], v[102:105], v[114:117], v[216:219]\n"
        "s_waitcnt lgkmcnt(4)\n"
        "v_mul_f32 v200, v32, v200\n"
        "s_waitcnt lgkmcnt(3)\n"
        "v_mul_f32 v204, v33, v204\n"
        "v_mfma_f32_16x16x32_bf16 v[216:219], v[98:101], v[118:121], v[216:219]\n"
        "v_cvt_pk_bf16_f32 v44, v192, v196\n"
        "v_cvt_pk_bf16_f32 v45, v200, v204\n"
        "v_mfma_f32_16x16x32_bf16 v[216:219], v[94:97], v[122:125], v[216:219]\n"
        "ds_write_b64 v6, v[44:45] offset:3072\n"
        "v_mfma_f32_16x16x32_bf16 v[216:219], v[90:93], v[126:129], v[216:219]\n"
        "v_mfma_f32_16x16x32_bf16 v[216:219], v[86:89], v[130:133], v[216:219]\n"
        "v_mfma_f32_16x16x32_bf16 v[216:219], v[82:85], v[134:137], v[216:219]\n"
        "v_mfma_f32_16x16x32_bf16 v[216:219], v[78:81], v[138:141], v[216:219]\n"
        "v_mfma_f32_16x16x32_bf16 v[216:219], v[74:77], v[142:145], v[216:219]\n"
        "v_mfma_f32_16x16x32_bf16 v[216:219], v[70:73], v[146:149], v[216:219]\n"
        "v_mfma_f32_16x16x32_bf16 v[216:219], v[66:69], v[150:153], v[216:219]\n"
        "v_mfma_f32_16x16x32_bf16 v[216:219], v[62:65], v[154:157], v[216:219]\n"
        "s_mov_b32 s57, 3276800\n"
        "s_waitcnt lgkmcnt(2)\n"
        "s_mov_b32 exec_hi, 0\n"
        "buffer_store_dwordx4 v[34:37], v9, s[48:51], s57 offen\n"
        "s_mov_b32 exec_hi, -1\n"
        "s_waitcnt vmcnt(2) lgkmcnt(0)\n"
        "s_barrier\n"
        "v_mfma_f32_16x16x32_bf16 v[216:219], v[58:61], v[158:161], v[216:219]\n"
        "ds_read_u16_d16_hi v30, v8 offset:28672\n"
        "v_mfma_f32_16x16x32_bf16 v[216:219], v[54:57], v[162:165], v[216:219]\n"
        "ds_read_u16_d16_hi v31, v8 offset:29696\n"
        "v_mfma_f32_16x16x32_bf16 v[216:219], v[50:53], v[166:169], v[216:219]\n"
        "ds_read_u16_d16_hi v32, v8 offset:30720\n"
        "v_mfma_f32_16x16x32_bf16 v[216:219], v[46:49], v[170:173], v[216:219]\n"
        "ds_read_u16_d16_hi v33, v8 offset:31744\n"
        "v_mfma_f32_16x16x32_bf16 v[220:223], v[106:109], v[112:115], 0\n"
        "ds_read_b64_tr_b16 v[34:35], v7 offset:3072\n"
        "v_mfma_f32_16x16x32_bf16 v[220:223], v[102:105], v[116:119], v[220:223]\n"
        "ds_read_b64_tr_b16 v[36:37], v7 offset:20224\n"
        "v_mfma_f32_16x16x32_bf16 v[220:223], v[98:101], v[120:123], v[220:223]\n"
        "v_mfma_f32_16x16x32_bf16 v[220:223], v[94:97], v[124:127], v[220:223]\n"
        "v_mfma_f32_16x16x32_bf16 v[220:223], v[90:93], v[128:131], v[220:223]\n"
        "v_mfma_f32_16x16x32_bf16 v[220:223], v[86:89], v[132:135], v[220:223]\n"
        "v_mfma_f32_16x16x32_bf16 v[220:223], v[82:85], v[136:139], v[220:223]\n"
        "v_mfma_f32_16x16x32_bf16 v[220:223], v[78:81], v[140:143], v[220:223]\n"
        "v_mfma_f32_16x16x32_bf16 v[220:223], v[74:77], v[144:147], v[220:223]\n"
        "v_mfma_f32_16x16x32_bf16 v[220:223], v[70:73], v[148:151], v[220:223]\n"
        "v_mfma_f32_16x16x32_bf16 v[220:223], v[66:69], v[152:155], v[220:223]\n"
        "v_mfma_f32_16x16x32_bf16 v[220:223], v[62:65], v[156:159], v[220:223]\n"
        "v_mfma_f32_16x16x32_bf16 v[220:223], v[58:61], v[160:163], v[220:223]\n"
        "v_mfma_f32_16x16x32_bf16 v[220:223], v[54:57], v[164:167], v[220:223]\n"
        "v_mfma_f32_16x16x32_bf16 v[220:223], v[50:53], v[168:171], v[220:223]\n"
        "v_mfma_f32_16x16x32_bf16 v[220:223], v[46:49], v[172:175], v[220:223]\n"
        "v_and_b32 v40, s31, v169\n"
        "v_and_b32 v41, s31, v171\n"
        "v_and_b32 v42, s31, v173\n"
        "v_and_b32 v43, s31, v175\n"
        "v_fmac_f32 v208, s58, v40\n"
        "v_fmac_f32 v212, s58, v41\n"
        "v_fmac_f32 v216, s58, v42\n"
        "s_nop 0\n"
        "v_fmac_f32 v220, s58, v43\n"
        "v_cndmask_b32 v40, v211, v207, s[60:61]\n"
        "v_cndmask_b32 v41, v215, v211, s[60:61]\n"
        "v_cndmask_b32 v42, v219, v215, s[60:61]\n"
        "v_cndmask_b32 v43, v223, v219, s[60:61]\n"
        "v_add_f32_dpp v210, v40, v210 wave_ror:1 row_mask:0xf bank_mask:0xf bound_ctrl:1\n"
        "v_add_f32_dpp v214, v41, v214 wave_ror:1 row_mask:0xf bank_mask:0xf bound_ctrl:1\n"
        "v_add_f32_dpp v218, v42, v218 wave_ror:1 row_mask:0xf bank_mask:0xf bound_ctrl:1\n"
        "v_add_f32_dpp v222, v43, v222 wave_ror:1 row_mask:0xf bank_mask:0xf bound_ctrl:1\n"
        "v_cndmask_b32 v40, v210, v206, s[60:61]\n"
        "v_cndmask_b32 v41, v214, v210, s[60:61]\n"
        "v_cndmask_b32 v42, v218, v214, s[60:61]\n"
        "v_cndmask_b32 v43, v222, v218, s[60:61]\n"
        "v_add_f32_dpp v209, v40, v209 wave_ror:1 row_mask:0xf bank_mask:0xf bound_ctrl:1\n"
        "v_add_f32_dpp v213, v41, v213 wave_ror:1 row_mask:0xf bank_mask:0xf bound_ctrl:1\n"
        "v_add_f32_dpp v217, v42, v217 wave_ror:1 row_mask:0xf bank_mask:0xf bound_ctrl:1\n"
        "v_add_f32_dpp v221, v43, v221 wave_ror:1 row_mask:0xf bank_mask:0xf bound_ctrl:1\n"
        "v_cndmask_b32 v40, v209, v205, s[60:61]\n"
        "v_cndmask_b32 v41, v213, v209, s[60:61]\n"
        "v_cndmask_b32 v42, v217, v213, s[60:61]\n"
        "v_cndmask_b32 v43, v221, v217, s[60:61]\n"
        "v_add_f32_dpp v208, v40, v208 wave_ror:1 row_mask:0xf bank_mask:0xf bound_ctrl:1\n"
        "v_add_f32_dpp v212, v41, v212 wave_ror:1 row_mask:0xf bank_mask:0xf bound_ctrl:1\n"
        "v_add_f32_dpp v216, v42, v216 wave_ror:1 row_mask:0xf bank_mask:0xf bound_ctrl:1\n"
        "v_add_f32_dpp v220, v43, v220 wave_ror:1 row_mask:0xf bank_mask:0xf bound_ctrl:1\n"
        "s_waitcnt lgkmcnt(5)\n"
        "v_mul_f32 v208, v30, v208\n"
        "s_waitcnt lgkmcnt(4)\n"
        "v_mul_f32 v212, v31, v212\n"
        "s_waitcnt lgkmcnt(3)\n"
        "v_mul_f32 v216, v32, v216\n"
        "s_waitcnt lgkmcnt(2)\n"
        "v_mul_f32 v220, v33, v220\n"
        "v_cvt_pk_bf16_f32 v44, v208, v212\n"
        "v_cvt_pk_bf16_f32 v45, v216, v220\n"
        "ds_write_b64 v6, v[44:45] offset:3584\n"
        "s_mov_b32 s57, 3932160\n"
        "s_waitcnt lgkmcnt(1)\n"
        "s_mov_b32 exec_hi, 0\n"
        "buffer_store_dwordx4 v[34:37], v9, s[48:51], s57 offen\n"
        "s_mov_b32 exec_hi, -1\n"
        "s_waitcnt lgkmcnt(0)\n"
        "s_barrier\n"
        "ds_read_b64_tr_b16 v[34:35], v7 offset:3584\n"
        "ds_read_b64_tr_b16 v[36:37], v7 offset:20736\n"
        "s_mov_b32 s57, 4587520\n"
        "s_waitcnt lgkmcnt(0)\n"
        "s_mov_b32 exec_hi, 0\n"
        "buffer_store_dwordx4 v[34:37], v9, s[48:51], s57 offen\n"
        "s_mov_b32 exec_hi, -1\n"
        "s_endpgm\n"
        ".Lspare:\n"
        "s_cmp_lt_u32 s28, 4\n"
        "s_cbranch_scc1 .Lsp2\n"
        "s_cmp_eq_u32 s28, 7\n"
        "s_cbranch_scc0 .Lend\n"
        "v_and_b32 v0, 63, v0\n"
        "s_sub_u32 s33, s30, 26\n"
        "v_mul_u32_u24 v1, 3840, v0\n"
        "v_mul_u32_u24 v2, 1280, v0\n"
        "s_waitcnt lgkmcnt(0)\n"
        "s_mov_b32 s36, s6\n"
        "s_and_b32 s37, s7, 0xffff\n"
        "s_mov_b32 s38, 2457600\n"
        "s_mov_b32 s39, 0x20000\n"
        "s_mov_b32 s48, s10\n"
        "s_and_b32 s49, s11, 0xffff\n"
        "s_mov_b32 s50, 819200\n"
        "s_mov_b32 s51, 0x20000\n"
        "s_mov_b32 s40, s12\n"
        "s_and_b32 s41, s13, 0xffff\n"
        "s_mov_b32 s42, 2621440\n"
        "s_mov_b32 s43, 0x20000\n"
        "s_mul_i32 s64, s29, 160\n"
        "s_and_b32 s64, s64, -128\n"
        "s_and_b32 s3, s33, 1\n"
        "s_lshl_b32 s3, s3, 7\n"
        "s_add_u32 s64, s64, s3\n"
        "s_lshr_b32 s3, s33, 1\n"
        "s_mul_i32 s3, s3, 81920\n"
        "s_add_u32 s64, s64, s3\n"
        "s_lshl_b32 s65, s33, 7\n"
        "v_mov_b32 v252, v0\n"
        "s_add_u32 s67, s33, 128\n"
        ".Lhk:\n"
        "s_mul_i32 s62, s67, 79\n"
        "s_lshr_b32 s62, s62, 10\n"
        "s_mul_i32 s3, s62, 13\n"
        "s_sub_u32 s63, s67, s3\n"
        "s_lshr_b32 s3, s29, 1\n"
        "s_lshl_b32 s3, s3, 3\n"
        "s_add_u32 s3, s3, s63\n"
        "s_mul_i32 s3, s3, 640\n"
        "s_add_u32 s66, s3, s62\n"
        "s_and_b32 s3, s29, 1\n"
        "s_mul_i32 s3, s3, 5\n"
        "s_add_u32 s3, s3, s63\n"
        "s_mul_i32 s62, s62, 1920\n"
        "s_add_u32 s3, s3, s62\n"
        "s_add_u32 s3, s3, 2\n"
        "s_cmp_lt_u32 s63, 8\n"
        "s_cselect_b32 s62, s66, s3\n"
        "s_movk_i32 s3, 3840\n"
        "s_cselect_b32 s3, 1280, s3\n"
        "s_mov_b32 s46, 2457600\n"
        "s_cselect_b32 s46, 2621440, s46\n"
        "v_mul_u32_u24 v253, s3, v252\n"
        "s_mov_b32 s47, 0x20000\n"
        "s_cselect_b64 s[44:45], s[4:5], s[6:7]\n"
        "s_lshl_b32 s62, s62, 7\n"
        "s_and_b32 s45, s45, 0xffff\n"
        "buffer_load_dword v254, v253, s[44:47], s62 offen\n"
        "s_waitcnt vmcnt(6)\n"
        "s_add_u32 s67, s67, 6\n"
        "s_cmp_lt_u32 s67, 130\n"
        "s_cbranch_scc1 .Lhk\n"
        "s_lshr_b32 s34, s29, 1\n"
        "s_mul_i32 s34, s34, 12\n"
        "s_add_u32 s34, s34, s33\n"
        "s_and_b32 s62, s29, 1\n"
        "s_mul_i32 s62, s62, 640\n"
        "s_add_u32 s62, s62, 2560\n"
        "s_mov_b32 s35, s33\n"
        ".Lhp1:\n"
        "s_mul_i32 s3, s34, 52\n"
        "s_lshr_b32 s3, s3, 8\n"
        "s_mul_i32 s63, s3, 245120\n"
        "s_lshl_b32 s3, s34, 7\n"
        "s_add_u32 s63, s63, s3\n"
        "s_add_u32 s63, s63, s62\n"
        "buffer_load_dword v254, v1, s[36:39], s63 offen\n"
        "s_add_u32 s34, s34, 6\n"
        "s_sub_u32 s3, s34, 50\n"
        "s_cmp_ge_u32 s34, 50\n"
        "s_cselect_b32 s34, s3, s34\n"
        "s_waitcnt vmcnt(6)\n"
        "s_add_u32 s35, s35, 6\n"
        "s_cmp_lt_u32 s35, 50\n"
        "s_cbranch_scc1 .Lhp1\n"
        "s_barrier\n"
        "s_mov_b32 s35, 3\n"
        ".Lhp2:\n"
        "buffer_load_dword v254, v2, s[40:43], s64 offen\n"
        "s_add_u32 s64, s64, 245760\n"
        "s_waitcnt vmcnt(6)\n"
        "s_sub_u32 s35, s35, 1\n"
        "s_cmp_eq_u32 s35, 0\n"
        "s_cbranch_scc0 .Lhp2\n"
        "s_lshr_b32 s34, s29, 1\n"
        "s_mul_i32 s34, s34, 12\n"
        "s_add_u32 s34, s34, s33\n"
        "s_and_b32 s62, s29, 1\n"
        "s_mul_i32 s62, s62, 640\n"
        "s_add_u32 s62, s62, 0\n"
        "s_mov_b32 s35, s33\n"
        ".Lhp3:\n"
        "s_mul_i32 s3, s34, 52\n"
        "s_lshr_b32 s3, s3, 8\n"
        "s_mul_i32 s63, s3, 245120\n"
        "s_lshl_b32 s3, s34, 7\n"
        "s_add_u32 s63, s63, s3\n"
        "s_add_u32 s63, s63, s62\n"
        "buffer_load_dword v254, v1, s[36:39], s63 offen\n"
        "s_add_u32 s34, s34, 6\n"
        "s_sub_u32 s3, s34, 50\n"
        "s_cmp_ge_u32 s34, 50\n"
        "s_cselect_b32 s34, s3, s34\n"
        "s_waitcnt vmcnt(6)\n"
        "s_add_u32 s35, s35, 6\n"
        "s_cmp_lt_u32 s35, 50\n"
        "s_cbranch_scc1 .Lhp3\n"
        "s_mul_i32 s65, s29, 102400\n"
        "s_mul_i32 s3, s33, 17152\n"
        "s_add_u32 s65, s65, s3\n"
        "v_min_u32 v3, 44, v0\n"
        "v_lshlrev_b32 v3, 7, v3\n"
        "s_mov_b32 s35, 3\n"
        ".Lhp4:\n"
        "buffer_load_dword v254, v3, s[48:51], s65 offen\n"
        "s_add_u32 s65, s65, 5760\n"
        "s_waitcnt vmcnt(6)\n"
        "s_sub_u32 s35, s35, 1\n"
        "s_cmp_eq_u32 s35, 0\n"
        "s_cbranch_scc0 .Lhp4\n"
        "s_lshl_b32 s3, s33, 13\n"
        "v_lshl_add_u32 v3, v0, 7, s3\n"
        "v_min_u32 v3, .Lend-.Lnopf, v3\n"
        "global_load_dword v254, v3, s[60:61]\n"
        "s_cmp_lt_u32 s33, 2\n"
        "s_cbranch_scc0 .Lhend\n"
        "s_mov_b32 s35, 23\n"
        ".Lhp5:\n"
        "buffer_load_dword v254, v2, s[40:43], s64 offen\n"
        "s_add_u32 s64, s64, 81920\n"
        "s_waitcnt vmcnt(6)\n"
        "s_sub_u32 s35, s35, 1\n"
        "s_cmp_eq_u32 s35, 0\n"
        "s_cbranch_scc0 .Lhp5\n"
        ".Lhend:\n"
        "s_endpgm\n"
        ".Lsp2:\n"
        "s_cmp_lt_u32 s30, 28\n"
        "s_cbranch_scc1 .Lidle\n"
        "v_and_b32 v0, 63, v0\n"
        "s_mov_b32 s35, s28\n"
        "v_lshlrev_b32 v5, 4, v0\n"
        "v_and_b32 v25, 3, v0\n"
        "v_lshlrev_b32 v1, 2, v25\n"
        "v_add_u32 v1, 163584, v1\n"
        "s_lshl_b32 s3, s35, 2\n"
        "s_add_u32 s3, s3, 163584\n"
        "v_mov_b32 v2, s3\n"
        "v_mov_b32_e64 v3, 0\n"
        "ds_write_b32 v2, v3 offset:32\n"
        "s_waitcnt lgkmcnt(0)\n"
        "s_mov_b32 s4, s26\n"
        "s_and_b32 s5, s27, 0xffff\n"
        "s_mov_b32 s6, 8388608\n"
        "s_mov_b32 s7, 0x20000\n"
        "v_mov_b32_e64 v24, 0\n"
        "buffer_load_dwordx2 v[6:7], v24, s[4:7], 0 offen sc1\n"
        "s_and_b32 s62, s29, 1\n"
        "s_lshr_b32 s33, s29, 1\n"
        "s_lshl2_add_u32 s62, s62, s33\n"
        "s_lshl2_add_u32 s62, s62, s30\n"
        "s_sub_u32 s62, s62, 28\n"
        "s_and_b32 s3, s35, 1\n"
        "s_lshl_b32 s3, s3, 10\n"
        "s_lshl_b32 s9, s62, 11\n"
        "s_add_u32 s9, s9, s3\n"
        "s_add_u32 s9, s9, 1572864\n"
        "s_lshl_b32 s8, s62, 15\n"
        "s_add_u32 s8, s8, s3\n"
        "s_add_u32 s8, s8, 262144\n"
        "v_mov_b32 v112, 0x7f817f81\n"
        "v_mov_b32 v113, 0x7f817f81\n"
        "v_mov_b32 v114, 0x7f817f81\n"
        "v_mov_b32 v115, 0x7f817f81\n"
        "buffer_store_dwordx4 v[112:115], v5, s[4:7], s9 offen sc1\n"
        "v_and_b32 v13, 1, v0\n"
        "v_lshlrev_b32_e64 v13, 4, v13\n"
        "v_mov_b32 v14, 0x3f80\n"
        "v_lshlrev_b32 v13, v13, v14\n"
        "v_and_b32 v14, 2, v0\n"
        "v_cmp_eq_u32 s[22:23], 0, v14\n"
        "v_lshlrev_b32 v15, 3, v0\n"
        "s_nop 0\n"
        "v_cndmask_b32 v22, 0, v13, s[22:23]\n"
        "v_cndmask_b32 v23, v13, 0, s[22:23]\n"
        "s_lshl_b32 s3, s62, 11\n"
        "s_add_u32 s3, s3, 65536\n"
        "v_and_b32 v13, 15, v0\n"
        "v_lshl_add_u32 v19, v13, 7, s3\n"
        "s_waitcnt vmcnt(1)\n"
        "v_add_co_u32 v6, vcc, 1, v6\n"
        "s_nop 1\n"
        "v_addc_co_u32 v7, vcc, 0, v7, vcc\n"
        "s_waitcnt vmcnt(0)\n"
        "s_barrier\n"
        "s_lshl_b32 s3, s2, 7\n"
        "s_add_u32 s3, s3, 4096\n"
        "s_lshl_b32 s63, s62, 7\n"
        "s_add_u32 s63, s63, 49152\n"
        "s_cmp_eq_u32 s35, 3\n"
        "s_cselect_b32 s3, s3, s63\n"
        "s_cmp_lt_u32 s35, 2\n"
        "s_cselect_b32 s3, 8388608, s3\n"
        "s_mov_b64 exec, 0xffff\n"
        "buffer_store_dwordx2 v[6:7], v15, s[4:7], s3 offen sc1\n"
        "s_mov_b64 exec, -1\n"
        "s_sub_u32 s17, s30, 22\n"
        "s_lshl_b32 s16, s29, 1\n"
        "s_add_u32 s16, s16, 1\n"
        "s_lshl_b32 s64, s17, 7\n"
        "s_add_u32 s40, s10, s64\n"
        "s_addc_u32 s41, s11, 0\n"
        "s_and_b32 s41, s41, 0xffff\n"
        "s_sub_u32 s42, 819200, s64\n"
        "s_mov_b32 s43, 0x20000\n"
        "v_lshrrev_b32 v13, 3, v0\n"
        "v_and_b32 v14, 2, v13\n"
        "v_lshlrev_b32 v14, 1, v14\n"
        "v_and_b32 v12, 7, v0\n"
        "v_xor_b32_e64 v14, v14, v12\n"
        "v_lshlrev_b32 v14, 4, v14\n"
        "s_movk_i32 s3, 1280\n"
        "v_mad_u32_u24 v8, v13, s3, v14\n"
        "v_and_b32 v9, 3, v13\n"
        "v_mul_u32_u24 v9, 20, v9\n"
        "v_lshrrev_b32_e64 v10, 5, v0\n"
        "v_lshl_add_u32 v9, v10, 1, v9\n"
        "v_mad_u32_u24 v9, v9, s3, v14\n"
        "s_mul_i32 s12, s35, 10240\n"
        "s_add_u32 s3, s35, s17\n"
        "s_and_b32 s3, s3, 3\n"
        "s_xor_b32 s63, s29, 0\n"
        "s_and_b32 s63, s63, 1\n"
        "s_lshl2_add_u32 s3, s63, s3\n"
        "s_mul_i32 s65, s3, 102400\n"
        "s_add_u32 s3, s35, s17\n"
        "s_and_b32 s3, s3, 3\n"
        "s_xor_b32 s63, s29, 1\n"
        "s_and_b32 s63, s63, 1\n"
        "s_lshl2_add_u32 s3, s63, s3\n"
        "s_mul_i32 s66, s3, 102400\n"
        "v_lshrrev_b32 v252, 3, v0\n"
        "v_and_b32 v254, 7, v0\n"
        "s_lshl_b32 s13, s16, 2\n"
        "s_add_u32 s13, s13, s35\n"
        "s_mul_i32 s14, s13, 40960\n"
        "s_add_u32 s14, s14, 2097152\n"
        "s_xor_b32 s39, s29, 0\n"
        "s_and_b32 s39, s39, 1\n"
        "s_add_u32 s3, s17, 0\n"
        "s_and_b32 s3, s3, 3\n"
        "s_lshl2_add_u32 s3, s39, s3\n"
        "s_mul_i32 s3, s3, 5120\n"
        "s_add_u32 s52, s3, s14\n"
        "s_add_u32 s3, s17, 1\n"
        "s_and_b32 s3, s3, 3\n"
        "s_lshl2_add_u32 s3, s39, s3\n"
        "s_mul_i32 s3, s3, 5120\n"
        "s_add_u32 s53, s3, s14\n"
        "s_add_u32 s3, s17, 2\n"
        "s_and_b32 s3, s3, 3\n"
        "s_lshl2_add_u32 s3, s39, s3\n"
        "s_mul_i32 s3, s3, 5120\n"
        "s_add_u32 s54, s3, s14\n"
        "s_add_u32 s3, s17, 3\n"
        "s_and_b32 s3, s3, 3\n"
        "s_lshl2_add_u32 s3, s39, s3\n"
        "s_mul_i32 s3, s3, 5120\n"
        "s_add_u32 s55, s3, s14\n"
        "s_xor_b32 s39, s29, 1\n"
        "s_and_b32 s39, s39, 1\n"
        "s_add_u32 s3, s17, 4\n"
        "s_and_b32 s3, s3, 3\n"
        "s_lshl2_add_u32 s3, s39, s3\n"
        "s_mul_i32 s3, s3, 5120\n"
        "s_add_u32 s56, s3, s14\n"
        "s_add_u32 s3, s17, 5\n"
        "s_and_b32 s3, s3, 3\n"
        "s_lshl2_add_u32 s3, s39, s3\n"
        "s_mul_i32 s3, s3, 5120\n"
        "s_add_u32 s57, s3, s14\n"
        "s_add_u32 s3, s17, 6\n"
        "s_and_b32 s3, s3, 3\n"
        "s_lshl2_add_u32 s3, s39, s3\n"
        "s_mul_i32 s3, s3, 5120\n"
        "s_add_u32 s58, s3, s14\n"
        "s_add_u32 s3, s17, 7\n"
        "s_and_b32 s3, s3, 3\n"
        "s_lshl2_add_u32 s3, s39, s3\n"
        "s_mul_i32 s3, s3, 5120\n"
        "s_add_u32 s59, s3, s14\n"
        "s_mul_i32 s3, s13, 81920\n"
        "s_add_u32 s3, s64, s3\n"
        "s_add_u32 s44, s24, s3\n"
        "s_addc_u32 s45, s25, 0\n"
        "s_and_b32 s45, s45, 0xffff\n"
        "s_sub_u32 s46, 5242880, s3\n"
        "s_mov_b32 s47, 0x20000\n"
        "s_movk_i32 s3, 2560\n"
        "v_lshlrev_b32_e64 v254, 4, v254\n"
        "v_mad_u32_u24 v12, v252, s3, v254\n"
        "s_mul_i32 s3, s35, 4608\n"
        "s_add_u32 s3, s3, 51200\n"
        "v_mul_u32_u24 v252, 144, v252\n"
        "v_add3_u32 v11, v252, v254, s3\n"
        "v_and_b32_e64 v252, 31, v0\n"
        "v_mul_u32_u24 v252, 144, v252\n"
        "v_lshrrev_b32 v253, 5, v0\n"
        "v_lshlrev_b32 v254, 3, v253\n"
        "v_add3_u32 v10, v252, v254, s3\n"
        "v_bfe_u32 v252, v0, 2, 2\n"
        "v_lshl_add_u32 v253, v253, 2, v252\n"
        "v_and_b32 v252, 2, v252\n"
        "v_lshlrev_b32 v252, 5, v252\n"
        "v_lshl_add_u32 v252, v253, 7, v252\n"
        "v_bfe_u32 v253, v0, 4, 1\n"
        "v_lshl_add_u32 v252, v253, 5, v252\n"
        "v_and_b32_e64 v253, 3, v0\n"
        "v_lshl_add_u32 v252, v253, 3, v252\n"
        "v_xor_b32_e64 v253, 0, v252\n"
        "v_add_u32 v24, 81920, v253\n"
        "v_add_u32 v26, 10240, v253\n"
        "v_xor_b32_e64 v253, 64, v252\n"
        "v_add_u32 v25, 81920, v253\n"
        "v_add_u32 v27, 10240, v253\n"
        "v_add_u32 v13, 1024, v5\n"
        "s_lshl1_add_u32 s3, s13, s39\n"
        "s_lshl_b32 s3, s3, 9\n"
        "s_add_u32 s3, s3, 196608\n"
        "v_and_b32_e64 v252, 3, v0\n"
        "v_lshl_add_u32 v14, v252, 7, s3\n"
        "v_xor_b32 v21, 512, v14\n"
        "s_and_b32 s3, s29, 6\n"
        "s_add_u32 s3, s3, 0\n"
        "s_and_b32 s3, s3, 7\n"
        "s_lshl_b32 s3, s3, 12\n"
        "s_add_u32 s36, s8, s3\n"
        "s_and_b32 s3, s29, 6\n"
        "s_add_u32 s3, s3, 1\n"
        "s_and_b32 s3, s3, 7\n"
        "s_lshl_b32 s3, s3, 12\n"
        "s_add_u32 s37, s8, s3\n"
        "s_and_b32 s3, s29, 6\n"
        "s_add_u32 s3, s3, 2\n"
        "s_and_b32 s3, s3, 7\n"
        "s_lshl_b32 s3, s3, 12\n"
        "s_add_u32 s38, s8, s3\n"
        "s_and_b32 s3, s29, 6\n"
        "s_add_u32 s3, s3, 3\n"
        "s_and_b32 s3, s3, 7\n"
        "s_lshl_b32 s3, s3, 12\n"
        "s_add_u32 s39, s8, s3\n"
        "s_and_b32 s3, s29, 6\n"
        "s_add_u32 s3, s3, 4\n"
        "s_and_b32 s3, s3, 7\n"
        "s_lshl_b32 s3, s3, 12\n"
        "s_add_u32 s48, s8, s3\n"
        "s_and_b32 s3, s29, 6\n"
        "s_add_u32 s3, s3, 5\n"
        "s_and_b32 s3, s3, 7\n"
        "s_lshl_b32 s3, s3, 12\n"
        "s_add_u32 s49, s8, s3\n"
        "s_and_b32 s3, s29, 6\n"
        "s_add_u32 s3, s3, 6\n"
        "s_and_b32 s3, s3, 7\n"
        "s_lshl_b32 s3, s3, 12\n"
        "s_add_u32 s50, s8, s3\n"
        "s_and_b32 s3, s29, 6\n"
        "s_add_u32 s3, s3, 7\n"
        "s_and_b32 s3, s3, 7\n"
        "s_lshl_b32 s3, s3, 12\n"
        "s_add_u32 s51, s8, s3\n"
        "s_mov_b32 s15, 0x4000\n"
        "s_mov_b32 s20, 0x40000\n"
        "s_mov_b32 s21, 0x7f817f81\n"
        "s_getpc_b64 s[60:61]\n"
        ".Lz327:\n"
        "s_add_u32 s60, s60, .Lz328-.Lz327\n"
        "s_addc_u32 s61, s61, 0\n"
        "s_mov_b64 exec, 0\n"
        "s_cmp_eq_u32 s35, 0\n"
        "s_cbranch_scc1 .Lwr_0\n"
        "s_cmp_eq_u32 s35, 1\n"
        "s_cbranch_scc1 .Lwr_1\n"
        "s_cmp_eq_u32 s35, 2\n"
        "s_cbranch_scc1 .Lwr_2\n"
        "s_branch .Lwr_3\n"
        ".Lz328:\n"
        "s_mov_b64 exec, -1\n"
        "s_barrier\n"
        "s_waitcnt vmcnt(0)\n"
        "s_cmp_lt_u32 s35, 2\n"
        "s_cbranch_scc0 .Lk329\n"
        "s_mov_b32 s3, 0x40000\n"
        ".Lp333:\n"
        "buffer_load_dwordx4 v[120:123], v19, s[4:7], 0 offen sc1\n"
        "s_waitcnt vmcnt(0)\n"
        "v_cmp_ne_u64 vcc, v[120:121], v[6:7]\n"
        "s_nop 0\n"
        "s_cbranch_vccz .Ld334\n"
        "s_sleep 4\n"
        "s_sub_u32 s3, s3, 1\n"
        "s_cmp_eq_u32 s3, 0\n"
        "s_cbranch_scc0 .Lp333\n"
        ".Ld334:\n"
        ".Lt335:\n"
        "s_sleep 1\n"
        "s_memrealtime s[60:61]\n"
        "s_waitcnt lgkmcnt(0)\n"
        "v_sub_u32 v123, s60, v122\n"
        "v_cmp_gt_u32 vcc, 296, v123\n"
        "s_nop 0\n"
        "s_cbranch_vccnz .Lt335\n"
        ".Lr0:\n"
        ".Lr330:\n"
        ".Lwr_0:\n"
        "s_cbranch_execz .Lwr_4\n"
        "buffer_load_dwordx4 v[32:35], v5, s[4:7], s36 offen offset:0 sc1\n"
        "buffer_load_dwordx4 v[36:39], v5, s[4:7], s36 offen offset:2048 sc1\n"
        "buffer_load_dwordx4 v[40:43], v5, s[4:7], s37 offen offset:0 sc1\n"
        "buffer_load_dwordx4 v[44:47], v5, s[4:7], s37 offen offset:2048 sc1\n"
        "buffer_load_dwordx4 v[48:51], v5, s[4:7], s38 offen offset:0 sc1\n"
        "buffer_load_dwordx4 v[52:55], v5, s[4:7], s38 offen offset:2048 sc1\n"
        "buffer_load_dwordx4 v[56:59], v5, s[4:7], s39 offen offset:0 sc1\n"
        "buffer_load_dwordx4 v[60:63], v5, s[4:7], s39 offen offset:2048 sc1\n"
        "buffer_load_dwordx4 v[64:67], v5, s[4:7], s48 offen offset:0 sc1\n"
        "buffer_load_dwordx4 v[68:71], v5, s[4:7], s48 offen offset:2048 sc1\n"
        "buffer_load_dwordx4 v[72:75], v5, s[4:7], s49 offen offset:0 sc1\n"
        "buffer_load_dwordx4 v[76:79], v5, s[4:7], s49 offen offset:2048 sc1\n"
        "buffer_load_dwordx4 v[80:83], v5, s[4:7], s50 offen offset:0 sc1\n"
        "buffer_load_dwordx4 v[84:87], v5, s[4:7], s50 offen offset:2048 sc1\n"
        "buffer_load_dwordx4 v[88:91], v5, s[4:7], s51 offen offset:0 sc1\n"
        "buffer_load_dwordx4 v[92:95], v5, s[4:7], s51 offen offset:2048 sc1\n"
        ".Lwr_1:\n"
        "s_cbranch_execz .Lwr_5\n"
        "s_waitcnt vmcnt(14)\n"
        "v_cmp_u_f32 vcc, v32, v36\n"
        "s_nop 0\n"
        "s_cbranch_vccnz .Lv336\n"
        ".Lb337:\n"
        "v_mfma_f32_4x4x4_16b_bf16 v[96:99], v[22:23], v[32:33], 0\n"
        "v_mfma_f32_4x4x4_16b_bf16 v[100:103], v[22:23], v[34:35], 0\n"
        "v_mfma_f32_4x4x4_16b_bf16 v[104:107], v[22:23], v[36:37], 0\n"
        "v_mfma_f32_4x4x4_16b_bf16 v[108:111], v[22:23], v[38:39], 0\n"
        "s_waitcnt vmcnt(12)\n"
        "v_cmp_u_f32 vcc, v40, v44\n"
        "s_nop 0\n"
        "s_cbranch_vccnz .Lv338\n"
        ".Lb339:\n"
        "v_mfma_f32_4x4x4_16b_bf16 v[96:99], v[22:23], v[40:41], v[96:99]\n"
        "v_mfma_f32_4x4x4_16b_bf16 v[100:103], v[22:23], v[42:43], v[100:103]\n"
        "v_mfma_f32_4x4x4_16b_bf16 v[104:107], v[22:23], v[44:45], v[104:107]\n"
        "v_mfma_f32_4x4x4_16b_bf16 v[108:111], v[22:23], v[46:47], v[108:111]\n"
        "s_waitcnt vmcnt(10)\n"
        "v_cmp_u_f32 vcc, v48, v52\n"
        "s_nop 0\n"
        "s_cbranch_vccnz .Lv340\n"
        ".Lb341:\n"
        "v_mfma_f32_4x4x4_16b_bf16 v[96:99], v[22:23], v[48:49], v[96:99]\n"
        "v_mfma_f32_4x4x4_16b_bf16 v[100:103], v[22:23], v[50:51], v[100:103]\n"
        ".Lwr_2:\n"
        "s_cbranch_execz .Lwr_6\n"
        "v_mfma_f32_4x4x4_16b_bf16 v[104:107], v[22:23], v[52:53], v[104:107]\n"
        "v_mfma_f32_4x4x4_16b_bf16 v[108:111], v[22:23], v[54:55], v[108:111]\n"
        "s_waitcnt vmcnt(8)\n"
        "v_cmp_u_f32_e64 vcc, v56, v60\n"
        "s_nop 0\n"
        "s_cbranch_vccnz .Lv342\n"
        ".Lb343:\n"
        "v_mfma_f32_4x4x4_16b_bf16 v[96:99], v[22:23], v[56:57], v[96:99]\n"
        "v_mfma_f32_4x4x4_16b_bf16 v[100:103], v[22:23], v[58:59], v[100:103]\n"
        "v_mfma_f32_4x4x4_16b_bf16 v[104:107], v[22:23], v[60:61], v[104:107]\n"
        "v_mfma_f32_4x4x4_16b_bf16 v[108:111], v[22:23], v[62:63], v[108:111]\n"
        "s_waitcnt vmcnt(6)\n"
        "v_cmp_u_f32 vcc, v64, v68\n"
        "s_nop 0\n"
        "s_cbranch_vccnz .Lv344\n"
        ".Lb345:\n"
        "v_mfma_f32_4x4x4_16b_bf16 v[96:99], v[22:23], v[64:65], v[96:99]\n"
        "v_mfma_f32_4x4x4_16b_bf16 v[100:103], v[22:23], v[66:67], v[100:103]\n"
        "v_mfma_f32_4x4x4_16b_bf16 v[104:107], v[22:23], v[68:69], v[104:107]\n"
        "v_mfma_f32_4x4x4_16b_bf16 v[108:111], v[22:23], v[70:71], v[108:111]\n"
        "s_waitcnt vmcnt(4)\n"
        "v_cmp_u_f32 vcc, v72, v76\n"
        "s_nop 0\n"
        "s_cbranch_vccnz .Lv346\n"
        ".Lb347:\n"
        ".Lwr_3:\n"
        "s_cbranch_execz .Lwr_7\n"
        "v_mfma_f32_4x4x4_16b_bf16 v[96:99], v[22:23], v[72:73], v[96:99]\n"
        "v_mfma_f32_4x4x4_16b_bf16 v[100:103], v[22:23], v[74:75], v[100:103]\n"
        "v_mfma_f32_4x4x4_16b_bf16 v[104:107], v[22:23], v[76:77], v[104:107]\n"
        "v_mfma_f32_4x4x4_16b_bf16 v[108:111], v[22:23], v[78:79], v[108:111]\n"
        "s_waitcnt vmcnt(2)\n"
        "v_cmp_u_f32_e64 vcc, v80, v84\n"
        "s_nop 0\n"
        "s_cbranch_vccnz .Lv348\n"
        ".Lb349:\n"
        "v_mfma_f32_4x4x4_16b_bf16 v[96:99], v[22:23], v[80:81], v[96:99]\n"
        "v_mfma_f32_4x4x4_16b_bf16 v[100:103], v[22:23], v[82:83], v[100:103]\n"
        "v_mfma_f32_4x4x4_16b_bf16 v[104:107], v[22:23], v[84:85], v[104:107]\n"
        "v_mfma_f32_4x4x4_16b_bf16 v[108:111], v[22:23], v[86:87], v[108:111]\n"
        "s_waitcnt vmcnt(0)\n"
        "v_cmp_u_f32 vcc, v88, v92\n"
        "s_nop 0\n"
        "s_cbranch_vccnz .Lv350\n"
        ".Lb351:\n"
        "v_mfma_f32_4x4x4_16b_bf16 v[96:99], v[22:23], v[88:89], v[96:99]\n"
        "v_mfma_f32_4x4x4_16b_bf16 v[100:103], v[22:23], v[90:91], v[100:103]\n"
        "v_mfma_f32_4x4x4_16b_bf16 v[104:107], v[22:23], v[92:93], v[104:107]\n"
        "v_mfma_f32_4x4x4_16b_bf16 v[108:111], v[22:23], v[94:95], v[108:111]\n"
        ".Lwr_4:\n"
        "s_cbranch_execz .Lwr_8\n"
        "s_nop 3\n"
        "v_pk_add_f32 v[96:97], v[96:97], v[104:105]\n"
        "v_pk_add_f32 v[98:99], v[98:99], v[106:107]\n"
        "v_pk_add_f32 v[100:101], v[100:101], v[108:109]\n"
        "v_pk_add_f32 v[102:103], v[102:103], v[110:111]\n"
        "v_cmp_u_f32_e64 vcc, v96, v100\n"
        "s_nop 0\n"
        "s_cbranch_vccnz .Lc331\n"
        ".Lo332:\n"
        "v_cvt_pk_bf16_f32 v112, v96, v97\n"
        "v_cvt_pk_bf16_f32 v113, v98, v99\n"
        "v_cvt_pk_bf16_f32 v114, v100, v101\n"
        "v_cvt_pk_bf16_f32 v115, v102, v103\n"
        "buffer_store_dwordx4 v[112:115], v5, s[4:7], s9 offen sc1\n"
        ".Lr1:\n"
        ".Lk329:\n"
        "v_mov_b32_e64 v3, 1\n"
        "ds_write_b32 v2, v3 offset:32\n"
        ".Lw352:\n"
        "ds_read_b32 v4, v1 offset:32\n"
        "s_waitcnt lgkmcnt(0)\n"
        "v_cmp_gt_u32 vcc, 1, v4\n"
        "s_nop 0\n"
        "s_cbranch_vccz .Ld353\n"
        "s_sleep 4\n"
        "s_sub_i32 s68, s68, 1\n"
        "s_cmp_gt_i32 s68, 0\n"
        "s_cbranch_scc1 .Lw352\n"
        ".Ld353:\n"
        "s_add_u32 m0, s12, 81920\n"
        "s_add_u32 s63, s65, 0\n"
        "buffer_load_dwordx4 v8, s[40:43], s63 offen lds\n"
        "s_add_u32 m0, s12, 82944\n"
        "s_add_u32 s63, s65, 10240\n"
        "buffer_load_dwordx4 v8, s[40:43], s63 offen lds\n"
        "s_add_u32 m0, s12, 83968\n"
        "s_add_u32 s63, s65, 25600\n"
        "buffer_load_dwordx4 v8, s[40:43], s63 offen lds\n"
        "s_add_u32 m0, s12, 84992\n"
        "s_add_u32 s63, s65, 35840\n"
        "buffer_load_dwordx4 v8, s[40:43], s63 offen lds\n"
        "s_add_u32 m0, s12, 86016\n"
        "s_add_u32 s63, s65, 51200\n"
        "buffer_load_dwordx4 v8, s[40:43], s63 offen lds\n"
        "s_add_u32 m0, s12, 87040\n"
        "s_add_u32 s63, s65, 61440\n"
        "buffer_load_dwordx4 v8, s[40:43], s63 offen lds\n"
        "s_add_u32 m0, s12, 88064\n"
        "s_add_u32 s63, s65, 76800\n"
        "buffer_load_dwordx4 v8, s[40:43], s63 offen lds\n"
        "s_add_u32 m0, s12, 89088\n"
        "s_add_u32 s63, s65, 87040\n"
        "buffer_load_dwordx4 v8, s[40:43], s63 offen lds\n"
        "s_add_u32 m0, s12, 90112\n"
        "s_add_u32 s63, s65, 20480\n"
        "buffer_load_dwordx4 v9, s[40:43], s63 offen lds\n"
        "s_add_u32 m0, s12, 91136\n"
        "s_add_u32 s63, s65, 21760\n"
        "buffer_load_dwordx4 v9, s[40:43], s63 offen lds\n"
        "s_add_u32 m0, s12, 10240\n"
        "s_add_u32 s63, s66, 0\n"
        "buffer_load_dwordx4 v8, s[40:43], s63 offen lds\n"
        "s_add_u32 m0, s12, 11264\n"
        "s_add_u32 s63, s66, 10240\n"
        "buffer_load_dwordx4 v8, s[40:43], s63 offen lds\n"
        "s_add_u32 m0, s12, 12288\n"
        "s_add_u32 s63, s66, 25600\n"
        "buffer_load_dwordx4 v8, s[40:43], s63 offen lds\n"
        "s_add_u32 m0, s12, 13312\n"
        "s_add_u32 s63, s66, 35840\n"
        "buffer_load_dwordx4 v8, s[40:43], s63 offen lds\n"
        "s_add_u32 m0, s12, 14336\n"
        "s_add_u32 s63, s66, 51200\n"
        "buffer_load_dwordx4 v8, s[40:43], s63 offen lds\n"
        "s_add_u32 m0, s12, 15360\n"
        "s_add_u32 s63, s66, 61440\n"
        "buffer_load_dwordx4 v8, s[40:43], s63 offen lds\n"
        "s_add_u32 m0, s12, 16384\n"
        "s_add_u32 s63, s66, 76800\n"
        "buffer_load_dwordx4 v8, s[40:43], s63 offen lds\n"
        "s_add_u32 m0, s12, 17408\n"
        "s_add_u32 s63, s66, 87040\n"
        "buffer_load_dwordx4 v8, s[40:43], s63 offen lds\n"
        "s_add_u32 m0, s12, 18432\n"
        "s_add_u32 s63, s66, 20480\n"
        "buffer_load_dwordx4 v9, s[40:43], s63 offen lds\n"
        "s_add_u32 m0, s12, 19456\n"
        "s_add_u32 s63, s66, 21760\n"
        "buffer_load_dwordx4 v9, s[40:43], s63 offen lds\n"
        "s_waitcnt vmcnt(0)\n"
        "s_cmp_lt_u32 s35, 2\n"
        "s_cbranch_scc0 .Lk354\n"
        ".Lt355:\n"
        "s_sleep 1\n"
        "s_memrealtime s[60:61]\n"
        "s_waitcnt lgkmcnt(0)\n"
        "v_sub_u32 v123, s60, v122\n"
        "v_cmp_gt_u32 vcc, 486, v123\n"
        "s_nop 0\n"
        "s_cbranch_vccnz .Lt355\n"
        ".Lk354:\n"
        "v_mov_b32_e64 v3, 1\n"
        "ds_write_b32 v2, v3 offset:16\n"
        "s_waitcnt lgkmcnt(0)\n"
        "s_branch .La3\n"
        ".Lidle:\n"
        "s_cmp_eq_u32 s2, 223\n"
        "s_cbranch_scc0 .Lend\n"
        "s_cmp_eq_u32 s28, 0\n"
        "s_cbranch_scc0 .Lend\n"
        "v_and_b32 v0, 63, v0\n"
        "s_waitcnt lgkmcnt(0)\n"
        "s_mov_b32 s4, s26\n"
        "s_and_b32 s5, s27, 0xffff\n"
        "s_mov_b32 s6, 8388608\n"
        "s_mov_b32 s7, 0x20000\n"
        "v_mov_b32_e64 v24, 0\n"
        "buffer_load_dwordx2 v[6:7], v24, s[4:7], 0 offen sc1\n"
        "s_waitcnt vmcnt(0)\n"
        "v_add_co_u32 v6, vcc, 1, v6\n"
        "s_nop 1\n"
        "v_addc_co_u32 v7, vcc, 0, v7, vcc\n"
        "v_lshlrev_b32_e64 v1, 7, v0\n"
        "v_add_u32 v1, 4096, v1\n"
        "v_add_u32 v2, 8192, v1\n"
        "v_add_u32 v3, 16384, v1\n"
        "v_add_u32 v4, 24576, v1\n"
        "s_mov_b32 s22, 0xffff0000\n"
        "s_mov_b32 s23, 0\n"
        "v_cndmask_b32 v4, v4, v1, s[22:23]\n"
        "s_barrier\n"
        "s_barrier\n"
        "s_mov_b32 s3, 0x40000\n"
        ".Lp356:\n"
        "buffer_load_dwordx2 v[16:17], v1, s[4:7], 0 offen sc1\n"
        "s_waitcnt vmcnt(0)\n"
        "v_cmp_ne_u64_e64 vcc, v[16:17], v[6:7]\n"
        "s_nop 0\n"
        "s_cbranch_vccz .Ld357\n"
        "s_sleep 16\n"
        "s_sub_u32 s3, s3, 1\n"
        "s_cmp_eq_u32 s3, 0\n"
        "s_cbranch_scc0 .Lp356\n"
        ".Ld357:\n"
        "s_mov_b32 s3, 0x40000\n"
        ".Lp358:\n"
        "buffer_load_dwordx2 v[16:17], v2, s[4:7], 0 offen sc1\n"
        "s_waitcnt vmcnt(0)\n"
        "v_cmp_ne_u64 vcc, v[16:17], v[6:7]\n"
        "s_nop 0\n"
        "s_cbranch_vccz .Ld359\n"
        "s_sleep 16\n"
        "s_sub_u32 s3, s3, 1\n"
        "s_cmp_eq_u32 s3, 0\n"
        "s_cbranch_scc0 .Lp358\n"
        ".Ld359:\n"
        "s_mov_b32 s3, 0x40000\n"
        ".Lp360:\n"
        "buffer_load_dwordx2 v[16:17], v3, s[4:7], 0 offen sc1\n"
        "s_waitcnt vmcnt(0)\n"
        "v_cmp_ne_u64 vcc, v[16:17], v[6:7]\n"
        "s_nop 0\n"
        "s_cbranch_vccz .Ld361\n"
        "s_sleep 16\n"
        "s_sub_u32 s3, s3, 1\n"
        "s_cmp_eq_u32 s3, 0\n"
        "s_cbranch_scc0 .Lp360\n"
        ".Ld361:\n"
        "s_mov_b32 s3, 0x40000\n"
        ".Lp362:\n"
        "buffer_load_dwordx2 v[16:17], v4, s[4:7], 0 offen sc1\n"
        "s_waitcnt vmcnt(0)\n"
        "v_cmp_ne_u64 vcc, v[16:17], v[6:7]\n"
        "s_nop 0\n"
        "s_cbranch_vccz .Ld363\n"
        "s_sleep 16\n"
        "s_sub_u32 s3, s3, 1\n"
        "s_cmp_eq_u32 s3, 0\n"
        "s_cbranch_scc0 .Lp362\n"
        ".Ld363:\n"
        "s_mov_b64 exec, 1\n"
        "buffer_store_dwordx2 v[6:7], v24, s[4:7], 0 offen sc1\n"
        "s_endpgm\n"
        ".Lks254:\n"
        "s_nop 1\n"
        "s_nop 0\n"
        "v_max3_f32 v24, v102, v103, v104\n"
        "v_max3_f32 v24, v24, v105, v106\n"
        "v_max3_f32 v24, v24, v107, v108\n"
        "v_max3_f32 v24, v24, v109, v110\n"
        "v_max3_f32 v24, v24, v111, v112\n"
        "v_max3_f32 v24, v24, v113, v114\n"
        "v_max3_f32 v24, v24, v115, v116\n"
        "v_max3_f32 v24, v24, v117, v118\n"
        "v_max3_f32 v24, v24, v119, v120\n"
        "v_max_f32_e64 v24, v24, v121\n"
        "v_min3_f32 v25, v102, v103, v104\n"
        "v_min3_f32 v25, v25, v105, v106\n"
        "v_min3_f32 v25, v25, v107, v108\n"
        "v_min3_f32 v25, v25, v109, v110\n"
        "v_min3_f32 v25, v25, v111, v112\n"
        "v_min3_f32 v25, v25, v113, v114\n"
        "v_min3_f32 v25, v25, v115, v116\n"
        "v_min3_f32 v25, v25, v117, v118\n"
        "v_min3_f32 v25, v25, v119, v120\n"
        "v_min_f32 v25, v25, v121\n"
        "v_sub_f32 v162, v102, v24\n"
        "v_sub_f32 v142, v25, v102\n"
        "v_sub_f32 v163, v103, v24\n"
        "v_sub_f32 v143, v25, v103\n"
        "v_sub_f32 v164, v104, v24\n"
        "v_sub_f32 v144, v25, v104\n"
        "v_sub_f32 v165, v105, v24\n"
        "v_sub_f32 v145, v25, v105\n"
        "v_sub_f32 v166, v106, v24\n"
        "v_sub_f32 v146, v25, v106\n"
        "v_sub_f32 v167, v107, v24\n"
        "v_sub_f32 v147, v25, v107\n"
        "v_sub_f32 v168, v108, v24\n"
        "v_sub_f32 v148, v25, v108\n"
        "v_sub_f32 v169, v109, v24\n"
        "v_sub_f32 v149, v25, v109\n"
        "v_sub_f32 v170, v110, v24\n"
        "v_sub_f32 v150, v25, v110\n"
        "v_sub_f32 v171, v111, v24\n"
        "v_sub_f32 v151, v25, v111\n"
        "v_sub_f32 v172, v112, v24\n"
        "v_sub_f32 v152, v25, v112\n"
        "v_sub_f32 v173, v113, v24\n"
        "v_sub_f32 v153, v25, v113\n"
        "v_sub_f32 v174, v114, v24\n"
        "v_sub_f32 v154, v25, v114\n"
        "v_sub_f32 v175, v115, v24\n"
        "v_sub_f32 v155, v25, v115\n"
        "v_sub_f32 v176, v116, v24\n"
        "v_sub_f32 v156, v25, v116\n"
        "v_sub_f32 v177, v117, v24\n"
        "v_sub_f32 v157, v25, v117\n"
        "v_sub_f32 v178, v118, v24\n"
        "v_sub_f32 v158, v25, v118\n"
        "v_sub_f32 v179, v119, v24\n"
        "v_sub_f32 v159, v25, v119\n"
        "v_sub_f32 v180, v120, v24\n"
        "v_sub_f32 v160, v25, v120\n"
        "v_sub_f32 v181, v121, v24\n"
        "v_sub_f32 v161, v25, v121\n"
        "v_exp_f32 v162, v162\n"
        "v_exp_f32 v142, v142\n"
        "v_exp_f32 v163, v163\n"
        "v_exp_f32 v143, v143\n"
        "v_exp_f32 v164, v164\n"
        "v_exp_f32 v144, v144\n"
        "v_exp_f32 v165, v165\n"
        "v_exp_f32 v145, v145\n"
        "v_exp_f32 v166, v166\n"
        "v_exp_f32 v146, v146\n"
        "v_exp_f32 v167, v167\n"
        "v_exp_f32 v147, v147\n"
        "v_exp_f32 v168, v168\n"
        "v_exp_f32 v148, v148\n"
        "v_exp_f32 v169, v169\n"
        "v_exp_f32 v149, v149\n"
        "v_exp_f32 v170, v170\n"
        "v_exp_f32 v150, v150\n"
        "v_exp_f32 v171, v171\n"
        "v_exp_f32 v151, v151\n"
        "v_exp_f32 v172, v172\n"
        "v_exp_f32 v152, v152\n"
        "v_exp_f32 v173, v173\n"
        "v_exp_f32 v153, v153\n"
        "v_exp_f32 v174, v174\n"
        "v_exp_f32 v154, v154\n"
        "v_exp_f32 v175, v175\n"
        "v_exp_f32 v155, v155\n"
        "v_exp_f32 v176, v176\n"
        "v_exp_f32 v156, v156\n"
        "v_exp_f32 v177, v177\n"
        "v_exp_f32 v157, v157\n"
        "v_exp_f32 v178, v178\n"
        "v_exp_f32 v158, v158\n"
        "v_exp_f32 v179, v179\n"
        "v_exp_f32 v159, v159\n"
        "v_exp_f32 v180, v180\n"
        "v_exp_f32 v160, v160\n"
        "v_exp_f32 v181, v181\n"
        "v_exp_f32 v161, v161\n"
        "v_add_f32 v25, v142, v143\n"
        "v_add_f32 v24, v162, v163\n"
        "v_add_f32 v25, v25, v144\n"
        "v_add_f32 v24, v24, v164\n"
        "v_add_f32 v25, v25, v145\n"
        "v_add_f32 v24, v24, v165\n"
        "v_add_f32 v25, v25, v146\n"
        "v_add_f32 v24, v24, v166\n"
        "v_add_f32 v25, v25, v147\n"
        "v_add_f32 v24, v24, v167\n"
        "v_add_f32 v25, v25, v148\n"
        "v_add_f32 v24, v24, v168\n"
        "v_add_f32 v25, v25, v149\n"
        "v_add_f32 v24, v24, v169\n"
        "v_add_f32 v25, v25, v150\n"
        "v_add_f32 v24, v24, v170\n"
        "v_add_f32 v25, v25, v151\n"
        "v_add_f32 v24, v24, v171\n"
        "v_add_f32 v25, v25, v152\n"
        "v_add_f32 v24, v24, v172\n"
        "v_add_f32 v25, v25, v153\n"
        "v_add_f32 v24, v24, v173\n"
        "v_add_f32 v25, v25, v154\n"
        "v_add_f32 v24, v24, v174\n"
        "v_add_f32 v25, v25, v155\n"
        "v_add_f32 v24, v24, v175\n"
        "v_add_f32 v25, v25, v156\n"
        "v_add_f32 v24, v24, v176\n"
        "v_add_f32 v25, v25, v157\n"
        "v_add_f32 v24, v24, v177\n"
        "v_add_f32 v25, v25, v158\n"
        "v_add_f32 v24, v24, v178\n"
        "v_add_f32 v25, v25, v159\n"
        "v_add_f32 v24, v24, v179\n"
        "v_add_f32 v25, v25, v160\n"
        "v_add_f32 v24, v24, v180\n"
        "v_add_f32 v25, v25, v161\n"
        "v_add_f32_e64 v24, v24, v181\n"
        "s_branch .Lkj255\n"
        ".Lks256:\n"
        "s_nop 1\n"
        "v_max3_f32 v24, v122, v123, v124\n"
        "v_max3_f32 v24, v24, v125, v126\n"
        "v_max3_f32 v24, v24, v127, v128\n"
        "v_max3_f32 v24, v24, v129, v130\n"
        "v_max3_f32 v24, v24, v131, v132\n"
        "v_max3_f32 v24, v24, v133, v134\n"
        "v_max3_f32 v24, v24, v135, v136\n"
        "v_max3_f32 v24, v24, v137, v138\n"
        "v_max3_f32 v24, v24, v139, v140\n"
        "v_max_f32_e64 v24, v24, v141\n"
        "v_min3_f32 v25, v122, v123, v124\n"
        "v_min3_f32 v25, v25, v125, v126\n"
        "v_min3_f32 v25, v25, v127, v128\n"
        "v_min3_f32 v25, v25, v129, v130\n"
        "v_min3_f32 v25, v25, v131, v132\n"
        "v_min3_f32 v25, v25, v133, v134\n"
        "v_min3_f32 v25, v25, v135, v136\n"
        "v_min3_f32 v25, v25, v137, v138\n"
        "v_min3_f32 v25, v25, v139, v140\n"
        "v_min_f32 v25, v25, v141\n"
        "v_sub_f32 v162, v122, v24\n"
        "v_sub_f32 v142, v25, v122\n"
        "v_sub_f32 v163, v123, v24\n"
        "v_sub_f32 v143, v25, v123\n"
        "v_sub_f32 v164, v124, v24\n"
        "v_sub_f32 v144, v25, v124\n"
        "v_sub_f32 v165, v125, v24\n"
        "v_sub_f32 v145, v25, v125\n"
        "v_sub_f32 v166, v126, v24\n"
        "v_sub_f32 v146, v25, v126\n"
        "v_sub_f32 v167, v127, v24\n"
        "v_sub_f32 v147, v25, v127\n"
        "v_sub_f32 v168, v128, v24\n"
        "v_sub_f32 v148, v25, v128\n"
        "v_sub_f32 v169, v129, v24\n"
        "v_sub_f32 v149, v25, v129\n"
        "v_sub_f32 v170, v130, v24\n"
        "v_sub_f32 v150, v25, v130\n"
        "v_sub_f32 v171, v131, v24\n"
        "v_sub_f32 v151, v25, v131\n"
        "v_sub_f32 v172, v132, v24\n"
        "v_sub_f32 v152, v25, v132\n"
        "v_sub_f32 v173, v133, v24\n"
        "v_sub_f32 v153, v25, v133\n"
        "v_sub_f32 v174, v134, v24\n"
        "v_sub_f32 v154, v25, v134\n"
        "v_sub_f32 v175, v135, v24\n"
        "v_sub_f32 v155, v25, v135\n"
        "v_sub_f32 v176, v136, v24\n"
        "v_sub_f32 v156, v25, v136\n"
        "v_sub_f32 v177, v137, v24\n"
        "v_sub_f32 v157, v25, v137\n"
        "v_sub_f32 v178, v138, v24\n"
        "v_sub_f32 v158, v25, v138\n"
        "v_sub_f32 v179, v139, v24\n"
        "v_sub_f32 v159, v25, v139\n"
        "v_sub_f32 v180, v140, v24\n"
        "v_sub_f32 v160, v25, v140\n"
        "v_sub_f32 v181, v141, v24\n"
        "v_sub_f32 v161, v25, v141\n"
        "v_exp_f32 v162, v162\n"
        "v_exp_f32 v142, v142\n"
        "v_exp_f32 v163, v163\n"
        "v_exp_f32 v143, v143\n"
        "v_exp_f32 v164, v164\n"
        "v_exp_f32 v144, v144\n"
        "v_exp_f32 v165, v165\n"
        "v_exp_f32 v145, v145\n"
        "v_exp_f32 v166, v166\n"
        "v_exp_f32 v146, v146\n"
        "v_exp_f32 v167, v167\n"
        "v_exp_f32 v147, v147\n"
        "v_exp_f32 v168, v168\n"
        "v_exp_f32 v148, v148\n"
        "v_exp_f32 v169, v169\n"
        "v_exp_f32 v149, v149\n"
        "v_exp_f32 v170, v170\n"
        "v_exp_f32 v150, v150\n"
        "v_exp_f32 v171, v171\n"
        "v_exp_f32 v151, v151\n"
        "v_exp_f32 v172, v172\n"
        "v_exp_f32 v152, v152\n"
        "v_exp_f32 v173, v173\n"
        "v_exp_f32 v153, v153\n"
        "v_exp_f32 v174, v174\n"
        "v_exp_f32 v154, v154\n"
        "v_exp_f32 v175, v175\n"
        "v_exp_f32 v155, v155\n"
        "v_exp_f32 v176, v176\n"
        "v_exp_f32 v156, v156\n"
        "v_exp_f32 v177, v177\n"
        "v_exp_f32 v157, v157\n"
        "v_exp_f32 v178, v178\n"
        "v_exp_f32 v158, v158\n"
        "v_exp_f32 v179, v179\n"
        "v_exp_f32 v159, v159\n"
        "v_exp_f32 v180, v180\n"
        "v_exp_f32 v160, v160\n"
        "v_exp_f32 v181, v181\n"
        "v_exp_f32 v161, v161\n"
        "v_add_f32 v25, v142, v143\n"
        "v_add_f32 v24, v162, v163\n"
        "v_add_f32 v25, v25, v144\n"
        "v_add_f32 v24, v24, v164\n"
        "v_add_f32 v25, v25, v145\n"
        "v_add_f32 v24, v24, v165\n"
        "v_add_f32 v25, v25, v146\n"
        "v_add_f32 v24, v24, v166\n"
        "v_add_f32 v25, v25, v147\n"
        "v_add_f32 v24, v24, v167\n"
        "v_add_f32 v25, v25, v148\n"
        "v_add_f32 v24, v24, v168\n"
        "v_add_f32 v25, v25, v149\n"
        "v_add_f32 v24, v24, v169\n"
        "v_add_f32 v25, v25, v150\n"
        "v_add_f32 v24, v24, v170\n"
        "v_add_f32 v25, v25, v151\n"
        "v_add_f32 v24, v24, v171\n"
        "v_add_f32 v25, v25, v152\n"
        "v_add_f32 v24, v24, v172\n"
        "v_add_f32 v25, v25, v153\n"
        "v_add_f32 v24, v24, v173\n"
        "v_add_f32 v25, v25, v154\n"
        "v_add_f32 v24, v24, v174\n"
        "v_add_f32 v25, v25, v155\n"
        "v_add_f32 v24, v24, v175\n"
        "v_add_f32 v25, v25, v156\n"
        "v_add_f32 v24, v24, v176\n"
        "v_add_f32 v25, v25, v157\n"
        "v_add_f32 v24, v24, v177\n"
        "v_add_f32 v25, v25, v158\n"
        "v_add_f32 v24, v24, v178\n"
        "v_add_f32 v25, v25, v159\n"
        "v_add_f32 v24, v24, v179\n"
        "v_add_f32 v25, v25, v160\n"
        "v_add_f32 v24, v24, v180\n"
        "v_add_f32 v25, v25, v161\n"
        "v_add_f32 v24, v24, v181\n"
        "s_branch .Lkj257\n"
        ".Ll261:\n"
        "s_mov_b32 s3, 0x40000\n"
        ".Lp263:\n"
        "buffer_load_dwordx4 v[176:179], v20, s[4:7], 0 offen sc1\n"
        "s_waitcnt vmcnt(0)\n"
        "v_cmp_ne_u64 vcc, v[176:177], v[6:7]\n"
        "s_nop 0\n"
        "s_cbranch_vccz .Ld264\n"
        "s_sleep 2\n"
        "s_sub_u32 s3, s3, 1\n"
        "s_cmp_eq_u32 s3, 0\n"
        "s_cbranch_scc0 .Lp263\n"
        ".Ld264:\n"
        "s_branch .Lj262\n"
        ".Lv277:\n"
        "v_cmp_eq_u32 vcc, s21, v32\n"
        "v_cmp_eq_u32 s[22:23], s21, v36\n"
        "s_nop 0\n"
        "s_or_b64 vcc, vcc, s[22:23]\n"
        "s_cbranch_vccz .Lb278\n"
        "s_sub_i32 s20, s20, 1\n"
        "s_cmp_lt_i32 s20, 0\n"
        "s_cbranch_scc1 .Lb278\n"
        "s_sleep 2\n"
        "buffer_load_dwordx4 v[32:35], v5, s[4:7], s52 offen offset:0 sc1\n"
        "buffer_load_dwordx4 v[36:39], v13, s[4:7], s52 offen offset:0 sc1\n"
        "s_waitcnt vmcnt(0)\n"
        "s_branch .Lv277\n"
        ".Lv279:\n"
        "v_cmp_eq_u32 vcc, s21, v52\n"
        "v_cmp_eq_u32 s[22:23], s21, v56\n"
        "s_nop 0\n"
        "s_or_b64 vcc, vcc, s[22:23]\n"
        "s_cbranch_vccz .Lb280\n"
        "s_sub_i32 s20, s20, 1\n"
        "s_cmp_lt_i32 s20, 0\n"
        "s_cbranch_scc1 .Lb280\n"
        "s_sleep 2\n"
        "buffer_load_dwordx4 v[52:55], v5, s[4:7], s53 offen offset:0 sc1\n"
        "buffer_load_dwordx4 v[56:59], v13, s[4:7], s53 offen offset:0 sc1\n"
        "s_waitcnt vmcnt(0)\n"
        "s_branch .Lv279\n"
        ".Lv281:\n"
        "v_cmp_eq_u32 vcc, s21, v72\n"
        "v_cmp_eq_u32 s[22:23], s21, v76\n"
        "s_nop 0\n"
        "s_or_b64 vcc, vcc, s[22:23]\n"
        "s_cbranch_vccz .Lb282\n"
        "s_sub_i32 s20, s20, 1\n"
        "s_cmp_lt_i32 s20, 0\n"
        "s_cbranch_scc1 .Lb282\n"
        "s_sleep 2\n"
        "buffer_load_dwordx4 v[72:75], v5, s[4:7], s54 offen offset:0 sc1\n"
        "buffer_load_dwordx4 v[76:79], v13, s[4:7], s54 offen offset:0 sc1\n"
        "s_waitcnt vmcnt(0)\n"
        "s_branch .Lv281\n"
        ".Lv283:\n"
        "v_cmp_eq_u32 vcc, s21, v92\n"
        "v_cmp_eq_u32 s[22:23], s21, v96\n"
        "s_nop 0\n"
        "s_or_b64 vcc, vcc, s[22:23]\n"
        "s_cbranch_vccz .Lb284\n"
        "s_sub_i32 s20, s20, 1\n"
        "s_cmp_lt_i32 s20, 0\n"
        "s_cbranch_scc1 .Lb284\n"
        "s_sleep 2\n"
        "buffer_load_dwordx4 v[92:95], v5, s[4:7], s55 offen offset:0 sc1\n"
        "buffer_load_dwordx4 v[96:99], v13, s[4:7], s55 offen offset:0 sc1\n"
        "s_waitcnt vmcnt(0)\n"
        "s_branch .Lv283\n"
        ".Lv285:\n"
        "v_cmp_eq_u32 vcc, s21, v40\n"
        "v_cmp_eq_u32 s[22:23], s21, v44\n"
        "s_nop 0\n"
        "s_or_b64 vcc, vcc, s[22:23]\n"
        "s_cbranch_vccz .Lb286\n"
        "s_sub_i32 s20, s20, 1\n"
        "s_cmp_lt_i32 s20, 0\n"
        "s_cbranch_scc1 .Lb286\n"
        "s_sleep 2\n"
        "buffer_load_dwordx4 v[40:43], v13, s[4:7], s52 offen offset:1024 sc1\n"
        "buffer_load_dwordx4 v[44:47], v13, s[4:7], s52 offen offset:2048 sc1\n"
        "s_waitcnt vmcnt(0)\n"
        "s_branch .Lv285\n"
        ".Lv287:\n"
        "v_cmp_eq_u32 vcc, s21, v60\n"
        "v_cmp_eq_u32 s[22:23], s21, v64\n"
        "s_nop 0\n"
        "s_or_b64 vcc, vcc, s[22:23]\n"
        "s_cbranch_vccz .Lb288\n"
        "s_sub_i32 s20, s20, 1\n"
        "s_cmp_lt_i32 s20, 0\n"
        "s_cbranch_scc1 .Lb288\n"
        "s_sleep 2\n"
        "buffer_load_dwordx4 v[60:63], v13, s[4:7], s53 offen offset:1024 sc1\n"
        "buffer_load_dwordx4 v[64:67], v13, s[4:7], s53 offen offset:2048 sc1\n"
        "s_waitcnt vmcnt(0)\n"
        "s_branch .Lv287\n"
        ".Lv289:\n"
        "v_cmp_eq_u32 vcc, s21, v80\n"
        "v_cmp_eq_u32 s[22:23], s21, v84\n"
        "s_nop 0\n"
        "s_or_b64 vcc, vcc, s[22:23]\n"
        "s_cbranch_vccz .Lb290\n"
        "s_sub_i32 s20, s20, 1\n"
        "s_cmp_lt_i32 s20, 0\n"
        "s_cbranch_scc1 .Lb290\n"
        "s_sleep 2\n"
        "buffer_load_dwordx4 v[80:83], v13, s[4:7], s54 offen offset:1024 sc1\n"
        "buffer_load_dwordx4 v[84:87], v13, s[4:7], s54 offen offset:2048 sc1\n"
        "s_waitcnt vmcnt(0)\n"
        "s_branch .Lv289\n"
        ".Lq291:\n"
        "s_sub_i32 s20, s20, 1\n"
        "s_cmp_lt_i32 s20, 0\n"
        "s_cbranch_scc1 .Lb292\n"
        "s_sleep 2\n"
        "buffer_load_dwordx2 v[28:29], v14, s[4:7], 0 offen sc1\n"
        "s_waitcnt vmcnt(0)\n"
        "v_cmp_ne_u64 vcc, v[28:29], v[6:7]\n"
        "s_nop 0\n"
        "s_cbranch_vccnz .Lq291\n"
        "s_branch .Lb292\n"
        ".Lv293:\n"
        "v_cmp_eq_u32_e64 vcc, s21, v100\n"
        "v_cmp_eq_u32 s[22:23], s21, v104\n"
        "s_nop 0\n"
        "s_or_b64 vcc, vcc, s[22:23]\n"
        "s_cbranch_vccz .Lb294\n"
        "s_sub_i32 s20, s20, 1\n"
        "s_cmp_lt_i32 s20, 0\n"
        "s_cbranch_scc1 .Lb294\n"
        "s_sleep 2\n"
        "buffer_load_dwordx4 v[100:103], v13, s[4:7], s55 offen offset:1024 sc1\n"
        "buffer_load_dwordx4 v[104:107], v13, s[4:7], s55 offen offset:2048 sc1\n"
        "s_waitcnt vmcnt(0)\n"
        "s_branch .Lv293\n"
        ".Lv295:\n"
        "v_cmp_eq_u32 vcc, s21, v48\n"
        "v_cmp_eq_u32 s[22:23], s21, v68\n"
        "s_nop 0\n"
        "s_or_b64 vcc, vcc, s[22:23]\n"
        "s_cbranch_vccz .Lb296\n"
        "s_sub_i32 s20, s20, 1\n"
        "s_cmp_lt_i32 s20, 0\n"
        "s_cbranch_scc1 .Lb296\n"
        "s_sleep 2\n"
        "buffer_load_dwordx4 v[48:51], v13, s[4:7], s52 offen offset:3072 sc1\n"
        "buffer_load_dwordx4 v[68:71], v13, s[4:7], s53 offen offset:3072 sc1\n"
        "s_waitcnt vmcnt(0)\n"
        "s_branch .Lv295\n"
        ".Lv297:\n"
        "v_cmp_eq_u32 vcc, s21, v88\n"
        "v_cmp_eq_u32 s[22:23], s21, v108\n"
        "s_nop 0\n"
        "s_or_b64 vcc, vcc, s[22:23]\n"
        "s_cbranch_vccz .Lb298\n"
        "s_sub_i32 s20, s20, 1\n"
        "s_cmp_lt_i32 s20, 0\n"
        "s_cbranch_scc1 .Lb298\n"
        "s_sleep 2\n"
        "buffer_load_dwordx4 v[88:91], v13, s[4:7], s54 offen offset:3072 sc1\n"
        "buffer_load_dwordx4 v[108:111], v13, s[4:7], s55 offen offset:3072 sc1\n"
        "s_waitcnt vmcnt(0)\n"
        "s_branch .Lv297\n"
        ".Lv299:\n"
        "v_cmp_eq_u32 vcc, s21, v112\n"
        "v_cmp_eq_u32 s[22:23], s21, v116\n"
        "s_nop 0\n"
        "s_or_b64 vcc, vcc, s[22:23]\n"
        "s_cbranch_vccz .Lb300\n"
        "s_sub_i32 s20, s20, 1\n"
        "s_cmp_lt_i32 s20, 0\n"
        "s_cbranch_scc1 .Lb300\n"
        "s_sleep 2\n"
        "buffer_load_dwordx4 v[112:115], v5, s[4:7], s56 offen offset:0 sc1\n"
        "buffer_load_dwordx4 v[116:119], v13, s[4:7], s56 offen offset:0 sc1\n"
        "s_waitcnt vmcnt(0)\n"
        "s_branch .Lv299\n"
        ".Lv301:\n"
        "v_cmp_eq_u32 vcc, s21, v132\n"
        "v_cmp_eq_u32 s[22:23], s21, v136\n"
        "s_nop 0\n"
        "s_or_b64 vcc, vcc, s[22:23]\n"
        "s_cbranch_vccz .Lb302\n"
        "s_sub_i32 s20, s20, 1\n"
        "s_cmp_lt_i32 s20, 0\n"
        "s_cbranch_scc1 .Lb302\n"
        "s_sleep 2\n"
        "buffer_load_dwordx4 v[132:135], v5, s[4:7], s57 offen offset:0 sc1\n"
        "buffer_load_dwordx4 v[136:139], v13, s[4:7], s57 offen offset:0 sc1\n"
        "s_waitcnt vmcnt(0)\n"
        "s_branch .Lv301\n"
        ".Lv303:\n"
        "v_cmp_eq_u32 vcc, s21, v152\n"
        "v_cmp_eq_u32 s[22:23], s21, v156\n"
        "s_nop 0\n"
        "s_or_b64 vcc, vcc, s[22:23]\n"
        "s_cbranch_vccz .Lb304\n"
        "s_sub_i32 s20, s20, 1\n"
        "s_cmp_lt_i32 s20, 0\n"
        "s_cbranch_scc1 .Lb304\n"
        "s_sleep 2\n"
        "buffer_load_dwordx4 v[152:155], v5, s[4:7], s58 offen offset:0 sc1\n"
        "buffer_load_dwordx4 v[156:159], v13, s[4:7], s58 offen offset:0 sc1\n"
        "s_waitcnt vmcnt(0)\n"
        "s_branch .Lv303\n"
        ".Lv305:\n"
        "v_cmp_eq_u32 vcc, s21, v172\n"
        "v_cmp_eq_u32 s[22:23], s21, v176\n"
        "s_nop 0\n"
        "s_or_b64 vcc, vcc, s[22:23]\n"
        "s_cbranch_vccz .Lb306\n"
        "s_sub_i32 s20, s20, 1\n"
        "s_cmp_lt_i32 s20, 0\n"
        "s_cbranch_scc1 .Lb306\n"
        "s_sleep 2\n"
        "buffer_load_dwordx4 v[172:175], v5, s[4:7], s59 offen offset:0 sc1\n"
        "buffer_load_dwordx4 v[176:179], v13, s[4:7], s59 offen offset:0 sc1\n"
        "s_waitcnt vmcnt(0)\n"
        "s_branch .Lv305\n"
        ".Lv307:\n"
        "v_cmp_eq_u32 vcc, s21, v120\n"
        "v_cmp_eq_u32 s[22:23], s21, v124\n"
        "s_nop 0\n"
        "s_or_b64 vcc, vcc, s[22:23]\n"
        "s_cbranch_vccz .Lb308\n"
        "s_sub_i32 s20, s20, 1\n"
        "s_cmp_lt_i32 s20, 0\n"
        "s_cbranch_scc1 .Lb308\n"
        "s_sleep 2\n"
        "buffer_load_dwordx4 v[120:123], v13, s[4:7], s56 offen offset:1024 sc1\n"
        "buffer_load_dwordx4 v[124:127], v13, s[4:7], s56 offen offset:2048 sc1\n"
        "s_waitcnt vmcnt(0)\n"
        "s_branch .Lv307\n"
        ".Lv309:\n"
        "v_cmp_eq_u32 vcc, s21, v140\n"
        "v_cmp_eq_u32 s[22:23], s21, v144\n"
        "s_nop 0\n"
        "s_or_b64 vcc, vcc, s[22:23]\n"
        "s_cbranch_vccz .Lb310\n"
        "s_sub_i32 s20, s20, 1\n"
        "s_cmp_lt_i32 s20, 0\n"
        "s_cbranch_scc1 .Lb310\n"
        "s_sleep 2\n"
        "buffer_load_dwordx4 v[140:143], v13, s[4:7], s57 offen offset:1024 sc1\n"
        "buffer_load_dwordx4 v[144:147], v13, s[4:7], s57 offen offset:2048 sc1\n"
        "s_waitcnt vmcnt(0)\n"
        "s_branch .Lv309\n"
        ".Lv311:\n"
        "v_cmp_eq_u32 vcc, s21, v160\n"
        "v_cmp_eq_u32 s[22:23], s21, v164\n"
        "s_nop 0\n"
        "s_or_b64 vcc, vcc, s[22:23]\n"
        "s_cbranch_vccz .Lb312\n"
        "s_sub_i32 s20, s20, 1\n"
        "s_cmp_lt_i32 s20, 0\n"
        "s_cbranch_scc1 .Lb312\n"
        "s_sleep 2\n"
        "buffer_load_dwordx4 v[160:163], v13, s[4:7], s58 offen offset:1024 sc1\n"
        "buffer_load_dwordx4 v[164:167], v13, s[4:7], s58 offen offset:2048 sc1\n"
        "s_waitcnt vmcnt(0)\n"
        "s_branch .Lv311\n"
        ".Lv313:\n"
        "v_cmp_eq_u32 vcc, s21, v180\n"
        "v_cmp_eq_u32 s[22:23], s21, v184\n"
        "s_nop 0\n"
        "s_or_b64 vcc, vcc, s[22:23]\n"
        "s_cbranch_vccz .Lb314\n"
        "s_sub_i32 s20, s20, 1\n"
        "s_cmp_lt_i32 s20, 0\n"
        "s_cbranch_scc1 .Lb314\n"
        "s_sleep 2\n"
        "buffer_load_dwordx4 v[180:183], v13, s[4:7], s59 offen offset:1024 sc1\n"
        "buffer_load_dwordx4 v[184:187], v13, s[4:7], s59 offen offset:2048 sc1\n"
        "s_waitcnt vmcnt(0)\n"
        "s_branch .Lv313\n"
        ".Lv315:\n"
        "v_cmp_eq_u32 vcc, s21, v128\n"
        "v_cmp_eq_u32 s[22:23], s21, v148\n"
        "s_nop 0\n"
        "s_or_b64 vcc, vcc, s[22:23]\n"
        "s_cbranch_vccz .Lb316\n"
        "s_sub_i32 s20, s20, 1\n"
        "s_cmp_lt_i32 s20, 0\n"
        "s_cbranch_scc1 .Lb316\n"
        "s_sleep 2\n"
        "buffer_load_dwordx4 v[128:131], v13, s[4:7], s56 offen offset:3072 sc1\n"
        "buffer_load_dwordx4 v[148:151], v13, s[4:7], s57 offen offset:3072 sc1\n"
        "s_waitcnt vmcnt(0)\n"
        "s_branch .Lv315\n"
        ".Lv317:\n"
        "v_cmp_eq_u32 vcc, s21, v168\n"
        "v_cmp_eq_u32 s[22:23], s21, v188\n"
        "s_nop 0\n"
        "s_or_b64 vcc, vcc, s[22:23]\n"
        "s_cbranch_vccz .Lb318\n"
        "s_sub_i32 s20, s20, 1\n"
        "s_cmp_lt_i32 s20, 0\n"
        "s_cbranch_scc1 .Lb318\n"
        "s_sleep 2\n"
        "buffer_load_dwordx4 v[168:171], v13, s[4:7], s58 offen offset:3072 sc1\n"
        "buffer_load_dwordx4 v[188:191], v13, s[4:7], s59 offen offset:3072 sc1\n"
        "s_waitcnt vmcnt(0)\n"
        "s_branch .Lv317\n"
        ".Lc319:\n"
        "s_sub_u32 s15, s15, 1\n"
        "s_cmp_gt_u32 s15, 0x3ffd\n"
        "s_cbranch_scc1 .Lr272\n"
        "s_mov_b32 s3, 0x7f817f81\n"
        "s_mov_b64 s[22:23], 0\n"
        "v_cmp_eq_u32 vcc, s3, v32\n"
        "s_nop 0\n"
        "s_or_b64 s[22:23], s[22:23], vcc\n"
        "v_cmp_eq_u32 vcc, s3, v33\n"
        "s_nop 0\n"
        "s_or_b64 s[22:23], s[22:23], vcc\n"
        "v_cmp_eq_u32 vcc, s3, v34\n"
        "s_nop 0\n"
        "s_or_b64 s[22:23], s[22:23], vcc\n"
        "v_cmp_eq_u32 vcc, s3, v35\n"
        "s_nop 0\n"
        "s_or_b64 s[22:23], s[22:23], vcc\n"
        "v_cmp_eq_u32 vcc, s3, v36\n"
        "s_nop 0\n"
        "s_or_b64 s[22:23], s[22:23], vcc\n"
        "v_cmp_eq_u32 vcc, s3, v37\n"
        "s_nop 0\n"
        "s_or_b64 s[22:23], s[22:23], vcc\n"
        "v_cmp_eq_u32 vcc, s3, v38\n"
        "s_nop 0\n"
        "s_or_b64 s[22:23], s[22:23], vcc\n"
        "v_cmp_eq_u32 vcc, s3, v39\n"
        "s_nop 0\n"
        "s_or_b64 s[22:23], s[22:23], vcc\n"
        "v_cmp_eq_u32 vcc, s3, v40\n"
        "s_nop 0\n"
        "s_or_b64 s[22:23], s[22:23], vcc\n"
        "v_cmp_eq_u32 vcc, s3, v41\n"
        "s_nop 0\n"
        "s_or_b64 s[22:23], s[22:23], vcc\n"
        "v_cmp_eq_u32 vcc, s3, v42\n"
        "s_nop 0\n"
        "s_or_b64 s[22:23], s[22:23], vcc\n"
        "v_cmp_eq_u32 vcc, s3, v43\n"
        "s_nop 0\n"
        "s_or_b64 s[22:23], s[22:23], vcc\n"
        "v_cmp_eq_u32 vcc, s3, v44\n"
        "s_nop 0\n"
        "s_or_b64 s[22:23], s[22:23], vcc\n"
        "v_cmp_eq_u32 vcc, s3, v45\n"
        "s_nop 0\n"
        "s_or_b64 s[22:23], s[22:23], vcc\n"
        "v_cmp_eq_u32 vcc, s3, v46\n"
        "s_nop 0\n"
        "s_or_b64 s[22:23], s[22:23], vcc\n"
        "v_cmp_eq_u32 vcc, s3, v47\n"
        "s_nop 0\n"
        "s_or_b64 s[22:23], s[22:23], vcc\n"
        "v_cmp_eq_u32 vcc, s3, v48\n"
        "s_nop 0\n"
        "s_or_b64 s[22:23], s[22:23], vcc\n"
        "v_cmp_eq_u32 vcc, s3, v49\n"
        "s_nop 0\n"
        "s_or_b64 s[22:23], s[22:23], vcc\n"
        "v_cmp_eq_u32 vcc, s3, v50\n"
        "s_nop 0\n"
        "s_or_b64 s[22:23], s[22:23], vcc\n"
        "v_cmp_eq_u32 vcc, s3, v51\n"
        "s_nop 0\n"
        "s_or_b64 s[22:23], s[22:23], vcc\n"
        "v_cmp_eq_u32 vcc, s3, v52\n"
        "s_nop 0\n"
        "s_or_b64 s[22:23], s[22:23], vcc\n"
        "v_cmp_eq_u32 vcc, s3, v53\n"
        "s_nop 0\n"
        "s_or_b64 s[22:23], s[22:23], vcc\n"
        "v_cmp_eq_u32 vcc, s3, v54\n"
        "s_nop 0\n"
        "s_or_b64 s[22:23], s[22:23], vcc\n"
        "v_cmp_eq_u32 vcc, s3, v55\n"
        "s_nop 0\n"
        "s_or_b64 s[22:23], s[22:23], vcc\n"
        "v_cmp_eq_u32 vcc, s3, v56\n"
        "s_nop 0\n"
        "s_or_b64 s[22:23], s[22:23], vcc\n"
        "v_cmp_eq_u32 vcc, s3, v57\n"
        "s_nop 0\n"
        "s_or_b64 s[22:23], s[22:23], vcc\n"
        "v_cmp_eq_u32 vcc, s3, v58\n"
        "s_nop 0\n"
        "s_or_b64 s[22:23], s[22:23], vcc\n"
        "v_cmp_eq_u32 vcc, s3, v59\n"
        "s_nop 0\n"
        "s_or_b64 s[22:23], s[22:23], vcc\n"
        "v_cmp_eq_u32 vcc, s3, v60\n"
        "s_nop 0\n"
        "s_or_b64 s[22:23], s[22:23], vcc\n"
        "v_cmp_eq_u32 vcc, s3, v61\n"
        "s_nop 0\n"
        "s_or_b64 s[22:23], s[22:23], vcc\n"
        "v_cmp_eq_u32 vcc, s3, v62\n"
        "s_nop 0\n"
        "s_or_b64 s[22:23], s[22:23], vcc\n"
        "v_cmp_eq_u32 vcc, s3, v63\n"
        "s_nop 0\n"
        "s_or_b64 s[22:23], s[22:23], vcc\n"
        "v_cmp_eq_u32 vcc, s3, v64\n"
        "s_nop 0\n"
        "s_or_b64 s[22:23], s[22:23], vcc\n"
        "v_cmp_eq_u32 vcc, s3, v65\n"
        "s_nop 0\n"
        "s_or_b64 s[22:23], s[22:23], vcc\n"
        "v_cmp_eq_u32 vcc, s3, v66\n"
        "s_nop 0\n"
        "s_or_b64 s[22:23], s[22:23], vcc\n"
        "v_cmp_eq_u32 vcc, s3, v67\n"
        "s_nop 0\n"
        "s_or_b64 s[22:23], s[22:23], vcc\n"
        "v_cmp_eq_u32 vcc, s3, v68\n"
        "s_nop 0\n"
        "s_or_b64 s[22:23], s[22:23], vcc\n"
        "v_cmp_eq_u32 vcc, s3, v69\n"
        "s_nop 0\n"
        "s_or_b64 s[22:23], s[22:23], vcc\n"
        "v_cmp_eq_u32 vcc, s3, v70\n"
        "s_nop 0\n"
        "s_or_b64 s[22:23], s[22:23], vcc\n"
        "v_cmp_eq_u32 vcc, s3, v71\n"
        "s_nop 0\n"
        "s_or_b64 s[22:23], s[22:23], vcc\n"
        "v_cmp_eq_u32 vcc, s3, v72\n"
        "s_nop 0\n"
        "s_or_b64 s[22:23], s[22:23], vcc\n"
        "v_cmp_eq_u32 vcc, s3, v73\n"
        "s_nop 0\n"
        "s_or_b64 s[22:23], s[22:23], vcc\n"
        "v_cmp_eq_u32 vcc, s3, v74\n"
        "s_nop 0\n"
        "s_or_b64 s[22:23], s[22:23], vcc\n"
        "v_cmp_eq_u32 vcc, s3, v75\n"
        "s_nop 0\n"
        "s_or_b64 s[22:23], s[22:23], vcc\n"
        "v_cmp_eq_u32 vcc, s3, v76\n"
        "s_nop 0\n"
        "s_or_b64 s[22:23], s[22:23], vcc\n"
        "v_cmp_eq_u32 vcc, s3, v77\n"
        "s_nop 0\n"
        "s_or_b64 s[22:23], s[22:23], vcc\n"
        "v_cmp_eq_u32 vcc, s3, v78\n"
        "s_nop 0\n"
        "s_or_b64 s[22:23], s[22:23], vcc\n"
        "v_cmp_eq_u32 vcc, s3, v79\n"
        "s_nop 0\n"
        "s_or_b64 s[22:23], s[22:23], vcc\n"
        "v_cmp_eq_u32 vcc, s3, v80\n"
        "s_nop 0\n"
        "s_or_b64 s[22:23], s[22:23], vcc\n"
        "v_cmp_eq_u32 vcc, s3, v81\n"
        "s_nop 0\n"
        "s_or_b64 s[22:23], s[22:23], vcc\n"
        "v_cmp_eq_u32 vcc, s3, v82\n"
        "s_nop 0\n"
        "s_or_b64 s[22:23], s[22:23], vcc\n"
        "v_cmp_eq_u32 vcc, s3, v83\n"
        "s_nop 0\n"
        "s_or_b64 s[22:23], s[22:23], vcc\n"
        "v_cmp_eq_u32 vcc, s3, v84\n"
        "s_nop 0\n"
        "s_or_b64 s[22:23], s[22:23], vcc\n"
        "v_cmp_eq_u32 vcc, s3, v85\n"
        "s_nop 0\n"
        "s_or_b64 s[22:23], s[22:23], vcc\n"
        "v_cmp_eq_u32 vcc, s3, v86\n"
        "s_nop 0\n"
        "s_or_b64 s[22:23], s[22:23], vcc\n"
        "v_cmp_eq_u32 vcc, s3, v87\n"
        "s_nop 0\n"
        "s_or_b64 s[22:23], s[22:23], vcc\n"
        "v_cmp_eq_u32 vcc, s3, v88\n"
        "s_nop 0\n"
        "s_or_b64 s[22:23], s[22:23], vcc\n"
        "v_cmp_eq_u32 vcc, s3, v89\n"
        "s_nop 0\n"
        "s_or_b64 s[22:23], s[22:23], vcc\n"
        "v_cmp_eq_u32 vcc, s3, v90\n"
        "s_nop 0\n"
        "s_or_b64 s[22:23], s[22:23], vcc\n"
        "v_cmp_eq_u32 vcc, s3, v91\n"
        "s_nop 0\n"
        "s_or_b64 s[22:23], s[22:23], vcc\n"
        "v_cmp_eq_u32 vcc, s3, v92\n"
        "s_nop 0\n"
        "s_or_b64 s[22:23], s[22:23], vcc\n"
        "v_cmp_eq_u32 vcc, s3, v93\n"
        "s_nop 0\n"
        "s_or_b64 s[22:23], s[22:23], vcc\n"
        "v_cmp_eq_u32 vcc, s3, v94\n"
        "s_nop 0\n"
        "s_or_b64 s[22:23], s[22:23], vcc\n"
        "v_cmp_eq_u32 vcc, s3, v95\n"
        "s_nop 0\n"
        "s_or_b64 s[22:23], s[22:23], vcc\n"
        "v_cmp_eq_u32 vcc, s3, v96\n"
        "s_nop 0\n"
        "s_or_b64 s[22:23], s[22:23], vcc\n"
        "v_cmp_eq_u32 vcc, s3, v97\n"
        "s_nop 0\n"
        "s_or_b64 s[22:23], s[22:23], vcc\n"
        "v_cmp_eq_u32 vcc, s3, v98\n"
        "s_nop 0\n"
        "s_or_b64 s[22:23], s[22:23], vcc\n"
        "v_cmp_eq_u32 vcc, s3, v99\n"
        "s_nop 0\n"
        "s_or_b64 s[22:23], s[22:23], vcc\n"
        "v_cmp_eq_u32 vcc, s3, v100\n"
        "s_nop 0\n"
        "s_or_b64 s[22:23], s[22:23], vcc\n"
        "v_cmp_eq_u32 vcc, s3, v101\n"
        "s_nop 0\n"
        "s_or_b64 s[22:23], s[22:23], vcc\n"
        "v_cmp_eq_u32 vcc, s3, v102\n"
        "s_nop 0\n"
        "s_or_b64 s[22:23], s[22:23], vcc\n"
        "v_cmp_eq_u32 vcc, s3, v103\n"
        "s_nop 0\n"
        "s_or_b64 s[22:23], s[22:23], vcc\n"
        "v_cmp_eq_u32 vcc, s3, v104\n"
        "s_nop 0\n"
        "s_or_b64 s[22:23], s[22:23], vcc\n"
        "v_cmp_eq_u32 vcc, s3, v105\n"
        "s_nop 0\n"
        "s_or_b64 s[22:23], s[22:23], vcc\n"
        "v_cmp_eq_u32 vcc, s3, v106\n"
        "s_nop 0\n"
        "s_or_b64 s[22:23], s[22:23], vcc\n"
        "v_cmp_eq_u32 vcc, s3, v107\n"
        "s_nop 0\n"
        "s_or_b64 s[22:23], s[22:23], vcc\n"
        "v_cmp_eq_u32 vcc, s3, v108\n"
        "s_nop 0\n"
        "s_or_b64 s[22:23], s[22:23], vcc\n"
        "v_cmp_eq_u32 vcc, s3, v109\n"
        "s_nop 0\n"
        "s_or_b64 s[22:23], s[22:23], vcc\n"
        "v_cmp_eq_u32 vcc, s3, v110\n"
        "s_nop 0\n"
        "s_or_b64 s[22:23], s[22:23], vcc\n"
        "v_cmp_eq_u32 vcc, s3, v111\n"
        "s_nop 0\n"
        "s_or_b64 s[22:23], s[22:23], vcc\n"
        "v_cmp_eq_u32 vcc, s3, v112\n"
        "s_nop 0\n"
        "s_or_b64 s[22:23], s[22:23], vcc\n"
        "v_cmp_eq_u32 vcc, s3, v113\n"
        "s_nop 0\n"
        "s_or_b64 s[22:23], s[22:23], vcc\n"
        "v_cmp_eq_u32 vcc, s3, v114\n"
        "s_nop 0\n"
        "s_or_b64 s[22:23], s[22:23], vcc\n"
        "v_cmp_eq_u32 vcc, s3, v115\n"
        "s_nop 0\n"
        "s_or_b64 s[22:23], s[22:23], vcc\n"
        "v_cmp_eq_u32 vcc, s3, v116\n"
        "s_nop 0\n"
        "s_or_b64 s[22:23], s[22:23], vcc\n"
        "v_cmp_eq_u32 vcc, s3, v117\n"
        "s_nop 0\n"
        "s_or_b64 s[22:23], s[22:23], vcc\n"
        "v_cmp_eq_u32 vcc, s3, v118\n"
        "s_nop 0\n"
        "s_or_b64 s[22:23], s[22:23], vcc\n"
        "v_cmp_eq_u32 vcc, s3, v119\n"
        "s_nop 0\n"
        "s_or_b64 s[22:23], s[22:23], vcc\n"
        "v_cmp_eq_u32 vcc, s3, v120\n"
        "s_nop 0\n"
        "s_or_b64 s[22:23], s[22:23], vcc\n"
        "v_cmp_eq_u32 vcc, s3, v121\n"
        "s_nop 0\n"
        "s_or_b64 s[22:23], s[22:23], vcc\n"
        "v_cmp_eq_u32 vcc, s3, v122\n"
        "s_nop 0\n"
        "s_or_b64 s[22:23], s[22:23], vcc\n"
        "v_cmp_eq_u32 vcc, s3, v123\n"
        "s_nop 0\n"
        "s_or_b64 s[22:23], s[22:23], vcc\n"
        "v_cmp_eq_u32 vcc, s3, v124\n"
        "s_nop 0\n"
        "s_or_b64 s[22:23], s[22:23], vcc\n"
        "v_cmp_eq_u32 vcc, s3, v125\n"
        "s_nop 0\n"
        "s_or_b64 s[22:23], s[22:23], vcc\n"
        "v_cmp_eq_u32 vcc, s3, v126\n"
        "s_nop 0\n"
        "s_or_b64 s[22:23], s[22:23], vcc\n"
        "v_cmp_eq_u32 vcc, s3, v127\n"
        "s_nop 0\n"
        "s_or_b64 s[22:23], s[22:23], vcc\n"
        "v_cmp_eq_u32 vcc, s3, v128\n"
        "s_nop 0\n"
        "s_or_b64 s[22:23], s[22:23], vcc\n"
        "v_cmp_eq_u32 vcc, s3, v129\n"
        "s_nop 0\n"
        "s_or_b64 s[22:23], s[22:23], vcc\n"
        "v_cmp_eq_u32 vcc, s3, v130\n"
        "s_nop 0\n"
        "s_or_b64 s[22:23], s[22:23], vcc\n"
        "v_cmp_eq_u32 vcc, s3, v131\n"
        "s_nop 0\n"
        "s_or_b64 s[22:23], s[22:23], vcc\n"
        "v_cmp_eq_u32 vcc, s3, v132\n"
        "s_nop 0\n"
        "s_or_b64 s[22:23], s[22:23], vcc\n"
        "v_cmp_eq_u32 vcc, s3, v133\n"
        "s_nop 0\n"
        "s_or_b64 s[22:23], s[22:23], vcc\n"
        "v_cmp_eq_u32 vcc, s3, v134\n"
        "s_nop 0\n"
        "s_or_b64 s[22:23], s[22:23], vcc\n"
        "v_cmp_eq_u32 vcc, s3, v135\n"
        "s_nop 0\n"
        "s_or_b64 s[22:23], s[22:23], vcc\n"
        "v_cmp_eq_u32 vcc, s3, v136\n"
        "s_nop 0\n"
        "s_or_b64 s[22:23], s[22:23], vcc\n"
        "v_cmp_eq_u32 vcc, s3, v137\n"
        "s_nop 0\n"
        "s_or_b64 s[22:23], s[22:23], vcc\n"
        "v_cmp_eq_u32 vcc, s3, v138\n"
        "s_nop 0\n"
        "s_or_b64 s[22:23], s[22:23], vcc\n"
        "v_cmp_eq_u32 vcc, s3, v139\n"
        "s_nop 0\n"
        "s_or_b64 s[22:23], s[22:23], vcc\n"
        "v_cmp_eq_u32 vcc, s3, v140\n"
        "s_nop 0\n"
        "s_or_b64 s[22:23], s[22:23], vcc\n"
        "v_cmp_eq_u32 vcc, s3, v141\n"
        "s_nop 0\n"
        "s_or_b64 s[22:23], s[22:23], vcc\n"
        "v_cmp_eq_u32 vcc, s3, v142\n"
        "s_nop 0\n"
        "s_or_b64 s[22:23], s[22:23], vcc\n"
        "v_cmp_eq_u32 vcc, s3, v143\n"
        "s_nop 0\n"
        "s_or_b64 s[22:23], s[22:23], vcc\n"
        "v_cmp_eq_u32 vcc, s3, v144\n"
        "s_nop 0\n"
        "s_or_b64 s[22:23], s[22:23], vcc\n"
        "v_cmp_eq_u32 vcc, s3, v145\n"
        "s_nop 0\n"
        "s_or_b64 s[22:23], s[22:23], vcc\n"
        "v_cmp_eq_u32 vcc, s3, v146\n"
        "s_nop 0\n"
        "s_or_b64 s[22:23], s[22:23], vcc\n"
        "v_cmp_eq_u32 vcc, s3, v147\n"
        "s_nop 0\n"
        "s_or_b64 s[22:23], s[22:23], vcc\n"
        "v_cmp_eq_u32 vcc, s3, v148\n"
        "s_nop 0\n"
        "s_or_b64 s[22:23], s[22:23], vcc\n"
        "v_cmp_eq_u32 vcc, s3, v149\n"
        "s_nop 0\n"
        "s_or_b64 s[22:23], s[22:23], vcc\n"
        "v_cmp_eq_u32 vcc, s3, v150\n"
        "s_nop 0\n"
        "s_or_b64 s[22:23], s[22:23], vcc\n"
        "v_cmp_eq_u32 vcc, s3, v151\n"
        "s_nop 0\n"
        "s_or_b64 s[22:23], s[22:23], vcc\n"
        "v_cmp_eq_u32 vcc, s3, v152\n"
        "s_nop 0\n"
        "s_or_b64 s[22:23], s[22:23], vcc\n"
        "v_cmp_eq_u32 vcc, s3, v153\n"
        "s_nop 0\n"
        "s_or_b64 s[22:23], s[22:23], vcc\n"
        "v_cmp_eq_u32 vcc, s3, v154\n"
        "s_nop 0\n"
        "s_or_b64 s[22:23], s[22:23], vcc\n"
        "v_cmp_eq_u32 vcc, s3, v155\n"
        "s_nop 0\n"
        "s_or_b64 s[22:23], s[22:23], vcc\n"
        "v_cmp_eq_u32 vcc, s3, v156\n"
        "s_nop 0\n"
        "s_or_b64 s[22:23], s[22:23], vcc\n"
        "v_cmp_eq_u32 vcc, s3, v157\n"
        "s_nop 0\n"
        "s_or_b64 s[22:23], s[22:23], vcc\n"
        "v_cmp_eq_u32 vcc, s3, v158\n"
        "s_nop 0\n"
        "s_or_b64 s[22:23], s[22:23], vcc\n"
        "v_cmp_eq_u32 vcc, s3, v159\n"
        "s_nop 0\n"
        "s_or_b64 s[22:23], s[22:23], vcc\n"
        "v_cmp_eq_u32 vcc, s3, v160\n"
        "s_nop 0\n"
        "s_or_b64 s[22:23], s[22:23], vcc\n"
        "v_cmp_eq_u32 vcc, s3, v161\n"
        "s_nop 0\n"
        "s_or_b64 s[22:23], s[22:23], vcc\n"
        "v_cmp_eq_u32 vcc, s3, v162\n"
        "s_nop 0\n"
        "s_or_b64 s[22:23], s[22:23], vcc\n"
        "v_cmp_eq_u32 vcc, s3, v163\n"
        "s_nop 0\n"
        "s_or_b64 s[22:23], s[22:23], vcc\n"
        "v_cmp_eq_u32 vcc, s3, v164\n"
        "s_nop 0\n"
        "s_or_b64 s[22:23], s[22:23], vcc\n"
        "v_cmp_eq_u32 vcc, s3, v165\n"
        "s_nop 0\n"
        "s_or_b64 s[22:23], s[22:23], vcc\n"
        "v_cmp_eq_u32 vcc, s3, v166\n"
        "s_nop 0\n"
        "s_or_b64 s[22:23], s[22:23], vcc\n"
        "v_cmp_eq_u32 vcc, s3, v167\n"
        "s_nop 0\n"
        "s_or_b64 s[22:23], s[22:23], vcc\n"
        "v_cmp_eq_u32 vcc, s3, v168\n"
        "s_nop 0\n"
        "s_or_b64 s[22:23], s[22:23], vcc\n"
        "v_cmp_eq_u32 vcc, s3, v169\n"
        "s_nop 0\n"
        "s_or_b64 s[22:23], s[22:23], vcc\n"
        "v_cmp_eq_u32 vcc, s3, v170\n"
        "s_nop 0\n"
        "s_or_b64 s[22:23], s[22:23], vcc\n"
        "v_cmp_eq_u32 vcc, s3, v171\n"
        "s_nop 0\n"
        "s_or_b64 s[22:23], s[22:23], vcc\n"
        "v_cmp_eq_u32 vcc, s3, v172\n"
        "s_nop 0\n"
        "s_or_b64 s[22:23], s[22:23], vcc\n"
        "v_cmp_eq_u32 vcc, s3, v173\n"
        "s_nop 0\n"
        "s_or_b64 s[22:23], s[22:23], vcc\n"
        "v_cmp_eq_u32 vcc, s3, v174\n"
        "s_nop 0\n"
        "s_or_b64 s[22:23], s[22:23], vcc\n"
        "v_cmp_eq_u32 vcc, s3, v175\n"
        "s_nop 0\n"
        "s_or_b64 s[22:23], s[22:23], vcc\n"
        "v_cmp_eq_u32 vcc, s3, v176\n"
        "s_nop 0\n"
        "s_or_b64 s[22:23], s[22:23], vcc\n"
        "v_cmp_eq_u32 vcc, s3, v177\n"
        "s_nop 0\n"
        "s_or_b64 s[22:23], s[22:23], vcc\n"
        "v_cmp_eq_u32 vcc, s3, v178\n"
        "s_nop 0\n"
        "s_or_b64 s[22:23], s[22:23], vcc\n"
        "v_cmp_eq_u32 vcc, s3, v179\n"
        "s_nop 0\n"
        "s_or_b64 s[22:23], s[22:23], vcc\n"
        "v_cmp_eq_u32 vcc, s3, v180\n"
        "s_nop 0\n"
        "s_or_b64 s[22:23], s[22:23], vcc\n"
        "v_cmp_eq_u32 vcc, s3, v181\n"
        "s_nop 0\n"
        "s_or_b64 s[22:23], s[22:23], vcc\n"
        "v_cmp_eq_u32 vcc, s3, v182\n"
        "s_nop 0\n"
        "s_or_b64 s[22:23], s[22:23], vcc\n"
        "v_cmp_eq_u32 vcc, s3, v183\n"
        "s_nop 0\n"
        "s_or_b64 s[22:23], s[22:23], vcc\n"
        "v_cmp_eq_u32 vcc, s3, v184\n"
        "s_nop 0\n"
        "s_or_b64 s[22:23], s[22:23], vcc\n"
        "v_cmp_eq_u32 vcc, s3, v185\n"
        "s_nop 0\n"
        "s_or_b64 s[22:23], s[22:23], vcc\n"
        "v_cmp_eq_u32 vcc, s3, v186\n"
        "s_nop 0\n"
        "s_or_b64 s[22:23], s[22:23], vcc\n"
        "v_cmp_eq_u32 vcc, s3, v187\n"
        "s_nop 0\n"
        "s_or_b64 s[22:23], s[22:23], vcc\n"
        "v_cmp_eq_u32 vcc, s3, v188\n"
        "s_nop 0\n"
        "s_or_b64 s[22:23], s[22:23], vcc\n"
        "v_cmp_eq_u32 vcc, s3, v189\n"
        "s_nop 0\n"
        "s_or_b64 s[22:23], s[22:23], vcc\n"
        "v_cmp_eq_u32 vcc, s3, v190\n"
        "s_nop 0\n"
        "s_or_b64 s[22:23], s[22:23], vcc\n"
        "v_cmp_eq_u32 vcc, s3, v191\n"
        "s_nop 0\n"
        "s_or_b64 s[22:23], s[22:23], vcc\n"
        "s_cmp_eq_u64 s[22:23], 0\n"
        "s_cbranch_scc1 .Lo320\n"
        "s_cmp_eq_u32 s15, 0\n"
        "s_cbranch_scc1 .Lo320\n"
        "s_branch .Lr272\n"
        ".Lqs321:\n"
        "s_nop 7\n"
        "s_mov_b32 s18, 0xffff\n"
        "s_mov_b32 s19, 0x0\n"
        "s_mov_b32 s20, 0xffff0000\n"
        "s_mov_b32 s21, 0x0\n"
        "s_mov_b32 s22, 0x0\n"
        "s_mov_b32 s23, 0xffff\n"
        "s_mov_b32 s24, 0x0\n"
        "s_mov_b32 s25, 0xffff0000\n"
        "v_max3_f32 v216, v86, v87, v88\n"
        "v_max_f32_e64 v216, v216, v89\n"
        "v_max3_f32 v200, v54, v55, v56\n"
        "v_max_f32 v200, v200, v57\n"
        "v_max_f32 v217, v200, v216\n"
        "v_cndmask_b32 v200, v200, v217, s[18:19]\n"
        "v_mov_b32 v217, v200\n"
        "s_nop 1\n"
        "v_permlane32_swap_b32 v200, v217\n"
        "v_max_f32 v200, v200, v217\n"
        "v_mov_b32 v217, v200\n"
        "s_nop 1\n"
        "v_permlane16_swap_b32 v200, v217\n"
        "v_max_f32 v200, v200, v217\n"
        "v_max3_f32 v201, v62, v63, v64\n"
        "v_max_f32 v201, v201, v65\n"
        "v_max_f32 v217, v201, v216\n"
        "v_cndmask_b32 v201, v201, v217, s[20:21]\n"
        "v_mov_b32 v217, v201\n"
        "s_nop 1\n"
        "v_permlane32_swap_b32 v201, v217\n"
        "v_max_f32 v201, v201, v217\n"
        "v_mov_b32 v217, v201\n"
        "s_nop 1\n"
        "v_permlane16_swap_b32 v201, v217\n"
        "v_max_f32 v201, v201, v217\n"
        "v_max3_f32 v202, v70, v71, v72\n"
        "v_max_f32 v202, v202, v73\n"
        "v_max_f32 v217, v202, v216\n"
        "v_cndmask_b32 v202, v202, v217, s[22:23]\n"
        "v_mov_b32 v217, v202\n"
        "s_nop 1\n"
        "v_permlane32_swap_b32 v202, v217\n"
        "v_max_f32 v202, v202, v217\n"
        "v_mov_b32 v217, v202\n"
        "s_nop 1\n"
        "v_permlane16_swap_b32 v202, v217\n"
        "v_max_f32 v202, v202, v217\n"
        "v_max3_f32 v203, v78, v79, v80\n"
        "v_max_f32 v203, v203, v81\n"
        "v_max_f32 v217, v203, v216\n"
        "v_cndmask_b32 v203, v203, v217, s[24:25]\n"
        "v_mov_b32 v217, v203\n"
        "s_nop 1\n"
        "v_permlane32_swap_b32 v203, v217\n"
        "v_max_f32 v203, v203, v217\n"
        "v_mov_b32 v217, v203\n"
        "s_nop 1\n"
        "v_permlane16_swap_b32 v203, v217\n"
        "v_max_f32 v203, v203, v217\n"
        "v_mov_b32_e64 v204, v200\n"
        "v_cndmask_b32 v204, v204, v201, s[20:21]\n"
        "v_cndmask_b32 v204, v204, v202, s[22:23]\n"
        "v_cndmask_b32 v204, v204, v203, s[24:25]\n"
        "v_min3_f32 v216, v86, v87, v88\n"
        "v_min_f32_e64 v216, v216, v89\n"
        "v_min3_f32 v208, v54, v55, v56\n"
        "v_min_f32 v208, v208, v57\n"
        "v_min_f32 v217, v208, v216\n"
        "v_cndmask_b32 v208, v208, v217, s[18:19]\n"
        "v_mov_b32 v217, v208\n"
        "s_nop 1\n"
        "v_permlane32_swap_b32 v208, v217\n"
        "v_min_f32 v208, v208, v217\n"
        "v_mov_b32 v217, v208\n"
        "s_nop 1\n"
        "v_permlane16_swap_b32 v208, v217\n"
        "v_min_f32 v208, v208, v217\n"
        "v_min3_f32 v209, v62, v63, v64\n"
        "v_min_f32 v209, v209, v65\n"
        "v_min_f32 v217, v209, v216\n"
        "v_cndmask_b32 v209, v209, v217, s[20:21]\n"
        "v_mov_b32 v217, v209\n"
        "s_nop 1\n"
        "v_permlane32_swap_b32 v209, v217\n"
        "v_min_f32 v209, v209, v217\n"
        "v_mov_b32 v217, v209\n"
        "s_nop 1\n"
        "v_permlane16_swap_b32 v209, v217\n"
        "v_min_f32 v209, v209, v217\n"
        "v_min3_f32 v210, v70, v71, v72\n"
        "v_min_f32 v210, v210, v73\n"
        "v_min_f32 v217, v210, v216\n"
        "v_cndmask_b32 v210, v210, v217, s[22:23]\n"
        "v_mov_b32 v217, v210\n"
        "s_nop 1\n"
        "v_permlane32_swap_b32 v210, v217\n"
        "v_min_f32 v210, v210, v217\n"
        "v_mov_b32 v217, v210\n"
        "s_nop 1\n"
        "v_permlane16_swap_b32 v210, v217\n"
        "v_min_f32 v210, v210, v217\n"
        "v_min3_f32 v211, v78, v79, v80\n"
        "v_min_f32 v211, v211, v81\n"
        "v_min_f32 v217, v211, v216\n"
        "v_cndmask_b32 v211, v211, v217, s[24:25]\n"
        "v_mov_b32 v217, v211\n"
        "s_nop 1\n"
        "v_permlane32_swap_b32 v211, v217\n"
        "v_min_f32 v211, v211, v217\n"
        "v_mov_b32 v217, v211\n"
        "s_nop 1\n"
        "v_permlane16_swap_b32 v211, v217\n"
        "v_min_f32 v211, v211, v217\n"
        "v_mov_b32_e64 v212, v208\n"
        "v_cndmask_b32 v212, v212, v209, s[20:21]\n"
        "v_cndmask_b32 v212, v212, v210, s[22:23]\n"
        "v_cndmask_b32 v212, v212, v211, s[24:25]\n"
        "v_sub_f32 v160, v208, v54\n"
        "v_sub_f32 v54, v54, v200\n"
        "v_sub_f32 v161, v208, v55\n"
        "v_sub_f32 v55, v55, v200\n"
        "v_sub_f32 v162, v208, v56\n"
        "v_sub_f32 v56, v56, v200\n"
        "v_sub_f32 v163, v208, v57\n"
        "v_sub_f32 v57, v57, v200\n"
        "v_sub_f32 v168, v209, v62\n"
        "v_sub_f32 v62, v62, v201\n"
        "v_sub_f32 v169, v209, v63\n"
        "v_sub_f32 v63, v63, v201\n"
        "v_sub_f32 v170, v209, v64\n"
        "v_sub_f32 v64, v64, v201\n"
        "v_sub_f32 v171, v209, v65\n"
        "v_sub_f32 v65, v65, v201\n"
        "v_sub_f32 v176, v210, v70\n"
        "v_sub_f32 v70, v70, v202\n"
        "v_sub_f32 v177, v210, v71\n"
        "v_sub_f32 v71, v71, v202\n"
        "v_sub_f32 v178, v210, v72\n"
        "v_sub_f32 v72, v72, v202\n"
        "v_sub_f32 v179, v210, v73\n"
        "v_sub_f32 v73, v73, v202\n"
        "v_sub_f32 v184, v211, v78\n"
        "v_sub_f32 v78, v78, v203\n"
        "v_sub_f32 v185, v211, v79\n"
        "v_sub_f32 v79, v79, v203\n"
        "v_sub_f32 v186, v211, v80\n"
        "v_sub_f32 v80, v80, v203\n"
        "v_sub_f32 v187, v211, v81\n"
        "v_sub_f32 v81, v81, v203\n"
        "v_sub_f32 v192, v212, v86\n"
        "v_sub_f32 v86, v86, v204\n"
        "v_sub_f32 v193, v212, v87\n"
        "v_sub_f32 v87, v87, v204\n"
        "v_sub_f32 v194, v212, v88\n"
        "v_sub_f32 v88, v88, v204\n"
        "v_sub_f32 v195, v212, v89\n"
        "v_sub_f32 v89, v89, v204\n"
        "v_max3_f32 v216, v90, v91, v92\n"
        "v_max_f32_e64 v216, v216, v93\n"
        "v_max3_f32 v200, v58, v59, v60\n"
        "v_max_f32 v200, v200, v61\n"
        "v_max_f32 v217, v200, v216\n"
        "v_cndmask_b32 v200, v200, v217, s[18:19]\n"
        "v_mov_b32 v217, v200\n"
        "s_nop 1\n"
        "v_permlane32_swap_b32 v200, v217\n"
        "v_max_f32 v200, v200, v217\n"
        "v_mov_b32 v217, v200\n"
        "s_nop 1\n"
        "v_permlane16_swap_b32 v200, v217\n"
        "v_max_f32 v200, v200, v217\n"
        "v_max3_f32 v201, v66, v67, v68\n"
        "v_max_f32 v201, v201, v69\n"
        "v_max_f32 v217, v201, v216\n"
        "v_cndmask_b32 v201, v201, v217, s[20:21]\n"
        "v_mov_b32 v217, v201\n"
        "s_nop 1\n"
        "v_permlane32_swap_b32 v201, v217\n"
        "v_max_f32 v201, v201, v217\n"
        "v_mov_b32 v217, v201\n"
        "s_nop 1\n"
        "v_permlane16_swap_b32 v201, v217\n"
        "v_max_f32 v201, v201, v217\n"
        "v_max3_f32 v202, v74, v75, v76\n"
        "v_max_f32 v202, v202, v77\n"
        "v_max_f32 v217, v202, v216\n"
        "v_cndmask_b32 v202, v202, v217, s[22:23]\n"
        "v_mov_b32 v217, v202\n"
        "s_nop 1\n"
        "v_permlane32_swap_b32 v202, v217\n"
        "v_max_f32 v202, v202, v217\n"
        "v_mov_b32 v217, v202\n"
        "s_nop 1\n"
        "v_permlane16_swap_b32 v202, v217\n"
        "v_max_f32 v202, v202, v217\n"
        "v_max3_f32 v203, v82, v83, v84\n"
        "v_max_f32 v203, v203, v85\n"
        "v_max_f32 v217, v203, v216\n"
        "v_cndmask_b32 v203, v203, v217, s[24:25]\n"
        "v_mov_b32 v217, v203\n"
        "s_nop 1\n"
        "v_permlane32_swap_b32 v203, v217\n"
        "v_max_f32 v203, v203, v217\n"
        "v_mov_b32 v217, v203\n"
        "s_nop 1\n"
        "v_permlane16_swap_b32 v203, v217\n"
        "v_max_f32 v203, v203, v217\n"
        "v_mov_b32_e64 v204, v200\n"
        "v_cndmask_b32 v204, v204, v201, s[20:21]\n"
        "v_cndmask_b32 v204, v204, v202, s[22:23]\n"
        "v_cndmask_b32 v204, v204, v203, s[24:25]\n"
        "v_min3_f32 v216, v90, v91, v92\n"
        "v_min_f32_e64 v216, v216, v93\n"
        "v_min3_f32 v208, v58, v59, v60\n"
        "v_min_f32 v208, v208, v61\n"
        "v_min_f32 v217, v208, v216\n"
        "v_cndmask_b32 v208, v208, v217, s[18:19]\n"
        "v_mov_b32 v217, v208\n"
        "s_nop 1\n"
        "v_permlane32_swap_b32 v208, v217\n"
        "v_min_f32 v208, v208, v217\n"
        "v_mov_b32 v217, v208\n"
        "s_nop 1\n"
        "v_permlane16_swap_b32 v208, v217\n"
        "v_min_f32 v208, v208, v217\n"
        "v_min3_f32 v209, v66, v67, v68\n"
        "v_min_f32 v209, v209, v69\n"
        "v_min_f32 v217, v209, v216\n"
        "v_cndmask_b32 v209, v209, v217, s[20:21]\n"
        "v_mov_b32 v217, v209\n"
        "s_nop 1\n"
        "v_permlane32_swap_b32 v209, v217\n"
        "v_min_f32 v209, v209, v217\n"
        "v_mov_b32 v217, v209\n"
        "s_nop 1\n"
        "v_permlane16_swap_b32 v209, v217\n"
        "v_min_f32 v209, v209, v217\n"
        "v_min3_f32 v210, v74, v75, v76\n"
        "v_min_f32 v210, v210, v77\n"
        "v_min_f32 v217, v210, v216\n"
        "v_cndmask_b32 v210, v210, v217, s[22:23]\n"
        "v_mov_b32 v217, v210\n"
        "s_nop 1\n"
        "v_permlane32_swap_b32 v210, v217\n"
        "v_min_f32 v210, v210, v217\n"
        "v_mov_b32 v217, v210\n"
        "s_nop 1\n"
        "v_permlane16_swap_b32 v210, v217\n"
        "v_min_f32 v210, v210, v217\n"
        "v_min3_f32 v211, v82, v83, v84\n"
        "v_min_f32 v211, v211, v85\n"
        "v_min_f32 v217, v211, v216\n"
        "v_cndmask_b32 v211, v211, v217, s[24:25]\n"
        "v_mov_b32 v217, v211\n"
        "s_nop 1\n"
        "v_permlane32_swap_b32 v211, v217\n"
        "v_min_f32 v211, v211, v217\n"
        "v_mov_b32 v217, v211\n"
        "s_nop 1\n"
        "v_permlane16_swap_b32 v211, v217\n"
        "v_min_f32 v211, v211, v217\n"
        "v_mov_b32_e64 v212, v208\n"
        "v_cndmask_b32 v212, v212, v209, s[20:21]\n"
        "v_cndmask_b32 v212, v212, v210, s[22:23]\n"
        "v_cndmask_b32 v212, v212, v211, s[24:25]\n"
        "v_sub_f32 v164, v208, v58\n"
        "v_sub_f32 v58, v58, v200\n"
        "v_sub_f32 v165, v208, v59\n"
        "v_sub_f32 v59, v59, v200\n"
        "v_sub_f32 v166, v208, v60\n"
        "v_sub_f32 v60, v60, v200\n"
        "v_sub_f32 v167, v208, v61\n"
        "v_sub_f32 v61, v61, v200\n"
        "v_sub_f32 v172, v209, v66\n"
        "v_sub_f32 v66, v66, v201\n"
        "v_sub_f32 v173, v209, v67\n"
        "v_sub_f32 v67, v67, v201\n"
        "v_sub_f32 v174, v209, v68\n"
        "v_sub_f32 v68, v68, v201\n"
        "v_sub_f32 v175, v209, v69\n"
        "v_sub_f32 v69, v69, v201\n"
        "v_sub_f32 v180, v210, v74\n"
        "v_sub_f32 v74, v74, v202\n"
        "v_sub_f32 v181, v210, v75\n"
        "v_sub_f32 v75, v75, v202\n"
        "v_sub_f32 v182, v210, v76\n"
        "v_sub_f32 v76, v76, v202\n"
        "v_sub_f32 v183, v210, v77\n"
        "v_sub_f32 v77, v77, v202\n"
        "v_sub_f32 v188, v211, v82\n"
        "v_sub_f32 v82, v82, v203\n"
        "v_sub_f32 v189, v211, v83\n"
        "v_sub_f32 v83, v83, v203\n"
        "v_sub_f32 v190, v211, v84\n"
        "v_sub_f32 v84, v84, v203\n"
        "v_sub_f32 v191, v211, v85\n"
        "v_sub_f32 v85, v85, v203\n"
        "v_sub_f32 v196, v212, v90\n"
        "v_sub_f32 v90, v90, v204\n"
        "v_sub_f32 v197, v212, v91\n"
        "v_sub_f32 v91, v91, v204\n"
        "v_sub_f32 v198, v212, v92\n"
        "v_sub_f32 v92, v92, v204\n"
        "v_sub_f32 v199, v212, v93\n"
        "v_sub_f32 v93, v93, v204\n"
        "v_exp_f32 v144, v54\n"
        "v_exp_f32 v145, v55\n"
        "v_exp_f32 v146, v56\n"
        "v_exp_f32 v147, v57\n"
        "v_cvt_pk_bf16_f32 v96, v144, v145\n"
        "v_cvt_pk_bf16_f32 v97, v146, v147\n"
        "v_exp_f32 v148, v160\n"
        "v_exp_f32 v149, v161\n"
        "v_exp_f32 v150, v162\n"
        "v_exp_f32 v151, v163\n"
        "v_cvt_pk_bf16_f32 v108, v148, v149\n"
        "v_cvt_pk_bf16_f32 v109, v150, v151\n"
        "v_exp_f32 v152, v58\n"
        "v_exp_f32 v153, v59\n"
        "v_exp_f32 v154, v60\n"
        "v_exp_f32 v155, v61\n"
        "v_cvt_pk_bf16_f32 v120, v152, v153\n"
        "v_cvt_pk_bf16_f32 v121, v154, v155\n"
        "v_exp_f32 v156, v164\n"
        "v_exp_f32 v157, v165\n"
        "v_exp_f32 v158, v166\n"
        "v_exp_f32 v159, v167\n"
        "v_cvt_pk_bf16_f32 v132, v156, v157\n"
        "v_cvt_pk_bf16_f32 v133, v158, v159\n"
        "v_exp_f32 v144, v62\n"
        "v_exp_f32 v145, v63\n"
        "v_exp_f32 v146, v64\n"
        "v_exp_f32 v147, v65\n"
        "v_cvt_pk_bf16_f32 v100, v144, v145\n"
        "v_cvt_pk_bf16_f32 v101, v146, v147\n"
        "v_exp_f32 v148, v168\n"
        "v_exp_f32 v149, v169\n"
        "v_exp_f32 v150, v170\n"
        "v_exp_f32 v151, v171\n"
        "v_cvt_pk_bf16_f32 v112, v148, v149\n"
        "v_cvt_pk_bf16_f32 v113, v150, v151\n"
        "v_exp_f32 v152, v66\n"
        "v_exp_f32 v153, v67\n"
        "v_exp_f32 v154, v68\n"
        "v_exp_f32 v155, v69\n"
        "v_cvt_pk_bf16_f32 v124, v152, v153\n"
        "v_cvt_pk_bf16_f32 v125, v154, v155\n"
        "v_exp_f32 v156, v172\n"
        "v_exp_f32 v157, v173\n"
        "v_exp_f32 v158, v174\n"
        "v_exp_f32 v159, v175\n"
        "v_cvt_pk_bf16_f32 v136, v156, v157\n"
        "v_cvt_pk_bf16_f32 v137, v158, v159\n"
        "v_exp_f32 v144, v70\n"
        "v_exp_f32 v145, v71\n"
        "v_exp_f32 v146, v72\n"
        "v_exp_f32 v147, v73\n"
        "v_cvt_pk_bf16_f32 v102, v144, v145\n"
        "v_cvt_pk_bf16_f32 v103, v146, v147\n"
        "v_exp_f32 v148, v176\n"
        "v_exp_f32 v149, v177\n"
        "v_exp_f32 v150, v178\n"
        "v_exp_f32 v151, v179\n"
        "v_cvt_pk_bf16_f32 v114, v148, v149\n"
        "v_cvt_pk_bf16_f32 v115, v150, v151\n"
        "v_exp_f32 v152, v74\n"
        "v_exp_f32 v153, v75\n"
        "v_exp_f32 v154, v76\n"
        "v_exp_f32 v155, v77\n"
        "v_cvt_pk_bf16_f32 v126, v152, v153\n"
        "v_cvt_pk_bf16_f32 v127, v154, v155\n"
        "v_exp_f32 v156, v180\n"
        "v_exp_f32 v157, v181\n"
        "v_exp_f32 v158, v182\n"
        "v_exp_f32 v159, v183\n"
        "v_cvt_pk_bf16_f32 v138, v156, v157\n"
        "v_cvt_pk_bf16_f32 v139, v158, v159\n"
        "v_exp_f32 v144, v78\n"
        "v_exp_f32 v145, v79\n"
        "v_exp_f32 v146, v80\n"
        "v_exp_f32 v147, v81\n"
        "v_cvt_pk_bf16_f32 v106, v144, v145\n"
        "v_cvt_pk_bf16_f32 v107, v146, v147\n"
        "v_exp_f32 v148, v184\n"
        "v_exp_f32 v149, v185\n"
        "v_exp_f32 v150, v186\n"
        "v_exp_f32 v151, v187\n"
        "v_cvt_pk_bf16_f32 v118, v148, v149\n"
        "v_cvt_pk_bf16_f32 v119, v150, v151\n"
        "v_exp_f32 v152, v82\n"
        "v_exp_f32 v153, v83\n"
        "v_exp_f32 v154, v84\n"
        "v_exp_f32 v155, v85\n"
        "v_cvt_pk_bf16_f32 v130, v152, v153\n"
        "v_cvt_pk_bf16_f32 v131, v154, v155\n"
        "v_exp_f32 v156, v188\n"
        "v_exp_f32 v157, v189\n"
        "v_exp_f32 v158, v190\n"
        "v_exp_f32 v159, v191\n"
        "v_cvt_pk_bf16_f32 v142, v156, v157\n"
        "v_cvt_pk_bf16_f32 v143, v158, v159\n"
        "v_exp_f32 v144, v86\n"
        "v_exp_f32 v145, v87\n"
        "v_exp_f32 v146, v88\n"
        "v_exp_f32 v147, v89\n"
        "v_cvt_pk_bf16_f32 v98, v144, v145\n"
        "v_cvt_pk_bf16_f32 v99, v146, v147\n"
        "v_cvt_pk_bf16_f32 v104, v144, v145\n"
        "v_cvt_pk_bf16_f32 v105, v146, v147\n"
        "v_exp_f32 v148, v192\n"
        "v_exp_f32 v149, v193\n"
        "v_exp_f32 v150, v194\n"
        "v_exp_f32 v151, v195\n"
        "v_cvt_pk_bf16_f32 v110, v148, v149\n"
        "v_cvt_pk_bf16_f32 v111, v150, v151\n"
        "v_cvt_pk_bf16_f32 v116, v148, v149\n"
        "v_cvt_pk_bf16_f32 v117, v150, v151\n"
        "v_exp_f32 v152, v90\n"
        "v_exp_f32 v153, v91\n"
        "v_exp_f32 v154, v92\n"
        "v_exp_f32 v155, v93\n"
        "v_cvt_pk_bf16_f32 v122, v152, v153\n"
        "v_cvt_pk_bf16_f32 v123, v154, v155\n"
        "v_cvt_pk_bf16_f32 v128, v152, v153\n"
        "v_cvt_pk_bf16_f32 v129, v154, v155\n"
        "v_exp_f32 v156, v196\n"
        "v_exp_f32 v157, v197\n"
        "v_exp_f32 v158, v198\n"
        "v_exp_f32 v159, v199\n"
        "v_cvt_pk_bf16_f32 v134, v156, v157\n"
        "v_cvt_pk_bf16_f32 v135, v158, v159\n"
        "v_cvt_pk_bf16_f32 v140, v156, v157\n"
        "v_cvt_pk_bf16_f32 v141, v158, v159\n"
        "s_nop 1\n"
        "s_branch .Lqj322\n"
        ".Lv336:\n"
        "v_cmp_eq_u32_e64 vcc, s21, v32\n"
        "v_cmp_eq_u32 s[22:23], s21, v36\n"
        "s_nop 0\n"
        "s_or_b64 vcc, vcc, s[22:23]\n"
        "s_cbranch_vccz .Lb337\n"
        "s_sub_i32 s20, s20, 1\n"
        "s_cmp_lt_i32 s20, 0\n"
        "s_cbranch_scc1 .Lb337\n"
        "s_sleep 2\n"
        "buffer_load_dwordx4 v[32:35], v5, s[4:7], s36 offen offset:0 sc1\n"
        "buffer_load_dwordx4 v[36:39], v5, s[4:7], s36 offen offset:2048 sc1\n"
        "s_waitcnt vmcnt(0)\n"
        "s_branch .Lv336\n"
        ".Lv338:\n"
        "v_cmp_eq_u32 vcc, s21, v40\n"
        "v_cmp_eq_u32 s[22:23], s21, v44\n"
        "s_nop 0\n"
        "s_or_b64 vcc, vcc, s[22:23]\n"
        "s_cbranch_vccz .Lb339\n"
        "s_sub_i32 s20, s20, 1\n"
        "s_cmp_lt_i32 s20, 0\n"
        "s_cbranch_scc1 .Lb339\n"
        "s_sleep 2\n"
        "buffer_load_dwordx4 v[40:43], v5, s[4:7], s37 offen offset:0 sc1\n"
        "buffer_load_dwordx4 v[44:47], v5, s[4:7], s37 offen offset:2048 sc1\n"
        "s_waitcnt vmcnt(0)\n"
        "s_branch .Lv338\n"
        ".Lv340:\n"
        "v_cmp_eq_u32 vcc, s21, v48\n"
        "v_cmp_eq_u32 s[22:23], s21, v52\n"
        "s_nop 0\n"
        "s_or_b64 vcc, vcc, s[22:23]\n"
        "s_cbranch_vccz .Lb341\n"
        "s_sub_i32 s20, s20, 1\n"
        "s_cmp_lt_i32 s20, 0\n"
        "s_cbranch_scc1 .Lb341\n"
        "s_sleep 2\n"
        "buffer_load_dwordx4 v[48:51], v5, s[4:7], s38 offen offset:0 sc1\n"
        "buffer_load_dwordx4 v[52:55], v5, s[4:7], s38 offen offset:2048 sc1\n"
        "s_waitcnt vmcnt(0)\n"
        "s_branch .Lv340\n"
        ".Lv342:\n"
        "v_cmp_eq_u32 vcc, s21, v56\n"
        "v_cmp_eq_u32 s[22:23], s21, v60\n"
        "s_nop 0\n"
        "s_or_b64 vcc, vcc, s[22:23]\n"
        "s_cbranch_vccz .Lb343\n"
        "s_sub_i32 s20, s20, 1\n"
        "s_cmp_lt_i32 s20, 0\n"
        "s_cbranch_scc1 .Lb343\n"
        "s_sleep 2\n"
        "buffer_load_dwordx4 v[56:59], v5, s[4:7], s39 offen offset:0 sc1\n"
        "buffer_load_dwordx4 v[60:63], v5, s[4:7], s39 offen offset:2048 sc1\n"
        "s_waitcnt vmcnt(0)\n"
        "s_branch .Lv342\n"
        ".Lv344:\n"
        "v_cmp_eq_u32 vcc, s21, v64\n"
        "v_cmp_eq_u32 s[22:23], s21, v68\n"
        "s_nop 0\n"
        "s_or_b64 vcc, vcc, s[22:23]\n"
        "s_cbranch_vccz .Lb345\n"
        "s_sub_i32 s20, s20, 1\n"
        "s_cmp_lt_i32 s20, 0\n"
        "s_cbranch_scc1 .Lb345\n"
        "s_sleep 2\n"
        "buffer_load_dwordx4 v[64:67], v5, s[4:7], s48 offen offset:0 sc1\n"
        "buffer_load_dwordx4 v[68:71], v5, s[4:7], s48 offen offset:2048 sc1\n"
        "s_waitcnt vmcnt(0)\n"
        "s_branch .Lv344\n"
        ".Lv346:\n"
        "v_cmp_eq_u32 vcc, s21, v72\n"
        "v_cmp_eq_u32 s[22:23], s21, v76\n"
        "s_nop 0\n"
        "s_or_b64 vcc, vcc, s[22:23]\n"
        "s_cbranch_vccz .Lb347\n"
        "s_sub_i32 s20, s20, 1\n"
        "s_cmp_lt_i32 s20, 0\n"
        "s_cbranch_scc1 .Lb347\n"
        "s_sleep 2\n"
        "buffer_load_dwordx4 v[72:75], v5, s[4:7], s49 offen offset:0 sc1\n"
        "buffer_load_dwordx4 v[76:79], v5, s[4:7], s49 offen offset:2048 sc1\n"
        "s_waitcnt vmcnt(0)\n"
        "s_branch .Lv346\n"
        ".Lv348:\n"
        "v_cmp_eq_u32 vcc, s21, v80\n"
        "v_cmp_eq_u32 s[22:23], s21, v84\n"
        "s_nop 0\n"
        "s_or_b64 vcc, vcc, s[22:23]\n"
        "s_cbranch_vccz .Lb349\n"
        "s_sub_i32 s20, s20, 1\n"
        "s_cmp_lt_i32 s20, 0\n"
        "s_cbranch_scc1 .Lb349\n"
        "s_sleep 2\n"
        "buffer_load_dwordx4 v[80:83], v5, s[4:7], s50 offen offset:0 sc1\n"
        "buffer_load_dwordx4 v[84:87], v5, s[4:7], s50 offen offset:2048 sc1\n"
        "s_waitcnt vmcnt(0)\n"
        "s_branch .Lv348\n"
        ".Lv350:\n"
        "v_cmp_eq_u32 vcc, s21, v88\n"
        "v_cmp_eq_u32 s[22:23], s21, v92\n"
        "s_nop 0\n"
        "s_or_b64 vcc, vcc, s[22:23]\n"
        "s_cbranch_vccz .Lb351\n"
        "s_sub_i32 s20, s20, 1\n"
        "s_cmp_lt_i32 s20, 0\n"
        "s_cbranch_scc1 .Lb351\n"
        "s_sleep 2\n"
        "buffer_load_dwordx4 v[88:91], v5, s[4:7], s51 offen offset:0 sc1\n"
        "buffer_load_dwordx4 v[92:95], v5, s[4:7], s51 offen offset:2048 sc1\n"
        "s_waitcnt vmcnt(0)\n"
        "s_branch .Lv350\n"
        ".Lc331:\n"
        "s_sub_u32 s15, s15, 1\n"
        "s_cmp_gt_u32 s15, 0x3ffd\n"
        "s_cbranch_scc1 .Lr330\n"
        "s_mov_b32 s3, 0x7f817f81\n"
        "s_mov_b64 s[22:23], 0\n"
        "v_cmp_eq_u32 vcc, s3, v32\n"
        "s_nop 0\n"
        "s_or_b64 s[22:23], s[22:23], vcc\n"
        "v_cmp_eq_u32 vcc, s3, v33\n"
        "s_nop 0\n"
        "s_or_b64 s[22:23], s[22:23], vcc\n"
        "v_cmp_eq_u32 vcc, s3, v34\n"
        "s_nop 0\n"
        "s_or_b64 s[22:23], s[22:23], vcc\n"
        "v_cmp_eq_u32 vcc, s3, v35\n"
        "s_nop 0\n"
        "s_or_b64 s[22:23], s[22:23], vcc\n"
        "v_cmp_eq_u32 vcc, s3, v36\n"
        "s_nop 0\n"
        "s_or_b64 s[22:23], s[22:23], vcc\n"
        "v_cmp_eq_u32 vcc, s3, v37\n"
        "s_nop 0\n"
        "s_or_b64 s[22:23], s[22:23], vcc\n"
        "v_cmp_eq_u32 vcc, s3, v38\n"
        "s_nop 0\n"
        "s_or_b64 s[22:23], s[22:23], vcc\n"
        "v_cmp_eq_u32 vcc, s3, v39\n"
        "s_nop 0\n"
        "s_or_b64 s[22:23], s[22:23], vcc\n"
        "v_cmp_eq_u32 vcc, s3, v40\n"
        "s_nop 0\n"
        "s_or_b64 s[22:23], s[22:23], vcc\n"
        "v_cmp_eq_u32 vcc, s3, v41\n"
        "s_nop 0\n"
        "s_or_b64 s[22:23], s[22:23], vcc\n"
        "v_cmp_eq_u32 vcc, s3, v42\n"
        "s_nop 0\n"
        "s_or_b64 s[22:23], s[22:23], vcc\n"
        "v_cmp_eq_u32 vcc, s3, v43\n"
        "s_nop 0\n"
        "s_or_b64 s[22:23], s[22:23], vcc\n"
        "v_cmp_eq_u32 vcc, s3, v44\n"
        "s_nop 0\n"
        "s_or_b64 s[22:23], s[22:23], vcc\n"
        "v_cmp_eq_u32 vcc, s3, v45\n"
        "s_nop 0\n"
        "s_or_b64 s[22:23], s[22:23], vcc\n"
        "v_cmp_eq_u32 vcc, s3, v46\n"
        "s_nop 0\n"
        "s_or_b64 s[22:23], s[22:23], vcc\n"
        "v_cmp_eq_u32 vcc, s3, v47\n"
        "s_nop 0\n"
        "s_or_b64 s[22:23], s[22:23], vcc\n"
        "v_cmp_eq_u32 vcc, s3, v48\n"
        "s_nop 0\n"
        "s_or_b64 s[22:23], s[22:23], vcc\n"
        "v_cmp_eq_u32 vcc, s3, v49\n"
        "s_nop 0\n"
        "s_or_b64 s[22:23], s[22:23], vcc\n"
        "v_cmp_eq_u32 vcc, s3, v50\n"
        "s_nop 0\n"
        "s_or_b64 s[22:23], s[22:23], vcc\n"
        "v_cmp_eq_u32 vcc, s3, v51\n"
        "s_nop 0\n"
        "s_or_b64 s[22:23], s[22:23], vcc\n"
        "v_cmp_eq_u32 vcc, s3, v52\n"
        "s_nop 0\n"
        "s_or_b64 s[22:23], s[22:23], vcc\n"
        "v_cmp_eq_u32 vcc, s3, v53\n"
        "s_nop 0\n"
        "s_or_b64 s[22:23], s[22:23], vcc\n"
        "v_cmp_eq_u32 vcc, s3, v54\n"
        "s_nop 0\n"
        "s_or_b64 s[22:23], s[22:23], vcc\n"
        "v_cmp_eq_u32 vcc, s3, v55\n"
        "s_nop 0\n"
        "s_or_b64 s[22:23], s[22:23], vcc\n"
        "v_cmp_eq_u32 vcc, s3, v56\n"
        "s_nop 0\n"
        "s_or_b64 s[22:23], s[22:23], vcc\n"
        "v_cmp_eq_u32 vcc, s3, v57\n"
        "s_nop 0\n"
        "s_or_b64 s[22:23], s[22:23], vcc\n"
        "v_cmp_eq_u32 vcc, s3, v58\n"
        "s_nop 0\n"
        "s_or_b64 s[22:23], s[22:23], vcc\n"
        "v_cmp_eq_u32 vcc, s3, v59\n"
        "s_nop 0\n"
        "s_or_b64 s[22:23], s[22:23], vcc\n"
        "v_cmp_eq_u32 vcc, s3, v60\n"
        "s_nop 0\n"
        "s_or_b64 s[22:23], s[22:23], vcc\n"
        "v_cmp_eq_u32 vcc, s3, v61\n"
        "s_nop 0\n"
        "s_or_b64 s[22:23], s[22:23], vcc\n"
        "v_cmp_eq_u32 vcc, s3, v62\n"
        "s_nop 0\n"
        "s_or_b64 s[22:23], s[22:23], vcc\n"
        "v_cmp_eq_u32 vcc, s3, v63\n"
        "s_nop 0\n"
        "s_or_b64 s[22:23], s[22:23], vcc\n"
        "v_cmp_eq_u32 vcc, s3, v64\n"
        "s_nop 0\n"
        "s_or_b64 s[22:23], s[22:23], vcc\n"
        "v_cmp_eq_u32 vcc, s3, v65\n"
        "s_nop 0\n"
        "s_or_b64 s[22:23], s[22:23], vcc\n"
        "v_cmp_eq_u32 vcc, s3, v66\n"
        "s_nop 0\n"
        "s_or_b64 s[22:23], s[22:23], vcc\n"
        "v_cmp_eq_u32 vcc, s3, v67\n"
        "s_nop 0\n"
        "s_or_b64 s[22:23], s[22:23], vcc\n"
        "v_cmp_eq_u32 vcc, s3, v68\n"
        "s_nop 0\n"
        "s_or_b64 s[22:23], s[22:23], vcc\n"
        "v_cmp_eq_u32 vcc, s3, v69\n"
        "s_nop 0\n"
        "s_or_b64 s[22:23], s[22:23], vcc\n"
        "v_cmp_eq_u32 vcc, s3, v70\n"
        "s_nop 0\n"
        "s_or_b64 s[22:23], s[22:23], vcc\n"
        "v_cmp_eq_u32 vcc, s3, v71\n"
        "s_nop 0\n"
        "s_or_b64 s[22:23], s[22:23], vcc\n"
        "v_cmp_eq_u32 vcc, s3, v72\n"
        "s_nop 0\n"
        "s_or_b64 s[22:23], s[22:23], vcc\n"
        "v_cmp_eq_u32 vcc, s3, v73\n"
        "s_nop 0\n"
        "s_or_b64 s[22:23], s[22:23], vcc\n"
        "v_cmp_eq_u32 vcc, s3, v74\n"
        "s_nop 0\n"
        "s_or_b64 s[22:23], s[22:23], vcc\n"
        "v_cmp_eq_u32 vcc, s3, v75\n"
        "s_nop 0\n"
        "s_or_b64 s[22:23], s[22:23], vcc\n"
        "v_cmp_eq_u32 vcc, s3, v76\n"
        "s_nop 0\n"
        "s_or_b64 s[22:23], s[22:23], vcc\n"
        "v_cmp_eq_u32 vcc, s3, v77\n"
        "s_nop 0\n"
        "s_or_b64 s[22:23], s[22:23], vcc\n"
        "v_cmp_eq_u32 vcc, s3, v78\n"
        "s_nop 0\n"
        "s_or_b64 s[22:23], s[22:23], vcc\n"
        "v_cmp_eq_u32 vcc, s3, v79\n"
        "s_nop 0\n"
        "s_or_b64 s[22:23], s[22:23], vcc\n"
        "v_cmp_eq_u32 vcc, s3, v80\n"
        "s_nop 0\n"
        "s_or_b64 s[22:23], s[22:23], vcc\n"
        "v_cmp_eq_u32 vcc, s3, v81\n"
        "s_nop 0\n"
        "s_or_b64 s[22:23], s[22:23], vcc\n"
        "v_cmp_eq_u32 vcc, s3, v82\n"
        "s_nop 0\n"
        "s_or_b64 s[22:23], s[22:23], vcc\n"
        "v_cmp_eq_u32 vcc, s3, v83\n"
        "s_nop 0\n"
        "s_or_b64 s[22:23], s[22:23], vcc\n"
        "v_cmp_eq_u32 vcc, s3, v84\n"
        "s_nop 0\n"
        "s_or_b64 s[22:23], s[22:23], vcc\n"
        "v_cmp_eq_u32 vcc, s3, v85\n"
        "s_nop 0\n"
        "s_or_b64 s[22:23], s[22:23], vcc\n"
        "v_cmp_eq_u32 vcc, s3, v86\n"
        "s_nop 0\n"
        "s_or_b64 s[22:23], s[22:23], vcc\n"
        "v_cmp_eq_u32 vcc, s3, v87\n"
        "s_nop 0\n"
        "s_or_b64 s[22:23], s[22:23], vcc\n"
        "v_cmp_eq_u32 vcc, s3, v88\n"
        "s_nop 0\n"
        "s_or_b64 s[22:23], s[22:23], vcc\n"
        "v_cmp_eq_u32 vcc, s3, v89\n"
        "s_nop 0\n"
        "s_or_b64 s[22:23], s[22:23], vcc\n"
        "v_cmp_eq_u32 vcc, s3, v90\n"
        "s_nop 0\n"
        "s_or_b64 s[22:23], s[22:23], vcc\n"
        "v_cmp_eq_u32 vcc, s3, v91\n"
        "s_nop 0\n"
        "s_or_b64 s[22:23], s[22:23], vcc\n"
        "v_cmp_eq_u32 vcc, s3, v92\n"
        "s_nop 0\n"
        "s_or_b64 s[22:23], s[22:23], vcc\n"
        "v_cmp_eq_u32 vcc, s3, v93\n"
        "s_nop 0\n"
        "s_or_b64 s[22:23], s[22:23], vcc\n"
        "v_cmp_eq_u32 vcc, s3, v94\n"
        "s_nop 0\n"
        "s_or_b64 s[22:23], s[22:23], vcc\n"
        "v_cmp_eq_u32 vcc, s3, v95\n"
        "s_nop 0\n"
        "s_or_b64 s[22:23], s[22:23], vcc\n"
        "s_cmp_eq_u64 s[22:23], 0\n"
        "s_cbranch_scc1 .Lo332\n"
        "s_cmp_eq_u32 s15, 0\n"
        "s_cbranch_scc1 .Lo332\n"
        "s_branch .Lr330\n"
        ".Lwr_5:\n"
        ".Lwr_6:\n"
        ".Lwr_7:\n"
        ".Lwr_8:\n"
        "s_setpc_b64 s[60:61]\n"
        ".Lend:\n"
        :
        : "{v0}"(threadIdx.x), "{s[0:1]}"(__builtin_amdgcn_kernarg_segment_ptr()), "{s2}"(blockIdx.x), "i"((__attribute__((address_space(3))) char*)smem)
        : "v1", "v2", "v3", "v4", "v5", "v6", "v7", "v8", "v9", "v10", "v11", "v12", "v13", "v14", "v15", "v16", "v17", "v18", "v19", "v20", "v21", "v22", "v23", "v24", "v25", "v26", "v27", "v28", "v29", "v30", "v31", "v32", "v33", "v34", "v35", "v36", "v37", "v38", "v39", "v40", "v41", "v42", "v43", "v44", "v45", "v46", "v47", "v48", "v49", "v50", "v51", "v52", "v53", "v54", "v55", "v56", "v57", "v58", "v59", "v60", "v61", "v62", "v63", "v64", "v65", "v66", "v67", "v68", "v69", "v70", "v71", "v72", "v73", "v74", "v75", "v76", "v77", "v78", "v79", "v80", "v81", "v82", "v83", "v84", "v85", "v86", "v87", "v88", "v89", "v90", "v91", "v92", "v93", "v94", "v95", "v96", "v97", "v98", "v99", "v100", "v101", "v102", "v103", "v104", "v105", "v106", "v107", "v108", "v109", "v110", "v111", "v112", "v113", "v114", "v115", "v116", "v117", "v118", "v119", "v120", "v121", "v122", "v123", "v124", "v125", "v126", "v127", "v128", "v129", "v130", "v131", "v132", "v133", "v134", "v135", "v136", "v137", "v138", "v139", "v140", "v141", "v142", "v143", "v144", "v145", "v146", "v147", "v148", "v149", "v150", "v151", "v152", "v153", "v154", "v155", "v156", "v157", "v158", "v159", "v160", "v161", "v162", "v163", "v164", "v165", "v166", "v167", "v168", "v169", "v170", "v171", "v172", "v173", "v174", "v175", "v176", "v177", "v178", "v179", "v180", "v181", "v182", "v183", "v184", "v185", "v186", "v187", "v188", "v189", "v190", "v191", "v192", "v193", "v194", "v195", "v196", "v197", "v198", "v199", "v200", "v201", "v202", "v203", "v204", "v205", "v206", "v207", "v208", "v209", "v210", "v211", "v212", "v213", "v214", "v215", "v216", "v217", "v218", "v219", "v220", "v221", "v222", "v223", "v224", "v225", "v226", "v227", "v228", "v229", "v230", "v231", "v232", "v233", "v234", "v235", "v236", "v237", "v238", "v239", "v240", "v241", "v242", "v243", "v244", "v245", "v246", "v247", "v248", "v249", "v250", "v251", "v252", "v253", "v254", "v255", "s3", "s4", "s5", "s6", "s7", "s8", "s9", "s10", "s11", "s12", "s13", "s14", "s15", "s16", "s17", "s18", "s19", "s20", "s21", "s22", "s23", "s24", "s25", "s26", "s27", "s28", "s29", "s30", "s31", "s33", "s34", "s35", "s36", "s37", "s38", "s39", "s40", "s41", "s42", "s43", "s44", "s45", "s46", "s47", "s48", "s49", "s50", "s51", "s52", "s53", "s54", "s55", "s56", "s57", "s58", "s59", "s60", "s61", "s62", "s63", "s64", "s65", "s66", "s67", "s68", "s69", "s70", "s71", "s72", "s73", "s74", "s75", "s76", "s77", "s78", "s79", "s80", "s81", "s82", "s83", "s84", "s85", "s86", "s87", "s88", "s89", "s90", "s91", "s92", "s93", "s94", "s95", "s96", "s97", "s98", "s99", "vcc", "scc", "memory");
}

extern "C" void kernel_launch(void* const* d_in, const int* in_sizes, int n_in, void* d_out, int out_size, void* d_ws, size_t ws_size,
                              hipStream_t stream) {
    fused_kernel<<<256, 512, 0, stream>>>(d_in[0], d_in[1], d_in[2], d_in[3], d_in[4], d_in[5], d_in[6], d_in[7], d_in[8], d_in[9], d_out, d_ws);
}
```
